```python
import torch

def _softmax(x, axis=-1):
    return torch.softmax(x, dim=axis)
def _gelu(x):
    return torch.nn.functional.gelu(x, approximate="tanh")
def _silu(x):
    return torch.nn.functional.silu(x)
def _sigmoid(x):
    return torch.sigmoid(x)
def _layernorm(x, axis=-1, eps=1e-5):
    m = x.mean(dim=axis, keepdim=True)
    v = x.var(dim=axis, keepdim=True, unbiased=False)
    return (x - m) / torch.sqrt(v + eps)
def _scatter_add(tgt, idx, src, axis=0):
    return tgt.clone().index_add_(axis, idx, src)
def _segment_sum(src, seg, n):
    out = torch.zeros((n,) + src.shape[1:], dtype=src.dtype)
    return out.index_add_(0, seg, src)
def _pad(x, pw):
    flat = tuple(v for pair in reversed(pw) for v in pair)
    return torch.nn.functional.pad(x, flat)

kernel_name = 'sequential_preLN__sliding_block_cached__mixer_channel_mlp__decode'

def setup_inputs():

    _w = (lambda a: (lambda t: t.to(torch.bfloat16) if t.is_floating_point() else t)(torch.as_tensor(a)))
    def _A_setup():
        B, Sq, D = 1, 1, 576
        Hq, Hkv, Sk, Dh = 32, 16, 65536, 128
        assert Hq % Hkv == 0, (Hq, Hkv)
        return {
            "x": _w((lambda *s: torch.randn(*s, dtype=torch.bfloat16) * 0.05)(B, Sq, D)),
            "k_cache": _w((lambda *s: torch.randn(*s, dtype=torch.bfloat16) * 3.2)(B, Hkv, Sk, Dh)),
            "v_cache": _w((lambda *s: torch.randn(*s, dtype=torch.bfloat16) * 0.4)(B, Hkv, Sk, Dh)),
            "wq": _w((lambda *s: torch.randn(*s, dtype=torch.bfloat16) * 3.2)(D, Hq * Dh) * (D ** -0.5)),
            "wo": _w((lambda *s: torch.randn(*s, dtype=torch.bfloat16) * 0.4)(Hq * Dh, D) * ((Hq * Dh) ** -0.5)),
        }
    _A_in = _A_setup()

    def _B_setup():
        B, S, D, F = 1, 1, 576, 4608
        return {
            "x": _w((lambda *s: torch.randn(*s, dtype=torch.bfloat16) * 0.05)(B, S, D)),
            "w1": _w((lambda *s: torch.randn(*s, dtype=torch.bfloat16) * 3.2)(D, F) * (D ** -0.5)),
            "b1": _w((lambda *s: torch.randn(*s, dtype=torch.bfloat16) * 3.2)(F)),
            "w2": _w((lambda *s: torch.randn(*s, dtype=torch.bfloat16) * 0.1)(F, D) * (F ** -0.5)),
            "b2": _w((lambda *s: torch.randn(*s, dtype=torch.bfloat16) * 0.1)(D)),
        }
    _B_in = _B_setup()

    return {
        "A_x": _A_in["x"],
        "A_k_cache": _A_in["k_cache"],
        "A_v_cache": _A_in["v_cache"],
        "A_wq": _A_in["wq"],
        "A_wo": _A_in["wo"],
        "B_x": _B_in["x"],
        "B_w1": _B_in["w1"],
        "B_b1": _B_in["b1"],
        "B_w2": _B_in["w2"],
        "B_b2": _B_in["b2"],
    }

def reference(A_x, A_k_cache, A_v_cache, A_wq, A_wo, B_x, B_w1, B_b1, B_w2, B_b2):
    def _A_ref(x, k_cache, v_cache, wq, wo):
        B, Sq, D = x.shape
        Hq, Dh = 32, 128
        Hkv, Sk = k_cache.shape[1], k_cache.shape[2]
        W = min(4096, max(256, Sk // 4))
        rep = Hq // Hkv
        q = (lambda x, *d: x.permute(*d))((x @ wq).reshape(B, Sq, Hq, Dh), 0, 2, 1, 3)
        k = (lambda x, n, axis: torch.repeat_interleave(x, n, dim=axis))(k_cache[:, :, Sk - W:, :], rep, axis=1)
        v = (lambda x, n, axis: torch.repeat_interleave(x, n, dim=axis))(v_cache[:, :, Sk - W:, :], rep, axis=1)
        scores = torch.einsum("bhqd,bhkd->bhqk", q, k) * (Dh ** -0.5)
        p = _softmax(scores, axis=-1)
        o = torch.einsum("bhqk,bhkd->bhqd", p, v)
        return (lambda x, *d: x.permute(*d))(o, 0, 2, 1, 3).reshape(B, Sq, Hq * Dh) @ wo

    def _B_ref(x, w1, b1, w2, b2):
        y = _layernorm(x, axis=-1)
        h = _gelu(torch.einsum("bsd,df->bsf", y, w1) + b1)
        return torch.einsum("bsf,fd->bsd", h, w2) + b2

    x = A_x
    h = x + _A_ref(_layernorm(x, axis=-1), A_k_cache, A_v_cache, A_wq, A_wo)
    h2 = h + _B_ref(_layernorm(h, axis=-1), B_w1, B_b1, B_w2, B_b2)
    return h2
```

```cpp
#include <hip/hip_runtime.h>
#include <hip/hip_bf16.h>
#define ROUND 0
typedef unsigned short u16;
__device__ __forceinline__ float bf2f(u16 b){ return __uint_as_float(((unsigned)b)<<16); }
__device__ __forceinline__ u16 f2bf(float f){ unsigned u=__float_as_uint(f); u += 0x7fffu + ((u>>16)&1u); return (u16)(u>>16); }
__device__ __forceinline__ float rb(float f){ return ROUND ? bf2f(f2bf(f)) : f; }
constexpr int D=576, HQ=32, HKV=16, SK=65536, DH=128, W=4096, F=4608;

__device__ void layernorm_block(const float* in, float* out, float* red){
  int t=threadIdx.x; float s=0; for(int i=t;i<D;i+=256) s+=in[i];
  red[t]=s; __syncthreads(); for(int o=128;o;o>>=1){ if(t<o) red[t]+=red[t+o]; __syncthreads(); }
  float m=red[0]/D; __syncthreads();
  float v=0; for(int i=t;i<D;i+=256){ float d=in[i]-m; v+=d*d; }
  red[t]=v; __syncthreads(); for(int o=128;o;o>>=1){ if(t<o) red[t]+=red[t+o]; __syncthreads(); }
  float var=red[0]/D; __syncthreads();
  float mb=rb(m), vb=rb(var); float den=rb(sqrtf(rb(vb+1e-5f)));
  for(int i=t;i<D;i+=256) out[i]=rb(rb(in[i]-mb)/den);
  __syncthreads();
}
__global__ void k_q(const u16* x,const u16* wq,float* q){
  __shared__ float a[D],y[D],red[256]; int t=threadIdx.x;
  for(int i=t;i<D;i+=256) a[i]=bf2f(x[i]); __syncthreads();
  layernorm_block(a,y,red);
  int c=blockIdx.x*256+t; float s=0; for(int k=0;k<D;k++) s+=y[k]*bf2f(wq[(size_t)k*4096+c]);
  q[c]=rb(s);
}
__global__ void k_attn(const float* q,const u16* kc,const u16* vc,float* o){
  __shared__ float sc[W],red[256],qs[DH]; int h=blockIdx.x,t=threadIdx.x; int kvh=h/2;
  if(t<DH) qs[t]=q[h*DH+t]; __syncthreads();
  const u16* K=kc+((size_t)kvh*SK+(SK-W))*DH; const u16* V=vc+((size_t)kvh*SK+(SK-W))*DH;
  float mx=-INFINITY;
  for(int j=t;j<W;j+=256){ float s=0; for(int d=0;d<DH;d++) s+=qs[d]*bf2f(K[(size_t)j*DH+d]); s=rb(rb(s)*0.08838834764831845f); sc[j]=s; mx=fmaxf(mx,s);}
  red[t]=mx; __syncthreads(); for(int o_=128;o_;o_>>=1){ if(t<o_) red[t]=fmaxf(red[t],red[t+o_]); __syncthreads(); }
  mx=red[0]; __syncthreads(); float l=0; for(int j=t;j<W;j+=256){ float e=expf(sc[j]-mx); sc[j]=e; l+=e; }
  red[t]=l; __syncthreads(); for(int o_=128;o_;o_>>=1){ if(t<o_) red[t]+=red[t+o_]; __syncthreads(); }
  l=red[0]; __syncthreads(); for(int j=t;j<W;j+=256) sc[j]=rb(sc[j]/l); __syncthreads();
  int d=t&127, half=t>>7; float acc=0; for(int j=half;j<W;j+=2) acc+=sc[j]*bf2f(V[(size_t)j*DH+d]);
  red[t]=acc; __syncthreads(); if(t<128) o[h*DH+t]=rb(red[t]+red[t+128]);
}
__global__ void k_wo(const float* o,const u16* wo,const u16* x,float* h){
  int c=blockIdx.x*64+threadIdx.x; float s=0; for(int k=0;k<4096;k++) s+=o[k]*bf2f(wo[(size_t)k*D+c]);
  h[c]=rb(bf2f(x[c])+rb(s));
}
__global__ void k_w1(const float* h,const u16* w1,const u16* b1,float* h1){
  __shared__ float a[D],y[D],red[256]; int t=threadIdx.x;
  for(int i=t;i<D;i+=256) a[i]=h[i]; __syncthreads();
  layernorm_block(a,y,red); layernorm_block(y,a,red);
  int c=blockIdx.x*256+t; float s=0; for(int k=0;k<D;k++) s+=a[k]*bf2f(w1[(size_t)k*F+c]);
  float z=rb(rb(s)+bf2f(b1[c]));
  float g=0.5f*z*(1.f+tanhf(0.7978845608028654f*(z+0.044715f*z*z*z)));
  h1[c]=rb(g);
}
__global__ void k_w2(const float* h1,const u16* w2,const u16* b2,const float* h,u16* out){
  int c=blockIdx.x*64+threadIdx.x; float s=0; for(int k=0;k<F;k++) s+=h1[k]*bf2f(w2[(size_t)k*D+c]);
  out[c]=f2bf(h[c]+rb(rb(s)+bf2f(b2[c])));
}
extern "C" void kernel_launch(void* const* d_in, const int* in_sizes, int n_in, void* d_out, int out_size, void* d_ws, size_t ws_size, hipStream_t stream){
  const u16* x=(const u16*)d_in[0]; const u16* kc=(const u16*)d_in[1]; const u16* vc=(const u16*)d_in[2];
  const u16* wq=(const u16*)d_in[3]; const u16* wo=(const u16*)d_in[4]; const u16* w1=(const u16*)d_in[6]; const u16* b1=(const u16*)d_in[7];
  const u16* w2=(const u16*)d_in[8]; const u16* b2=(const u16*)d_in[9];
  float* ws=(float*)d_ws; float* q=ws; float* o=ws+4096; float* h=ws+8192; float* h1=ws+8192+1024;
  k_q<<<16,256,0,stream>>>(x,wq,q);
  k_attn<<<32,256,0,stream>>>(q,kc,vc,o);
  k_wo<<<9,64,0,stream>>>(o,wo,x,h);
  k_w1<<<18,256,0,stream>>>(h,w1,b1,h1);
  k_w2<<<9,64,0,stream>>>(h1,w2,b2,h,(u16*)d_out);
}
```

```cpp
#include <hip/hip_runtime.h>
#include <hip/hip_bf16.h>

typedef unsigned short u16;
typedef unsigned long long u64;
typedef unsigned v4u __attribute__((ext_vector_type(4)));
typedef unsigned v3u __attribute__((ext_vector_type(3)));
typedef unsigned v2u __attribute__((ext_vector_type(2)));
typedef float v4f __attribute__((ext_vector_type(4)));
typedef float v2f __attribute__((ext_vector_type(2)));
typedef __bf16 bf16x2 __attribute__((ext_vector_type(2)));
#define GAS __attribute__((address_space(1)))
#define LAS __attribute__((address_space(3)))
#define RLX_AGENT __ATOMIC_RELAXED, __HIP_MEMORY_SCOPE_AGENT
#define STAMP 0

constexpr int D = 576, SK = 65536, DH = 128, WIN = 4096, FF = 4608, QW = 4096;
constexpr int NTHR = 576, NBLK = 256;
constexpr unsigned EP_OFF = 0, FL_OFF = 4096, QP_OFF = 8192, OP_OFF = QP_OFF + 16 * 4 * 256 * 8, ST_OFF = OP_OFF + 16 * 16 * 256 * 4,
                   HP_OFF = ST_OFF + 16 * 2 * 16 * 8, MP_OFF = HP_OFF + 16 * 576 * 4, WS_END = MP_OFF + 144 * 576 * 4, DBG_OFF = 1u << 20;
constexpr unsigned SENT = 0xFFFFFFFFu;
constexpr unsigned SPIN_CAP = 1u << 20;
constexpr int AUX_SC1 = 16, AUX_NT = 2;

struct Params { const u16 *x, *k, *v, *wq, *wo, *w1, *b1, *w2, *b2; u16* out; char* ws; };

__device__ __forceinline__ float asf(unsigned u) { return __builtin_bit_cast(float, u); }
__device__ __forceinline__ unsigned asu(float f) { return __builtin_bit_cast(unsigned, f); }
__device__ __forceinline__ float lo16(unsigned u) { return asf(u << 16); }
__device__ __forceinline__ float hi16(unsigned u) { return asf(u & 0xffff0000u); }
__device__ __forceinline__ unsigned pk(float lo, float hi) {
  bf16x2 r; r.x = (__bf16)lo; r.y = (__bf16)hi; return __builtin_bit_cast(unsigned, r);
}
__device__ __forceinline__ float rb(float f) { return lo16(pk(f, f) & 0xffffu); }
__device__ __forceinline__ float dot2(unsigned a, unsigned b, float c) {
  return __builtin_amdgcn_fdot2_f32_bf16(__builtin_bit_cast(bf16x2, a), __builtin_bit_cast(bf16x2, b), c, false);
}
template <int CTRL> __device__ __forceinline__ float dpp(float x) { return asf(__builtin_amdgcn_mov_dpp(asu(x), CTRL, 0xf, 0xf, true)); }
template <int CTRL> __device__ __forceinline__ unsigned dppu(unsigned x) { return __builtin_amdgcn_mov_dpp(x, CTRL, 0xf, 0xf, true); }
constexpr int XOR1 = 0xB1, XOR2 = 0x4E, XOR7 = 0x141, XOR8 = 0x128, ROR4 = 0x124, NEWB = 0x150;
__device__ __forceinline__ float x16_sum(float x) { auto s = __builtin_amdgcn_permlane16_swap(asu(x), asu(x), false, false); return asf(s[0]) + asf(s[1]); }
__device__ __forceinline__ float x32_sum(float x) { auto s = __builtin_amdgcn_permlane32_swap(asu(x), asu(x), false, false); return asf(s[0]) + asf(s[1]); }
__device__ __forceinline__ float x16_max(float x) { auto s = __builtin_amdgcn_permlane16_swap(asu(x), asu(x), false, false); return fmaxf(asf(s[0]), asf(s[1])); }
__device__ __forceinline__ float x32_max(float x) { auto s = __builtin_amdgcn_permlane32_swap(asu(x), asu(x), false, false); return fmaxf(asf(s[0]), asf(s[1])); }
__device__ __forceinline__ float t32(float a, float b) { auto s = __builtin_amdgcn_permlane32_swap(asu(a), asu(b), false, false); return asf(s[0]) + asf(s[1]); }
__device__ __forceinline__ float t16(float a, float b) { auto s = __builtin_amdgcn_permlane16_swap(asu(a), asu(b), false, false); return asf(s[0]) + asf(s[1]); }
template <int CTRL> __device__ __forceinline__ float tdpp(float a, float b, bool hi) { const float s = hi ? a : b, k = hi ? b : a; return k + dpp<CTRL>(s); }
__device__ __forceinline__ float row_sum(float x) { x += dpp<XOR1>(x); x += dpp<XOR2>(x); x += dpp<XOR7>(x); x += dpp<XOR8>(x); return x; }
__device__ __forceinline__ float wave_sum(float x) { return x32_sum(x16_sum(row_sum(x))); }
__device__ __forceinline__ v4u ldnt4(const void* p) { return __builtin_nontemporal_load((const GAS v4u*)p); }
__device__ __forceinline__ v3u ldnt3(const void* p) { return __builtin_nontemporal_load((const GAS v3u*)p); }
__device__ __forceinline__ unsigned clean(float f) { const unsigned u = asu(f); return u < SENT ? u : SENT - 1u; }
#define DRAIN() asm volatile("s_waitcnt vmcnt(0)" ::: "memory")
#define CBAR() asm volatile("" ::: "memory")
#define LGKM0() asm volatile("s_waitcnt lgkmcnt(0)" ::: "memory")
__device__ __forceinline__ void bar() { LGKM0(); __builtin_amdgcn_s_barrier(); CBAR(); }
#define ST(i) do {} while (0)

__device__ __forceinline__ void ln_stats9(const float (&h)[9], float c, float& mb, float& den) {
  float s = 0.f, q = 0.f;
#pragma unroll
  for (int i = 0; i < 9; ++i) { const float d = h[i] - c; s += d; q = fmaf(d, d, q); }
  s = wave_sum(s); q = wave_sum(q);
  const float md = s * (1.f / D);
  const float var = fmaxf(q * (1.f / D) - md * md, 0.f);
  mb = rb(c + md);
  den = rb(__builtin_sqrtf(rb(rb(var) + 1e-5f)));
}
__device__ __forceinline__ float ln_apply(float h, float mb, float den) { return rb(rb(h - mb) / den); }

constexpr int L_FLAG = 0, L_Q = 16, L_Y = L_Q + 128, L_PO = L_Y + 576, L_PS = L_PO + 8 * 256, L_H = L_PS + 32, L_Z = L_H + 576, L_R = L_Z + 9 * 32, L_END = L_R + 8 * 96, L_PAD = 21 * 1024;

__global__ void __launch_bounds__(NTHR, 3) fused(Params p) {
  __shared__ __attribute__((aligned(16))) float lds[L_PAD];
  const int b = blockIdx.x, tid = threadIdx.x, wave = __builtin_amdgcn_readfirstlane(tid >> 6), lane = tid & 63;
  const int g = (b & 7) * 2 + ((b >> 3) & 1), j = b >> 4;
  const bool is_wo = j < 6, is_mlp = j >= 6 && j < 15;
  const int u = g * 9 + (j - 6);
  const int red = j == 15 ? g : (j == 0 ? 16 + g : (j == 1 && g < 4 ? 32 + g : -1));
  const __amdgpu_buffer_rsrc_t ws = __builtin_amdgcn_make_buffer_rsrc(p.ws, 0, WS_END, 0x00020000);
  volatile LAS unsigned* lflag = (volatile LAS unsigned*)lds;
  ST(0);
  if (tid == 0) lflag[L_FLAG] = 0u;
  bar();
  const int r = lane >> 4, c = lane & 15;
  u64 tag = 0;
  if (wave == 0) {
    const v4u sent = {SENT, SENT, SENT, SENT};
    __builtin_amdgcn_raw_buffer_store_b128(sent, ws, OP_OFF + (g * 16 + j) * 1024 + lane * 16, 0, AUX_SC1);
    if (lane < 2) __builtin_amdgcn_raw_buffer_store_b64(v2u{SENT, SENT}, ws, ST_OFF + ((g * 2 + lane) * 16 + j) * 8, 0, AUX_SC1);
    if (is_wo) { if (lane < 24) __builtin_amdgcn_raw_buffer_store_b128(sent, ws, HP_OFF + (g * 576 + j * 96) * 4 + lane * 16, 0, AUX_SC1); }
    if (is_mlp) {
      __builtin_amdgcn_raw_buffer_store_b128(sent, ws, MP_OFF + u * 2304 + lane * 16, 0, AUX_SC1);
      __builtin_amdgcn_raw_buffer_store_b128(sent, ws, MP_OFF + u * 2304 + 1024 + lane * 16, 0, AUX_SC1);
      if (lane < 16) __builtin_amdgcn_raw_buffer_store_b128(sent, ws, MP_OFF + u * 2304 + 2048 + lane * 16, 0, AUX_SC1);
    }
    const v2u ep = __builtin_amdgcn_raw_buffer_load_b64(ws, EP_OFF, 0, AUX_SC1);
    float xv[9];
#pragma unroll
    for (int i = 0; i < 9; ++i) xv[i] = lo16(((const GAS u16*)p.x)[lane + 64 * i]);
    const int qt = j >> 2, qc = j & 3, rr = lane >> 3, cc = lane & 7;
    const u16* wqb = p.wq + (size_t)(144 * qt + 18 * rr) * QW + 256 * g + 64 * qc + 8 * cc;
    v4u w[18];
#pragma unroll
    for (int i = 0; i < 18; ++i) w[i] = ldnt4(wqb + (size_t)i * QW);
    float mb, den;
    ln_stats9(xv, asf(__builtin_amdgcn_readfirstlane(asu(xv[0]))), mb, den);
#pragma unroll
    for (int i = 0; i < 9; ++i) lds[L_Y + lane + 64 * i] = ln_apply(xv[i], mb, den);
    LGKM0();
    float acc[8];
#pragma unroll
    for (int k = 0; k < 8; ++k) acc[k] = 0.f;
#pragma unroll
    for (int i = 0; i < 18; i += 2) {
      const v2f y = *(const LAS v2f*)&((LAS float*)lds)[L_Y + 144 * qt + 18 * rr + i];
      const unsigned yp = (asu(y.x) >> 16) | (asu(y.y) & 0xffff0000u);
#pragma unroll
      for (int k = 0; k < 4; ++k) {
        acc[2 * k] = dot2(__builtin_amdgcn_perm(w[i + 1][k], w[i][k], 0x05040100), yp, acc[2 * k]);
        acc[2 * k + 1] = dot2(__builtin_amdgcn_perm(w[i + 1][k], w[i][k], 0x07060302), yp, acc[2 * k + 1]);
      }
    }
    float a4[4], a2[2];
#pragma unroll
    for (int k = 0; k < 4; ++k) a4[k] = t32(acc[k], acc[k + 4]);
#pragma unroll
    for (int k = 0; k < 2; ++k) a2[k] = t16(a4[k], a4[k + 2]);
    const float qp = tdpp<XOR8>(a2[0], a2[1], lane & 8);
    const int col = 8 * cc + ((lane >> 5) << 2) + (((lane >> 4) & 1) << 1) + ((lane >> 3) & 1);
    tag = (((u64)ep.y << 32) | ep.x) + 1ull;
    tag = ((u64)(unsigned)__builtin_amdgcn_readfirstlane((unsigned)(tag >> 32)) << 32) | (unsigned)__builtin_amdgcn_readfirstlane((unsigned)tag);
    DRAIN();
    const unsigned tlo = (unsigned)tag & 0xffu, thi = (unsigned)(tag >> 8);
    __builtin_amdgcn_raw_buffer_store_b64(v2u{((asu(qp) + 0x80u) & 0xffffff00u) | tlo, thi}, ws, QP_OFF + ((g * 4 + qt) * 256 + 64 * qc + col) * 8, 0, AUX_SC1);
    if (lane == 0) __builtin_amdgcn_raw_buffer_store_b64(v2u{(unsigned)tag, (unsigned)(tag >> 32)}, ws, FL_OFF + b * 8, 0, AUX_SC1);
    ST(1);
    for (unsigned spin = 0; spin < SPIN_CAP; ++spin) {
      v4u gq[8]; bool ok = true;
      CBAR();
#pragma unroll
      for (int t = 0; t < 8; ++t) gq[t] = __builtin_amdgcn_raw_buffer_load_b128(ws, QP_OFF + (g * 4 * 256 + t * 128) * 8 + lane * 16, 0, AUX_SC1);
#pragma unroll
      for (int t = 0; t < 8; ++t) ok = ok && (gq[t].x & 0xffu) == tlo && gq[t].y == thi && (gq[t].z & 0xffu) == tlo && gq[t].w == thi;
      if (__all(ok)) {
#pragma unroll
        for (int h = 0; h < 2; ++h) {
          const float q0 = (asf(gq[h].x & 0xffffff00u) + asf(gq[2 + h].x & 0xffffff00u)) + (asf(gq[4 + h].x & 0xffffff00u) + asf(gq[6 + h].x & 0xffffff00u));
          const float q1 = (asf(gq[h].z & 0xffffff00u) + asf(gq[2 + h].z & 0xffffff00u)) + (asf(gq[4 + h].z & 0xffffff00u) + asf(gq[6 + h].z & 0xffffff00u));
          ((LAS unsigned*)lds)[L_Q + h * 64 + lane] = pk(q0, q1);
        }
        break;
      }
    }
    LGKM0();
    lflag[L_FLAG] = 1u;
    ST(2);
    if (!is_wo || red >= 0) {
      for (unsigned spin = 0; spin < SPIN_CAP; ++spin) {
        CBAR();
        const v4u f0 = __builtin_amdgcn_raw_buffer_load_b128(ws, FL_OFF + lane * 16, 0, AUX_SC1);
        const v4u f1 = __builtin_amdgcn_raw_buffer_load_b128(ws, FL_OFF + 1024 + lane * 16, 0, AUX_SC1);
        const unsigned a = (unsigned)tag, bb = (unsigned)(tag >> 32);
        if (__all(f0.x == a && f0.y == bb && f0.z == a && f0.w == bb && f1.x == a && f1.y == bb && f1.z == a && f1.w == bb)) break;
      }
    }
    ST(3);
  } else {
    const int a = wave - 1;
    const size_t off = ((size_t)g * SK + (SK - WIN) + 256 * j + 32 * a) * DH + lane * 8;
    v4u kk[8], vv[8];
#pragma unroll
    for (int i = 0; i < 8; ++i) kk[i] = ldnt4(p.k + off + i * 512);
#pragma unroll
    for (int i = 0; i < 8; ++i) vv[i] = ldnt4(p.v + off + i * 512);
    ST(1);
    for (unsigned spin = 0; spin < (SPIN_CAP << 6); ++spin) { if (lflag[L_FLAG] != 0u) break; __builtin_amdgcn_s_sleep(1); }
    CBAR();
    ST(2);
    const v4u q0 = *(const LAS v4u*)&((LAS unsigned*)lds)[L_Q + 4 * c], q1 = *(const LAS v4u*)&((LAS unsigned*)lds)[L_Q + 64 + 4 * c];
    float x[16];
#pragma unroll
    for (int i = 0; i < 8; ++i) {
      float s0 = 0.f, s1 = 0.f;
#pragma unroll
      for (int k = 0; k < 4; ++k) { s0 = dot2(kk[i][k], q0[k], s0); s1 = dot2(kk[i][k], q1[k], s1); }
      x[2 * i] = s0; x[2 * i + 1] = s1;
    }
#pragma unroll
    for (int h = 0; h < 8; ++h) x[h] = tdpp<XOR8>(x[h], x[h + 8], c & 8);
#pragma unroll
    for (int h = 0; h < 4; ++h) x[h] = tdpp<XOR7>(x[h], x[h + 4], c & 4);
#pragma unroll
    for (int h = 0; h < 2; ++h) x[h] = tdpp<XOR2>(x[h], x[h + 2], c & 2);
    const float sraw = tdpp<XOR1>(x[0], x[1], c & 1);
    const float s = rb(rb(sraw) * 0.08838834764831845f);
    float m = s; m = fmaxf(m, dpp<XOR2>(m)); m = fmaxf(m, dpp<ROR4>(m)); m = fmaxf(m, dpp<XOR8>(m)); m = x32_max(x16_max(m));
    const float pe = __builtin_amdgcn_exp2f((s - m) * 1.4426950408889634f);
    float l = pe; l += dpp<XOR2>(l); l += dpp<ROR4>(l); l += dpp<XOR8>(l); l = x32_sum(x16_sum(l));
    ST(3);
    float acc0[8], acc1[8];
#pragma unroll
    for (int k = 0; k < 8; ++k) { acc0[k] = 0.f; acc1[k] = 0.f; }
#define PV(i) { const float p0 = dpp<NEWB + 2 * (i)>(pe), p1 = dpp<NEWB + 2 * (i) + 1>(pe); \
    _Pragma("unroll") for (int k = 0; k < 4; ++k) { const float va = lo16(vv[i][k]), vb = hi16(vv[i][k]); \
      acc0[2 * k] = fmaf(p0, va, acc0[2 * k]); acc0[2 * k + 1] = fmaf(p0, vb, acc0[2 * k + 1]); \
      acc1[2 * k] = fmaf(p1, va, acc1[2 * k]); acc1[2 * k + 1] = fmaf(p1, vb, acc1[2 * k + 1]); } }
    PV(0) PV(1) PV(2) PV(3) PV(4) PV(5) PV(6) PV(7)
#undef PV
    float o8[8]; v4f o4;
#pragma unroll
    for (int k = 0; k < 8; ++k) o8[k] = t32(acc0[k], acc1[k]);
#pragma unroll
    for (int k = 0; k < 4; ++k) o4[k] = t16(o8[k], o8[k + 4]);
    *(LAS v4f*)&((LAS float*)lds)[L_PO + a * 256 + (r >> 1) * 128 + 8 * c + 4 * (r & 1)] = o4;
    if (lane < 2) *(LAS v2f*)&((LAS float*)lds)[L_PS + (lane * 8 + a) * 2] = v2f{m, l};
    ST(4);
  }
  bar();
  ST(5);
  if (wave == 0) {
    const int h = lane >> 5;
    float mm[8], ll[8];
#pragma unroll
    for (int a = 0; a < 8; a += 2) { const v4f t = *(const LAS v4f*)&((LAS float*)lds)[L_PS + (h * 8 + a) * 2]; mm[a] = t.x; ll[a] = t.y; mm[a + 1] = t.z; ll[a + 1] = t.w; }
    float M = mm[0];
#pragma unroll
    for (int a = 1; a < 8; ++a) M = fmaxf(M, mm[a]);
    v4f o = {0.f, 0.f, 0.f, 0.f}; float L = 0.f;
#pragma unroll
    for (int a = 0; a < 8; ++a) {
      const float wgt = __builtin_amdgcn_exp2f((mm[a] - M) * 1.4426950408889634f);
      const v4f t = *(const LAS v4f*)&((LAS float*)lds)[L_PO + a * 256 + 4 * lane];
      L = fmaf(wgt, ll[a], L);
#pragma unroll
      for (int k = 0; k < 4; ++k) o[k] = fmaf(wgt, t[k], o[k]);
    }
    __builtin_amdgcn_raw_buffer_store_b128(v4u{clean(o.x), clean(o.y), clean(o.z), clean(o.w)}, ws, OP_OFF + (g * 16 + j) * 1024 + lane * 16, 0, AUX_SC1);
    if ((lane & 31) == 0) __builtin_amdgcn_raw_buffer_store_b64(v2u{clean(M), clean(L)}, ws, ST_OFF + ((g * 2 + h) * 16 + j) * 8, 0, AUX_SC1);
    ST(6);
  }
  if (is_wo) {
    if (wave > 0) {
      const int a = wave - 1, hd = a >> 2;
      const u16* wb = p.wo + (size_t)(256 * g + 32 * a + 16 * (r & 1) + 8 * (r >> 1)) * D + 96 * j + 6 * c;
      v3u w[8];
#pragma unroll
      for (int i = 0; i < 8; ++i) w[i] = ldnt3(wb + (size_t)i * D);
      unsigned ov[8]; v2u st;
      for (unsigned spin = 0; spin < SPIN_CAP; ++spin) {
        bool ok = true;
        CBAR();
        st = __builtin_amdgcn_raw_buffer_load_b64(ws, ST_OFF + ((g * 2 + hd) * 16 + c) * 8, 0, AUX_SC1);
#pragma unroll
        for (int i = 0; i < 8; ++i) ov[i] = __builtin_amdgcn_raw_buffer_load_b32(ws, OP_OFF + (g * 16 + (lane >> 5) * 8 + i) * 1024 + (32 * a + (lane & 31)) * 4, 0, AUX_SC1);
        ok = st.x != SENT && st.y != SENT;
#pragma unroll
        for (int i = 0; i < 8; ++i) ok = ok && ov[i] != SENT;
        if (__all(ok)) break;
      }
      ST(7);
      const float mp = asf(st.x);
      float M = mp; M = fmaxf(M, dpp<XOR1>(M)); M = fmaxf(M, dpp<XOR2>(M)); M = fmaxf(M, dpp<XOR7>(M)); M = fmaxf(M, dpp<XOR8>(M));
      float wgt = __builtin_amdgcn_exp2f((mp - M) * 1.4426950408889634f);
      const float L = row_sum(wgt * asf(st.y));
      wgt = lane >= 32 ? dpp<XOR8>(wgt) : wgt;
      float o = 0.f;
#define OW(i) o = fmaf(dpp<NEWB + (i)>(wgt), asf(ov[i]), o);
      OW(0) OW(1) OW(2) OW(3) OW(4) OW(5) OW(6) OW(7)
#undef OW
      o = x32_sum(o) / L;
      o = lane >= 32 ? dpp<XOR8>(o) : o;
      const unsigned op = pk(o, dpp<XOR1>(o));
      float acc[6];
#pragma unroll
      for (int k = 0; k < 6; ++k) acc[k] = 0.f;
#define WO(i) { const unsigned ob = dppu<NEWB + (i)>(op); _Pragma("unroll") for (int k = 0; k < 3; ++k) { \
        acc[2 * k] = dot2(__builtin_amdgcn_perm(w[i + 1][k], w[i][k], 0x05040100), ob, acc[2 * k]); \
        acc[2 * k + 1] = dot2(__builtin_amdgcn_perm(w[i + 1][k], w[i][k], 0x07060302), ob, acc[2 * k + 1]); } }
      WO(0) WO(2) WO(4) WO(6)
#undef WO
      float a3[3];
#pragma unroll
      for (int k = 0; k < 3; ++k) a3[k] = x16_sum(t32(acc[k], acc[k + 3]));
      if ((r & 1) == 0) {
#pragma unroll
        for (int k = 0; k < 3; ++k) lds[L_R + a * 96 + 6 * c + 3 * (r >> 1) + k] = a3[k];
      }
    }
    bar();
    if (wave == 0 && lane < 24) {
      v4f s = *(const LAS v4f*)&((LAS float*)lds)[L_R + 4 * lane];
#pragma unroll
      for (int a = 1; a < 8; ++a) s += *(const LAS v4f*)&((LAS float*)lds)[L_R + a * 96 + 4 * lane];
      __builtin_amdgcn_raw_buffer_store_b128(v4u{clean(s.x), clean(s.y), clean(s.z), clean(s.w)}, ws, HP_OFF + (g * 576 + j * 96) * 4 + lane * 16, 0, AUX_SC1);
    }
    ST(8);
  } else if (is_mlp) {
    const int r4 = lane >> 2, c4 = lane & 3, r8 = lane >> 3, c8 = lane & 7;
    v4u w1r[4], w2r[4];
#pragma unroll
    for (int i = 0; i < 4; ++i) w1r[i] = ldnt4(p.w1 + (size_t)(64 * wave + 4 * r4 + i) * FF + 32 * u + 8 * c4);
#pragma unroll
    for (int i = 0; i < 4; ++i) w2r[i] = ldnt4(p.w2 + (size_t)(32 * u + 4 * r8 + i) * D + 64 * wave + 8 * c8);
    const v2u xr = *(const GAS v2u*)(p.x + 64 * wave + 4 * c);
    const float b1v = lo16(((const GAS u16*)p.b1)[32 * u + 4 * r8 + c4]);
    v4u hv[4];
    for (unsigned spin = 0; spin < SPIN_CAP; ++spin) {
      bool ok = true;
      CBAR();
#pragma unroll
      for (int i = 0; i < 4; ++i) hv[i] = __builtin_amdgcn_raw_buffer_load_b128(ws, HP_OFF + ((4 * i + r) * 576 + 64 * wave + 4 * c) * 4, 0, AUX_SC1);
#pragma unroll
      for (int i = 0; i < 4; ++i) ok = ok && hv[i].x != SENT && hv[i].y != SENT && hv[i].z != SENT && hv[i].w != SENT;
      if (__all(ok)) break;
    }
    ST(7);
    float s4[4];
#pragma unroll
    for (int k = 0; k < 4; ++k) s4[k] = (asf(hv[0][k]) + asf(hv[1][k])) + (asf(hv[2][k]) + asf(hv[3][k]));
    const float s2a = t32(s4[0], s4[2]), s2b = t32(s4[1], s4[3]);
    const float at = t16(s2a, s2b);
    const unsigned xw = (r & 2) ? xr.y : xr.x;
    const float hval = rb(((r & 1) ? hi16(xw) : lo16(xw)) + rb(at));
    lds[L_H + 64 * wave + 4 * c + r] = hval;
    bar();
    float h9[9];
#pragma unroll
    for (int i = 0; i < 9; ++i) h9[i] = lds[L_H + lane + 64 * i];
    const v4f hr = *(const LAS v4f*)&((LAS float*)lds)[L_H + 64 * wave + 4 * r4];
    float mb1, den1, mb2, den2;
    ln_stats9(h9, asf(__builtin_amdgcn_readfirstlane(asu(h9[0]))), mb1, den1);
#pragma unroll
    for (int i = 0; i < 9; ++i) h9[i] = ln_apply(h9[i], mb1, den1);
    ln_stats9(h9, 0.f, mb2, den2);
    float y[4];
#pragma unroll
    for (int i = 0; i < 4; ++i) y[i] = ln_apply(ln_apply(hr[i], mb1, den1), mb2, den2);
    const unsigned y01 = pk(y[0], y[1]), y23 = pk(y[2], y[3]);
    float z[8];
#pragma unroll
    for (int k = 0; k < 4; ++k) {
      z[2 * k] = dot2(__builtin_amdgcn_perm(w1r[1][k], w1r[0][k], 0x05040100), y01, dot2(__builtin_amdgcn_perm(w1r[3][k], w1r[2][k], 0x05040100), y23, 0.f));
      z[2 * k + 1] = dot2(__builtin_amdgcn_perm(w1r[1][k], w1r[0][k], 0x07060302), y01, dot2(__builtin_amdgcn_perm(w1r[3][k], w1r[2][k], 0x07060302), y23, 0.f));
    }
    float z4[4], z2[2];
#pragma unroll
    for (int k = 0; k < 4; ++k) z4[k] = t32(z[k], z[k + 4]);
#pragma unroll
    for (int k = 0; k < 2; ++k) z2[k] = t16(z4[k], z4[k + 2]);
    float z1 = tdpp<XOR8>(z2[0], z2[1], lane & 8);
    z1 += dpp<0x141>(dpp<0x1B>(z1));
    if ((lane & 4) == 0) lds[L_Z + wave * 32 + 8 * c4 + ((lane >> 5) << 2) + (((lane >> 4) & 1) << 1) + ((lane >> 3) & 1)] = z1;
    bar();
    float zz = 0.f;
#pragma unroll
    for (int i = 0; i < 9; ++i) zz += lds[L_Z + i * 32 + 4 * r8 + c4];
    const float zb = rb(rb(zz) + b1v);
    const float e = __builtin_amdgcn_exp2f(2.8853900817779268f * 0.7978845608028654f * (zb + 0.044715f * zb * zb * zb));
    const float th = 1.f - 2.f / (e + 1.f);
    const float gl = 0.5f * zb * (1.f + th);
    const unsigned gp = pk(gl, dpp<XOR1>(gl));
    const unsigned g01 = dppu<0x00>(gp), g23 = dppu<0xAA>(gp);
    float o[8];
#pragma unroll
    for (int k = 0; k < 4; ++k) {
      o[2 * k] = dot2(__builtin_amdgcn_perm(w2r[1][k], w2r[0][k], 0x05040100), g01, dot2(__builtin_amdgcn_perm(w2r[3][k], w2r[2][k], 0x05040100), g23, 0.f));
      o[2 * k + 1] = dot2(__builtin_amdgcn_perm(w2r[1][k], w2r[0][k], 0x07060302), g01, dot2(__builtin_amdgcn_perm(w2r[3][k], w2r[2][k], 0x07060302), g23, 0.f));
    }
    v4f o4;
#pragma unroll
    for (int k = 0; k < 4; ++k) { float t = t32(o[k], o[k + 4]); t = x16_sum(t); o4[k] = t + dpp<XOR8>(t); }
    if ((lane & 24) == 0)
      __builtin_amdgcn_raw_buffer_store_b128(v4u{clean(o4.x), clean(o4.y), clean(o4.z), clean(o4.w)}, ws, MP_OFF + u * 2304 + (64 * wave + 8 * c8 + 4 * (lane >> 5)) * 4, 0, AUX_SC1);
    ST(8);
  }
  if (red >= 0) {
    const int pq = lane >> 2, c4 = lane & 3;
    float hfin = 0.f, b2v = 0.f;
    if (wave == 0) {
      v4u hv;
      for (unsigned spin = 0; spin < SPIN_CAP; ++spin) {
        CBAR();
        hv = __builtin_amdgcn_raw_buffer_load_b128(ws, HP_OFF + (pq * 576 + 16 * red + 4 * c4) * 4, 0, AUX_SC1);
        if (__all(hv.x != SENT && hv.y != SENT && hv.z != SENT && hv.w != SENT)) break;
      }
      const float s2a = t32(asf(hv.x), asf(hv.z)), s2b = t32(asf(hv.y), asf(hv.w));
      float at = t16(s2a, s2b);
      at += dpp<XOR8>(at); at += dpp<0x141>(dpp<0x1B>(at));
      const int col = 16 * red + 4 * c4 + r;
      hfin = rb(lo16(((const GAS u16*)p.x)[col]) + rb(at));
      b2v = lo16(((const GAS u16*)p.b2)[col]);
    }
    v4u mv;
    for (unsigned spin = 0; spin < SPIN_CAP; ++spin) {
      CBAR();
      mv = __builtin_amdgcn_raw_buffer_load_b128(ws, MP_OFF + (16 * wave + pq) * 2304 + (16 * red + 4 * c4) * 4, 0, AUX_SC1);
      if (__all(mv.x != SENT && mv.y != SENT && mv.z != SENT && mv.w != SENT)) break;
    }
    ST(9);
    const float s2a = t32(asf(mv.x), asf(mv.z)), s2b = t32(asf(mv.y), asf(mv.w));
    float t = t16(s2a, s2b);
    t += dpp<XOR8>(t); t += dpp<0x141>(dpp<0x1B>(t));
    if ((lane & 12) == 0) lds[L_Z + wave * 16 + 4 * c4 + r] = t;
    bar();
    if (wave == 0) {
      float s = 0.f;
#pragma unroll
      for (int i = 0; i < 9; ++i) s += lds[L_Z + i * 16 + 4 * c4 + r];
      const float res = hfin + rb(rb(s) + b2v);
      if ((lane & 12) == 0) ((GAS u16*)p.out)[16 * red + 4 * c4 + r] = (u16)(pk(res, res) & 0xffffu);
      if (red == 0 && lane == 0) __builtin_amdgcn_raw_buffer_store_b64(v2u{(unsigned)tag, (unsigned)(tag >> 32)}, ws, EP_OFF, 0, AUX_SC1);
    }
    ST(10);
  }
}

extern "C" void kernel_launch(void* const* d_in, const int* in_sizes, int n_in, void* d_out, int out_size, void* d_ws, size_t ws_size, hipStream_t stream) {
  Params p;
  p.x = (const u16*)d_in[0]; p.k = (const u16*)d_in[1]; p.v = (const u16*)d_in[2]; p.wq = (const u16*)d_in[3]; p.wo = (const u16*)d_in[4];
  p.w1 = (const u16*)d_in[6]; p.b1 = (const u16*)d_in[7]; p.w2 = (const u16*)d_in[8]; p.b2 = (const u16*)d_in[9];
  p.out = (u16*)d_out; p.ws = (char*)d_ws;
  hipLaunchKernelGGL(fused, dim3(NBLK), dim3(NTHR), 0, stream, p);
}
```

```cpp
#define PROBE 0
#include <hip/hip_runtime.h>
#include <hip/hip_bf16.h>

typedef unsigned short u16;
typedef unsigned long long u64;
typedef unsigned v4u __attribute__((ext_vector_type(4)));
typedef unsigned v3u __attribute__((ext_vector_type(3)));
typedef unsigned v2u __attribute__((ext_vector_type(2)));
typedef float v4f __attribute__((ext_vector_type(4)));
typedef float v2f __attribute__((ext_vector_type(2)));
typedef __bf16 bf16x2 __attribute__((ext_vector_type(2)));
#define GAS __attribute__((address_space(1)))
#define LAS __attribute__((address_space(3)))
#define RLX_AGENT __ATOMIC_RELAXED, __HIP_MEMORY_SCOPE_AGENT
#define STAMP 0

constexpr int D = 576, SK = 65536, DH = 128, WIN = 4096, FF = 4608, QW = 4096;
constexpr int NTHR = 576, NBLK = 256;
constexpr unsigned EP_OFF = 0, FL_OFF = 4096, QP_OFF = 8192, OP_OFF = QP_OFF + 16 * 4 * 256 * 8, ST_OFF = OP_OFF + 16 * 16 * 256 * 4,
                   HP_OFF = ST_OFF + 16 * 2 * 16 * 8, MP_OFF = HP_OFF + 16 * 576 * 4, WS_END = MP_OFF + 144 * 576 * 4, DBG_OFF = 1u << 20;
constexpr unsigned SENT = 0xFFFFFFFFu;
constexpr unsigned SPIN_CAP = 1u << 20;
constexpr int AUX_SC1 = 16, AUX_NT = 2;

struct Params { const u16 *x, *k, *v, *wq, *wo, *w1, *b1, *w2, *b2; u16* out; char* ws; };

__device__ __forceinline__ float asf(unsigned u) { return __builtin_bit_cast(float, u); }
__device__ __forceinline__ unsigned asu(float f) { return __builtin_bit_cast(unsigned, f); }
__device__ __forceinline__ float lo16(unsigned u) { return asf(u << 16); }
__device__ __forceinline__ float hi16(unsigned u) { return asf(u & 0xffff0000u); }
__device__ __forceinline__ unsigned pk(float lo, float hi) {
  bf16x2 r; r.x = (__bf16)lo; r.y = (__bf16)hi; return __builtin_bit_cast(unsigned, r);
}
__device__ __forceinline__ float rb(float f) { return lo16(pk(f, f) & 0xffffu); }
__device__ __forceinline__ float dot2(unsigned a, unsigned b, float c) {
  return __builtin_amdgcn_fdot2_f32_bf16(__builtin_bit_cast(bf16x2, a), __builtin_bit_cast(bf16x2, b), c, false);
}
template <int CTRL> __device__ __forceinline__ float dpp(float x) { return asf(__builtin_amdgcn_mov_dpp(asu(x), CTRL, 0xf, 0xf, true)); }
template <int CTRL> __device__ __forceinline__ unsigned dppu(unsigned x) { return __builtin_amdgcn_mov_dpp(x, CTRL, 0xf, 0xf, true); }
constexpr int XOR1 = 0xB1, XOR2 = 0x4E, XOR7 = 0x141, XOR8 = 0x128, ROR4 = 0x124, NEWB = 0x150;
__device__ __forceinline__ float x16_sum(float x) { auto s = __builtin_amdgcn_permlane16_swap(asu(x), asu(x), false, false); return asf(s[0]) + asf(s[1]); }
__device__ __forceinline__ float x32_sum(float x) { auto s = __builtin_amdgcn_permlane32_swap(asu(x), asu(x), false, false); return asf(s[0]) + asf(s[1]); }
__device__ __forceinline__ float x16_max(float x) { auto s = __builtin_amdgcn_permlane16_swap(asu(x), asu(x), false, false); return fmaxf(asf(s[0]), asf(s[1])); }
__device__ __forceinline__ float x32_max(float x) { auto s = __builtin_amdgcn_permlane32_swap(asu(x), asu(x), false, false); return fmaxf(asf(s[0]), asf(s[1])); }
__device__ __forceinline__ float t32(float a, float b) { auto s = __builtin_amdgcn_permlane32_swap(asu(a), asu(b), false, false); return asf(s[0]) + asf(s[1]); }
__device__ __forceinline__ float t16(float a, float b) { auto s = __builtin_amdgcn_permlane16_swap(asu(a), asu(b), false, false); return asf(s[0]) + asf(s[1]); }
template <int CTRL> __device__ __forceinline__ float tdpp(float a, float b, bool hi) { const float s = hi ? a : b, k = hi ? b : a; return k + dpp<CTRL>(s); }
__device__ __forceinline__ float row_sum(float x) { x += dpp<XOR1>(x); x += dpp<XOR2>(x); x += dpp<XOR7>(x); x += dpp<XOR8>(x); return x; }
__device__ __forceinline__ float wave_sum(float x) { return x32_sum(x16_sum(row_sum(x))); }
__device__ __forceinline__ v4u ldnt4(const void* p) { return __builtin_nontemporal_load((const GAS v4u*)p); }
__device__ __forceinline__ v3u ldnt3(const void* p) { return __builtin_nontemporal_load((const GAS v3u*)p); }
__device__ __forceinline__ unsigned clean(float f) { const unsigned u = asu(f); return u < SENT ? u : SENT - 1u; }
#define DRAIN() asm volatile("s_waitcnt vmcnt(0)" ::: "memory")
#define CBAR() asm volatile("" ::: "memory")
#define LGKM0() asm volatile("s_waitcnt lgkmcnt(0)" ::: "memory")
__device__ __forceinline__ void bar() { LGKM0(); __builtin_amdgcn_s_barrier(); CBAR(); }
#define ST(i) do { if ((PROBE == (i) && wave != 0) || (PROBE == 100 + (i) && wave == 0)) { __builtin_amdgcn_sched_barrier(0); t_ev = __builtin_amdgcn_s_memrealtime(); __builtin_amdgcn_sched_barrier(0); } } while (0)

__device__ __forceinline__ void ln_stats9(const float (&h)[9], float c, float& mb, float& den) {
  float s = 0.f, q = 0.f;
#pragma unroll
  for (int i = 0; i < 9; ++i) { const float d = h[i] - c; s += d; q = fmaf(d, d, q); }
  s = wave_sum(s); q = wave_sum(q);
  const float md = s * (1.f / D);
  const float var = fmaxf(q * (1.f / D) - md * md, 0.f);
  mb = rb(c + md);
  den = rb(__builtin_sqrtf(rb(rb(var) + 1e-5f)));
}
__device__ __forceinline__ float ln_apply(float h, float mb, float den) { return rb(rb(h - mb) / den); }

constexpr int L_FLAG = 0, L_Q = 16, L_Y = L_Q + 128, L_PO = L_Y + 576, L_PS = L_PO + 8 * 256, L_H = L_PS + 32, L_Z = L_H + 576, L_R = L_Z + 9 * 32, L_END = L_R + 8 * 96, L_PAD = 21 * 1024;

__global__ void __launch_bounds__(NTHR, 3) fused(Params p) {
  __shared__ __attribute__((aligned(16))) float lds[L_PAD];
  const int b = blockIdx.x, tid = threadIdx.x, wave = __builtin_amdgcn_readfirstlane(tid >> 6), lane = tid & 63;
  const int g = (b & 7) * 2 + ((b >> 3) & 1), j = b >> 4;
  const bool is_wo = j < 6, is_mlp = j >= 6 && j < 15;
  const int u = g * 9 + (j - 6);
  const int red = j == 15 ? g : (j == 0 ? 16 + g : (j == 1 && g < 4 ? 32 + g : -1));
  const __amdgpu_buffer_rsrc_t ws = __builtin_amdgcn_make_buffer_rsrc(p.ws, 0, WS_END, 0x00020000);
  volatile LAS unsigned* lflag = (volatile LAS unsigned*)lds;
  u64 t_ev = 0;
  ST(0);
  if (tid == 0) lflag[L_FLAG] = 0u;
  bar();
  const int r = lane >> 4, c = lane & 15;
  u64 tag = 0;
  if (wave == 0) {
    const v4u sent = {SENT, SENT, SENT, SENT};
    __builtin_amdgcn_raw_buffer_store_b128(sent, ws, OP_OFF + (g * 16 + j) * 1024 + lane * 16, 0, AUX_SC1);
    if (lane < 2) __builtin_amdgcn_raw_buffer_store_b64(v2u{SENT, SENT}, ws, ST_OFF + ((g * 2 + lane) * 16 + j) * 8, 0, AUX_SC1);
    if (is_wo) { if (lane < 24) __builtin_amdgcn_raw_buffer_store_b128(sent, ws, HP_OFF + (g * 576 + j * 96) * 4 + lane * 16, 0, AUX_SC1); }
    if (is_mlp) {
      __builtin_amdgcn_raw_buffer_store_b128(sent, ws, MP_OFF + u * 2304 + lane * 16, 0, AUX_SC1);
      __builtin_amdgcn_raw_buffer_store_b128(sent, ws, MP_OFF + u * 2304 + 1024 + lane * 16, 0, AUX_SC1);
      if (lane < 16) __builtin_amdgcn_raw_buffer_store_b128(sent, ws, MP_OFF + u * 2304 + 2048 + lane * 16, 0, AUX_SC1);
    }
    const v2u ep = __builtin_amdgcn_raw_buffer_load_b64(ws, EP_OFF, 0, AUX_SC1);
    float xv[9];
#pragma unroll
    for (int i = 0; i < 9; ++i) xv[i] = lo16(((const GAS u16*)p.x)[lane + 64 * i]);
    const int qt = j >> 2, qc = j & 3, rr = lane >> 3, cc = lane & 7;
    const u16* wqb = p.wq + (size_t)(144 * qt + 18 * rr) * QW + 256 * g + 64 * qc + 8 * cc;
    v4u w[18];
#pragma unroll
    for (int i = 0; i < 18; ++i) w[i] = ldnt4(wqb + (size_t)i * QW);
    float mb, den;
    ln_stats9(xv, asf(__builtin_amdgcn_readfirstlane(asu(xv[0]))), mb, den);
#pragma unroll
    for (int i = 0; i < 9; ++i) lds[L_Y + lane + 64 * i] = ln_apply(xv[i], mb, den);
    LGKM0();
    float acc[8];
#pragma unroll
    for (int k = 0; k < 8; ++k) acc[k] = 0.f;
#pragma unroll
    for (int i = 0; i < 18; i += 2) {
      const v2f y = *(const LAS v2f*)&((LAS float*)lds)[L_Y + 144 * qt + 18 * rr + i];
      const unsigned yp = (asu(y.x) >> 16) | (asu(y.y) & 0xffff0000u);
#pragma unroll
      for (int k = 0; k < 4; ++k) {
        acc[2 * k] = dot2(__builtin_amdgcn_perm(w[i + 1][k], w[i][k], 0x05040100), yp, acc[2 * k]);
        acc[2 * k + 1] = dot2(__builtin_amdgcn_perm(w[i + 1][k], w[i][k], 0x07060302), yp, acc[2 * k + 1]);
      }
    }
    float a4[4], a2[2];
#pragma unroll
    for (int k = 0; k < 4; ++k) a4[k] = t32(acc[k], acc[k + 4]);
#pragma unroll
    for (int k = 0; k < 2; ++k) a2[k] = t16(a4[k], a4[k + 2]);
    const float qp = tdpp<XOR8>(a2[0], a2[1], lane & 8);
    const int col = 8 * cc + ((lane >> 5) << 2) + (((lane >> 4) & 1) << 1) + ((lane >> 3) & 1);
    tag = (((u64)ep.y << 32) | ep.x) + 1ull;
    tag = ((u64)(unsigned)__builtin_amdgcn_readfirstlane((unsigned)(tag >> 32)) << 32) | (unsigned)__builtin_amdgcn_readfirstlane((unsigned)tag);
    DRAIN();
    const unsigned tlo = (unsigned)tag & 0xffu, thi = (unsigned)(tag >> 8);
    __builtin_amdgcn_raw_buffer_store_b64(v2u{((asu(qp) + 0x80u) & 0xffffff00u) | tlo, thi}, ws, QP_OFF + ((g * 4 + qt) * 256 + 64 * qc + col) * 8, 0, AUX_SC1);
    if (lane == 0) __builtin_amdgcn_raw_buffer_store_b64(v2u{(unsigned)tag, (unsigned)(tag >> 32)}, ws, FL_OFF + b * 8, 0, AUX_SC1);
    ST(1);
    for (unsigned spin = 0; spin < SPIN_CAP; ++spin) {
      v4u gq[8]; bool ok = true;
      CBAR();
#pragma unroll
      for (int t = 0; t < 8; ++t) gq[t] = __builtin_amdgcn_raw_buffer_load_b128(ws, QP_OFF + (g * 4 * 256 + t * 128) * 8 + lane * 16, 0, AUX_SC1);
#pragma unroll
      for (int t = 0; t < 8; ++t) ok = ok && (gq[t].x & 0xffu) == tlo && gq[t].y == thi && (gq[t].z & 0xffu) == tlo && gq[t].w == thi;
      if (__all(ok)) {
#pragma unroll
        for (int h = 0; h < 2; ++h) {
          const float q0 = (asf(gq[h].x & 0xffffff00u) + asf(gq[2 + h].x & 0xffffff00u)) + (asf(gq[4 + h].x & 0xffffff00u) + asf(gq[6 + h].x & 0xffffff00u));
          const float q1 = (asf(gq[h].z & 0xffffff00u) + asf(gq[2 + h].z & 0xffffff00u)) + (asf(gq[4 + h].z & 0xffffff00u) + asf(gq[6 + h].z & 0xffffff00u));
          ((LAS unsigned*)lds)[L_Q + h * 64 + lane] = pk(q0, q1);
        }
        break;
      }
    }
    LGKM0();
    lflag[L_FLAG] = 1u;
    ST(2);
    if (!is_wo || red >= 0) {
      for (unsigned spin = 0; spin < SPIN_CAP; ++spin) {
        CBAR();
        const v4u f0 = __builtin_amdgcn_raw_buffer_load_b128(ws, FL_OFF + lane * 16, 0, AUX_SC1);
        const v4u f1 = __builtin_amdgcn_raw_buffer_load_b128(ws, FL_OFF + 1024 + lane * 16, 0, AUX_SC1);
        const unsigned a = (unsigned)tag, bb = (unsigned)(tag >> 32);
        if (__all(f0.x == a && f0.y == bb && f0.z == a && f0.w == bb && f1.x == a && f1.y == bb && f1.z == a && f1.w == bb)) break;
      }
    }
    ST(3);
  } else {
    const int a = wave - 1;
    const size_t off = ((size_t)g * SK + (SK - WIN) + 256 * j + 32 * a) * DH + lane * 8;
    v4u kk[8], vv[8];
#pragma unroll
    for (int i = 0; i < 8; ++i) kk[i] = ldnt4(p.k + off + i * 512);
#pragma unroll
    for (int i = 0; i < 8; ++i) vv[i] = ldnt4(p.v + off + i * 512);
    ST(1);
    for (unsigned spin = 0; spin < (SPIN_CAP << 6); ++spin) { if (lflag[L_FLAG] != 0u) break; __builtin_amdgcn_s_sleep(1); }
    CBAR();
    ST(2);
    const v4u q0 = *(const LAS v4u*)&((LAS unsigned*)lds)[L_Q + 4 * c], q1 = *(const LAS v4u*)&((LAS unsigned*)lds)[L_Q + 64 + 4 * c];
    float x[16];
#pragma unroll
    for (int i = 0; i < 8; ++i) {
      float s0 = 0.f, s1 = 0.f;
#pragma unroll
      for (int k = 0; k < 4; ++k) { s0 = dot2(kk[i][k], q0[k], s0); s1 = dot2(kk[i][k], q1[k], s1); }
      x[2 * i] = s0; x[2 * i + 1] = s1;
    }
#pragma unroll
    for (int h = 0; h < 8; ++h) x[h] = tdpp<XOR8>(x[h], x[h + 8], c & 8);
#pragma unroll
    for (int h = 0; h < 4; ++h) x[h] = tdpp<XOR7>(x[h], x[h + 4], c & 4);
#pragma unroll
    for (int h = 0; h < 2; ++h) x[h] = tdpp<XOR2>(x[h], x[h + 2], c & 2);
    const float sraw = tdpp<XOR1>(x[0], x[1], c & 1);
    const float s = rb(rb(sraw) * 0.08838834764831845f);
    float m = s; m = fmaxf(m, dpp<XOR2>(m)); m = fmaxf(m, dpp<ROR4>(m)); m = fmaxf(m, dpp<XOR8>(m)); m = x32_max(x16_max(m));
    const float pe = __builtin_amdgcn_exp2f((s - m) * 1.4426950408889634f);
    float l = pe; l += dpp<XOR2>(l); l += dpp<ROR4>(l); l += dpp<XOR8>(l); l = x32_sum(x16_sum(l));
    ST(3);
    float acc0[8], acc1[8];
#pragma unroll
    for (int k = 0; k < 8; ++k) { acc0[k] = 0.f; acc1[k] = 0.f; }
#define PV(i) { const float p0 = dpp<NEWB + 2 * (i)>(pe), p1 = dpp<NEWB + 2 * (i) + 1>(pe); \
    _Pragma("unroll") for (int k = 0; k < 4; ++k) { const float va = lo16(vv[i][k]), vb = hi16(vv[i][k]); \
      acc0[2 * k] = fmaf(p0, va, acc0[2 * k]); acc0[2 * k + 1] = fmaf(p0, vb, acc0[2 * k + 1]); \
      acc1[2 * k] = fmaf(p1, va, acc1[2 * k]); acc1[2 * k + 1] = fmaf(p1, vb, acc1[2 * k + 1]); } }
    PV(0) PV(1) PV(2) PV(3) PV(4) PV(5) PV(6) PV(7)
#undef PV
    float o8[8]; v4f o4;
#pragma unroll
    for (int k = 0; k < 8; ++k) o8[k] = t32(acc0[k], acc1[k]);
#pragma unroll
    for (int k = 0; k < 4; ++k) o4[k] = t16(o8[k], o8[k + 4]);
    *(LAS v4f*)&((LAS float*)lds)[L_PO + a * 256 + (r >> 1) * 128 + 8 * c + 4 * (r & 1)] = o4;
    if (lane < 2) *(LAS v2f*)&((LAS float*)lds)[L_PS + (lane * 8 + a) * 2] = v2f{m, l};
    ST(4);
  }
  bar();
  ST(5);
  if (wave == 0) {
    const int h = lane >> 5;
    float mm[8], ll[8];
#pragma unroll
    for (int a = 0; a < 8; a += 2) { const v4f t = *(const LAS v4f*)&((LAS float*)lds)[L_PS + (h * 8 + a) * 2]; mm[a] = t.x; ll[a] = t.y; mm[a + 1] = t.z; ll[a + 1] = t.w; }
    float M = mm[0];
#pragma unroll
    for (int a = 1; a < 8; ++a) M = fmaxf(M, mm[a]);
    v4f o = {0.f, 0.f, 0.f, 0.f}; float L = 0.f;
#pragma unroll
    for (int a = 0; a < 8; ++a) {
      const float wgt = __builtin_amdgcn_exp2f((mm[a] - M) * 1.4426950408889634f);
      const v4f t = *(const LAS v4f*)&((LAS float*)lds)[L_PO + a * 256 + 4 * lane];
      L = fmaf(wgt, ll[a], L);
#pragma unroll
      for (int k = 0; k < 4; ++k) o[k] = fmaf(wgt, t[k], o[k]);
    }
    __builtin_amdgcn_raw_buffer_store_b128(v4u{clean(o.x), clean(o.y), clean(o.z), clean(o.w)}, ws, OP_OFF + (g * 16 + j) * 1024 + lane * 16, 0, AUX_SC1);
    if ((lane & 31) == 0) __builtin_amdgcn_raw_buffer_store_b64(v2u{clean(M), clean(L)}, ws, ST_OFF + ((g * 2 + h) * 16 + j) * 8, 0, AUX_SC1);
    ST(6);
  }
  if (is_wo) {
    if (wave > 0) {
      const int a = wave - 1, hd = a >> 2;
      const u16* wb = p.wo + (size_t)(256 * g + 32 * a + 16 * (r & 1) + 8 * (r >> 1)) * D + 96 * j + 6 * c;
      v3u w[8];
#pragma unroll
      for (int i = 0; i < 8; ++i) w[i] = ldnt3(wb + (size_t)i * D);
      unsigned ov[8]; v2u st;
      for (unsigned spin = 0; spin < SPIN_CAP; ++spin) {
        bool ok = true;
        CBAR();
        st = __builtin_amdgcn_raw_buffer_load_b64(ws, ST_OFF + ((g * 2 + hd) * 16 + c) * 8, 0, AUX_SC1);
#pragma unroll
        for (int i = 0; i < 8; ++i) ov[i] = __builtin_amdgcn_raw_buffer_load_b32(ws, OP_OFF + (g * 16 + (lane >> 5) * 8 + i) * 1024 + (32 * a + (lane & 31)) * 4, 0, AUX_SC1);
        ok = st.x != SENT && st.y != SENT;
#pragma unroll
        for (int i = 0; i < 8; ++i) ok = ok && ov[i] != SENT;
        if (__all(ok)) break;
      }
      ST(7);
      const float mp = asf(st.x);
      float M = mp; M = fmaxf(M, dpp<XOR1>(M)); M = fmaxf(M, dpp<XOR2>(M)); M = fmaxf(M, dpp<XOR7>(M)); M = fmaxf(M, dpp<XOR8>(M));
      float wgt = __builtin_amdgcn_exp2f((mp - M) * 1.4426950408889634f);
      const float L = row_sum(wgt * asf(st.y));
      wgt = lane >= 32 ? dpp<XOR8>(wgt) : wgt;
      float o = 0.f;
#define OW(i) o = fmaf(dpp<NEWB + (i)>(wgt), asf(ov[i]), o);
      OW(0) OW(1) OW(2) OW(3) OW(4) OW(5) OW(6) OW(7)
#undef OW
      o = x32_sum(o) / L;
      o = lane >= 32 ? dpp<XOR8>(o) : o;
      const unsigned op = pk(o, dpp<XOR1>(o));
      float acc[6];
#pragma unroll
      for (int k = 0; k < 6; ++k) acc[k] = 0.f;
#define WO(i) { const unsigned ob = dppu<NEWB + (i)>(op); _Pragma("unroll") for (int k = 0; k < 3; ++k) { \
        acc[2 * k] = dot2(__builtin_amdgcn_perm(w[i + 1][k], w[i][k], 0x05040100), ob, acc[2 * k]); \
        acc[2 * k + 1] = dot2(__builtin_amdgcn_perm(w[i + 1][k], w[i][k], 0x07060302), ob, acc[2 * k + 1]); } }
      WO(0) WO(2) WO(4) WO(6)
#undef WO
      float a3[3];
#pragma unroll
      for (int k = 0; k < 3; ++k) a3[k] = x16_sum(t32(acc[k], acc[k + 3]));
      if ((r & 1) == 0) {
#pragma unroll
        for (int k = 0; k < 3; ++k) lds[L_R + a * 96 + 6 * c + 3 * (r >> 1) + k] = a3[k];
      }
    }
    bar();
    if (wave == 0 && lane < 24) {
      v4f s = *(const LAS v4f*)&((LAS float*)lds)[L_R + 4 * lane];
#pragma unroll
      for (int a = 1; a < 8; ++a) s += *(const LAS v4f*)&((LAS float*)lds)[L_R + a * 96 + 4 * lane];
      __builtin_amdgcn_raw_buffer_store_b128(v4u{clean(s.x), clean(s.y), clean(s.z), clean(s.w)}, ws, HP_OFF + (g * 576 + j * 96) * 4 + lane * 16, 0, AUX_SC1);
    }
    ST(8);
  } else if (is_mlp) {
    const int r4 = lane >> 2, c4 = lane & 3, r8 = lane >> 3, c8 = lane & 7;
    v4u w1r[4], w2r[4];
#pragma unroll
    for (int i = 0; i < 4; ++i) w1r[i] = ldnt4(p.w1 + (size_t)(64 * wave + 4 * r4 + i) * FF + 32 * u + 8 * c4);
#pragma unroll
    for (int i = 0; i < 4; ++i) w2r[i] = ldnt4(p.w2 + (size_t)(32 * u + 4 * r8 + i) * D + 64 * wave + 8 * c8);
    const v2u xr = *(const GAS v2u*)(p.x + 64 * wave + 4 * c);
    const float b1v = lo16(((const GAS u16*)p.b1)[32 * u + 4 * r8 + c4]);
    v4u hv[4];
    for (unsigned spin = 0; spin < SPIN_CAP; ++spin) {
      bool ok = true;
      CBAR();
#pragma unroll
      for (int i = 0; i < 4; ++i) hv[i] = __builtin_amdgcn_raw_buffer_load_b128(ws, HP_OFF + ((4 * i + r) * 576 + 64 * wave + 4 * c) * 4, 0, AUX_SC1);
#pragma unroll
      for (int i = 0; i < 4; ++i) ok = ok && hv[i].x != SENT && hv[i].y != SENT && hv[i].z != SENT && hv[i].w != SENT;
      if (__all(ok)) break;
    }
    ST(7);
    float s4[4];
#pragma unroll
    for (int k = 0; k < 4; ++k) s4[k] = (asf(hv[0][k]) + asf(hv[1][k])) + (asf(hv[2][k]) + asf(hv[3][k]));
    const float s2a = t32(s4[0], s4[2]), s2b = t32(s4[1], s4[3]);
    const float at = t16(s2a, s2b);
    const unsigned xw = (r & 2) ? xr.y : xr.x;
    const float hval = rb(((r & 1) ? hi16(xw) : lo16(xw)) + rb(at));
    lds[L_H + 64 * wave + 4 * c + r] = hval;
    bar();
    float h9[9];
#pragma unroll
    for (int i = 0; i < 9; ++i) h9[i] = lds[L_H + lane + 64 * i];
    const v4f hr = *(const LAS v4f*)&((LAS float*)lds)[L_H + 64 * wave + 4 * r4];
    float mb1, den1, mb2, den2;
    ln_stats9(h9, asf(__builtin_amdgcn_readfirstlane(asu(h9[0]))), mb1, den1);
#pragma unroll
    for (int i = 0; i < 9; ++i) h9[i] = ln_apply(h9[i], mb1, den1);
    ln_stats9(h9, 0.f, mb2, den2);
    float y[4];
#pragma unroll
    for (int i = 0; i < 4; ++i) y[i] = ln_apply(ln_apply(hr[i], mb1, den1), mb2, den2);
    const unsigned y01 = pk(y[0], y[1]), y23 = pk(y[2], y[3]);
    float z[8];
#pragma unroll
    for (int k = 0; k < 4; ++k) {
      z[2 * k] = dot2(__builtin_amdgcn_perm(w1r[1][k], w1r[0][k], 0x05040100), y01, dot2(__builtin_amdgcn_perm(w1r[3][k], w1r[2][k], 0x05040100), y23, 0.f));
      z[2 * k + 1] = dot2(__builtin_amdgcn_perm(w1r[1][k], w1r[0][k], 0x07060302), y01, dot2(__builtin_amdgcn_perm(w1r[3][k], w1r[2][k], 0x07060302), y23, 0.f));
    }
    float z4[4], z2[2];
#pragma unroll
    for (int k = 0; k < 4; ++k) z4[k] = t32(z[k], z[k + 4]);
#pragma unroll
    for (int k = 0; k < 2; ++k) z2[k] = t16(z4[k], z4[k + 2]);
    float z1 = tdpp<XOR8>(z2[0], z2[1], lane & 8);
    z1 += dpp<0x141>(dpp<0x1B>(z1));
    if ((lane & 4) == 0) lds[L_Z + wave * 32 + 8 * c4 + ((lane >> 5) << 2) + (((lane >> 4) & 1) << 1) + ((lane >> 3) & 1)] = z1;
    bar();
    float zz = 0.f;
#pragma unroll
    for (int i = 0; i < 9; ++i) zz += lds[L_Z + i * 32 + 4 * r8 + c4];
    const float zb = rb(rb(zz) + b1v);
    const float e = __builtin_amdgcn_exp2f(2.8853900817779268f * 0.7978845608028654f * (zb + 0.044715f * zb * zb * zb));
    const float th = 1.f - 2.f / (e + 1.f);
    const float gl = 0.5f * zb * (1.f + th);
    const unsigned gp = pk(gl, dpp<XOR1>(gl));
    const unsigned g01 = dppu<0x00>(gp), g23 = dppu<0xAA>(gp);
    float o[8];
#pragma unroll
    for (int k = 0; k < 4; ++k) {
      o[2 * k] = dot2(__builtin_amdgcn_perm(w2r[1][k], w2r[0][k], 0x05040100), g01, dot2(__builtin_amdgcn_perm(w2r[3][k], w2r[2][k], 0x05040100), g23, 0.f));
      o[2 * k + 1] = dot2(__builtin_amdgcn_perm(w2r[1][k], w2r[0][k], 0x07060302), g01, dot2(__builtin_amdgcn_perm(w2r[3][k], w2r[2][k], 0x07060302), g23, 0.f));
    }
    v4f o4;
#pragma unroll
    for (int k = 0; k < 4; ++k) { float t = t32(o[k], o[k + 4]); t = x16_sum(t); o4[k] = t + dpp<XOR8>(t); }
    if ((lane & 24) == 0)
      __builtin_amdgcn_raw_buffer_store_b128(v4u{clean(o4.x), clean(o4.y), clean(o4.z), clean(o4.w)}, ws, MP_OFF + u * 2304 + (64 * wave + 8 * c8 + 4 * (lane >> 5)) * 4, 0, AUX_SC1);
    ST(8);
  }
  if (red >= 0) {
    const int pq = lane >> 2, c4 = lane & 3;
    float hfin = 0.f, b2v = 0.f;
    if (wave == 0) {
      v4u hv;
      for (unsigned spin = 0; spin < SPIN_CAP; ++spin) {
        CBAR();
        hv = __builtin_amdgcn_raw_buffer_load_b128(ws, HP_OFF + (pq * 576 + 16 * red + 4 * c4) * 4, 0, AUX_SC1);
        if (__all(hv.x != SENT && hv.y != SENT && hv.z != SENT && hv.w != SENT)) break;
      }
      const float s2a = t32(asf(hv.x), asf(hv.z)), s2b = t32(asf(hv.y), asf(hv.w));
      float at = t16(s2a, s2b);
      at += dpp<XOR8>(at); at += dpp<0x141>(dpp<0x1B>(at));
      const int col = 16 * red + 4 * c4 + r;
      hfin = rb(lo16(((const GAS u16*)p.x)[col]) + rb(at));
      b2v = lo16(((const GAS u16*)p.b2)[col]);
    }
    v4u mv;
    for (unsigned spin = 0; spin < SPIN_CAP; ++spin) {
      CBAR();
      mv = __builtin_amdgcn_raw_buffer_load_b128(ws, MP_OFF + (16 * wave + pq) * 2304 + (16 * red + 4 * c4) * 4, 0, AUX_SC1);
      if (__all(mv.x != SENT && mv.y != SENT && mv.z != SENT && mv.w != SENT)) break;
    }
    ST(9);
    const float s2a = t32(asf(mv.x), asf(mv.z)), s2b = t32(asf(mv.y), asf(mv.w));
    float t = t16(s2a, s2b);
    t += dpp<XOR8>(t); t += dpp<0x141>(dpp<0x1B>(t));
    if ((lane & 12) == 0) lds[L_Z + wave * 16 + 4 * c4 + r] = t;
    bar();
    if (wave == 0) {
      float s = 0.f;
#pragma unroll
      for (int i = 0; i < 9; ++i) s += lds[L_Z + i * 16 + 4 * c4 + r];
      const float res = hfin + rb(rb(s) + b2v);
      if ((lane & 12) == 0) ((GAS u16*)p.out)[16 * red + 4 * c4 + r] = (u16)(pk(res, res) & 0xffffu);
      if (red == 0 && lane == 0) __builtin_amdgcn_raw_buffer_store_b64(v2u{(unsigned)tag, (unsigned)(tag >> 32)}, ws, EP_OFF, 0, AUX_SC1);
    }
    ST(10);
  }
  if (t_ev && blockIdx.x == 0) while (__builtin_amdgcn_s_memrealtime() < t_ev + 4000) __builtin_amdgcn_s_sleep(1);
}

extern "C" void kernel_launch(void* const* d_in, const int* in_sizes, int n_in, void* d_out, int out_size, void* d_ws, size_t ws_size, hipStream_t stream) {
  Params p;
  p.x = (const u16*)d_in[0]; p.k = (const u16*)d_in[1]; p.v = (const u16*)d_in[2]; p.wq = (const u16*)d_in[3]; p.wo = (const u16*)d_in[4];
  p.w1 = (const u16*)d_in[6]; p.b1 = (const u16*)d_in[7]; p.w2 = (const u16*)d_in[8]; p.b2 = (const u16*)d_in[9];
  p.out = (u16*)d_out; p.ws = (char*)d_ws;
  hipLaunchKernelGGL(fused, dim3(NBLK), dim3(NTHR), 0, stream, p);
}
```

```cpp
#define PROBE 0
#define PROBEW 0
#include <hip/hip_runtime.h>
#include <hip/hip_bf16.h>

typedef unsigned short u16;
typedef unsigned long long u64;
typedef unsigned v4u __attribute__((ext_vector_type(4)));
typedef unsigned v2u __attribute__((ext_vector_type(2)));
typedef float v4f __attribute__((ext_vector_type(4)));
typedef float v2f __attribute__((ext_vector_type(2)));
typedef short v4s __attribute__((ext_vector_type(4)));
typedef __bf16 bf16x2 __attribute__((ext_vector_type(2)));
typedef __bf16 bf16x8 __attribute__((ext_vector_type(8)));
#define GAS __attribute__((address_space(1)))
#define LAS __attribute__((address_space(3)))
#define STAMP 0
#define WSPOS(sz) 0
#define L2WARM 1
#define DB 24
#define DC 28
#define WOEARLY 15
#define HEARLY 12
#define TRUNC 0
#define QSTAG 16
#define DELTA3 165
#define QDELAY 16
#define WTHR_WO 0
#define WTHR_MLP 0

constexpr int D = 576, SK = 65536, DH = 128, WIN = 4096, FF = 4608, QW = 4096;
constexpr int NWAVE = 12, NTHR = NWAVE * 64, NBLK = 256;
constexpr unsigned EP_OFF = 0, QP_OFF = 4096, OP_OFF = QP_OFF + 16 * 4 * 256 * 4, ST_OFF = OP_OFF + 16 * 16 * 256 * 4,
                   HP_OFF = ST_OFF + 256 * 128, MP_OFF = HP_OFF + 16 * 576 * 4, H2_OFF = MP_OFF + 145 * 576 * 4, H3_OFF = H2_OFF + 96 * 128, WS_END = H3_OFF + 144 * 128, DBG_OFF = 1u << 20;
constexpr unsigned SENT = 0xFFFFFFFFu;
constexpr unsigned SPIN_CAP = 1u << 16, LSPIN_CAP = 1u << 22;
constexpr int AUX_SC1 = 16;
constexpr unsigned CODE_BYTES = 15360;
constexpr unsigned F_L = 0, F_I = 4, F_W = 8, F_Q = 12, F_P = 16, F_KD = 20, F_PUB = 24, F_Y = 28, F_GO = 32, MX_OFF = 64, TG_OFF = 96,
                   Q_OFF = 128, P_OFF = Q_OFF + 512, WG_OFF = P_OFF + 1024, O_OFF = WG_OFF + 128, S_OFF = O_OFF + 512, X_OFF = S_OFF + 512,
                   Z_OFF = X_OFF + 2304, M_END = Z_OFF + 2304;
constexpr unsigned W_OFF = 8192, K_OFF = W_OFF + 24576, V_OFF = K_OFF + 65536, LDS_BYTES = V_OFF + 65536;
static_assert(M_END <= W_OFF && LDS_BYTES <= 160 * 1024, "LDS");
constexpr unsigned W1_OFF = W_OFF, W2_OFF = W_OFF + 36864;

struct Params { const u16 *x, *k, *v, *wq, *wo, *w1, *b1, *w2, *b2; u16* out; char* ws; };

__device__ __forceinline__ float asf(unsigned u) { return __builtin_bit_cast(float, u); }
__device__ __forceinline__ unsigned asu(float f) { return __builtin_bit_cast(unsigned, f); }
__device__ __forceinline__ float lo16(unsigned u) { return asf(u << 16); }
__device__ __forceinline__ float hi16(unsigned u) { return asf(u & 0xffff0000u); }
__device__ __forceinline__ unsigned pk(float lo, float hi) { bf16x2 r; r.x = (__bf16)lo; r.y = (__bf16)hi; return __builtin_bit_cast(unsigned, r); }
__device__ __forceinline__ float rb(float f) { return lo16(pk(f, f)); }
__device__ __forceinline__ float dot2(unsigned a, unsigned b, float c) {
  return __builtin_amdgcn_fdot2_f32_bf16(__builtin_bit_cast(bf16x2, a), __builtin_bit_cast(bf16x2, b), c, false);
}
__device__ __forceinline__ v4f mfma(v4u a, v4u b, v4f c) {
  return __builtin_amdgcn_mfma_f32_16x16x32_bf16(__builtin_bit_cast(bf16x8, a), __builtin_bit_cast(bf16x8, b), c, 0, 0, 0);
}
template <int CTRL> __device__ __forceinline__ float dpp(float x) { return asf(__builtin_amdgcn_mov_dpp(asu(x), CTRL, 0xf, 0xf, true)); }
template <int CTRL> __device__ __forceinline__ unsigned dppu(unsigned x) { return __builtin_amdgcn_mov_dpp(x, CTRL, 0xf, 0xf, true); }
constexpr int XOR1 = 0xB1, XOR2 = 0x4E, XOR7 = 0x141, XOR8 = 0x128, XOR3 = 0x1B, NEWB = 0x150;
__device__ __forceinline__ float vmax(float a, float b) { float r; asm("v_max_f32 %0, %1, %2" : "=v"(r) : "v"(a), "v"(b)); return r; }
__device__ __forceinline__ float x16_sum(float x) { auto s = __builtin_amdgcn_permlane16_swap(asu(x), asu(x), false, false); return asf(s[0]) + asf(s[1]); }
__device__ __forceinline__ float x32_sum(float x) { auto s = __builtin_amdgcn_permlane32_swap(asu(x), asu(x), false, false); return asf(s[0]) + asf(s[1]); }
__device__ __forceinline__ float x16_max(float x) { auto s = __builtin_amdgcn_permlane16_swap(asu(x), asu(x), false, false); return fmaxf(asf(s[0]), asf(s[1])); }
__device__ __forceinline__ float x32_max(float x) { auto s = __builtin_amdgcn_permlane32_swap(asu(x), asu(x), false, false); return fmaxf(asf(s[0]), asf(s[1])); }
__device__ __forceinline__ float t32(float a, float b) { auto s = __builtin_amdgcn_permlane32_swap(asu(a), asu(b), false, false); return asf(s[0]) + asf(s[1]); }
__device__ __forceinline__ float t16(float a, float b) { auto s = __builtin_amdgcn_permlane16_swap(asu(a), asu(b), false, false); return asf(s[0]) + asf(s[1]); }
template <int CTRL> __device__ __forceinline__ float tdpp(float a, float b, bool hi) { const float s = hi ? a : b, k = hi ? b : a; return k + dpp<CTRL>(s); }
__device__ __forceinline__ float row_sum(float x) { x += dpp<XOR1>(x); x += dpp<XOR2>(x); x += dpp<XOR7>(x); x += dpp<XOR8>(x); return x; }
__device__ __forceinline__ float row_max(float x) { x = fmaxf(x, dpp<XOR1>(x)); x = fmaxf(x, dpp<XOR2>(x)); x = fmaxf(x, dpp<XOR7>(x)); x = fmaxf(x, dpp<XOR8>(x)); return x; }
__device__ __forceinline__ float wave_sum(float x) { return x32_sum(x16_sum(row_sum(x))); }
__device__ __forceinline__ float wave_max(float x) { return x32_max(x16_max(row_max(x))); }
__device__ __forceinline__ v4u ldnt4(const void* p) { return __builtin_nontemporal_load((const GAS v4u*)p); }
__device__ __forceinline__ unsigned clean(float f) { const unsigned u = asu(f); return u < SENT ? u : SENT - 1u; }
__device__ __forceinline__ bool has_sent(v4u v) { return max(max(v.x, v.y), max(v.z, v.w)) == SENT; }
#define DRAIN() asm volatile("s_waitcnt vmcnt(0)" ::: "memory")
#define CBAR() asm volatile("" ::: "memory")
#define LGKM0() asm volatile("s_waitcnt lgkmcnt(0)" ::: "memory")
#define SB() __builtin_amdgcn_sched_barrier(0)
__device__ __forceinline__ void bar() { LGKM0(); __builtin_amdgcn_s_barrier(); CBAR(); }
template <typename T> __device__ __forceinline__ T lds_ld(unsigned off) { return *(const LAS T*)(size_t)off; }
template <typename T> __device__ __forceinline__ void lds_st(unsigned off, T v) { *(LAS T*)(size_t)off = v; }
__device__ __forceinline__ v4u lds_vld4(unsigned off) { return *(const volatile LAS v4u*)(size_t)off; }
__device__ __forceinline__ unsigned lds_vld(unsigned off) { return *(const volatile LAS unsigned*)(size_t)off; }
__device__ __forceinline__ void lds_vst(unsigned off, unsigned v) { *(volatile LAS unsigned*)(size_t)off = v; }
__device__ __forceinline__ v4u tr8(unsigned a0, unsigned a1) {
  const v2u x = __builtin_bit_cast(v2u, __builtin_amdgcn_ds_read_tr16_b64_v4i16((LAS v4s*)(size_t)a0));
  const v2u y = __builtin_bit_cast(v2u, __builtin_amdgcn_ds_read_tr16_b64_v4i16((LAS v4s*)(size_t)a1));
  return v4u{x.x, x.y, y.x, y.y};
}

#define PIN(x) asm volatile("" : "+v"(x))
__device__ __forceinline__ unsigned oob(bool keep, unsigned off) { PIN(off); return keep ? off : WS_END; }
template <int SLEEP> __device__ __forceinline__ void wait_cnt(unsigned off, unsigned n) {
#pragma nounroll
  for (unsigned s = 0; s < LSPIN_CAP; ++s) { if (__builtin_amdgcn_readfirstlane(lds_vld(off)) >= n) break; if (SLEEP) __builtin_amdgcn_s_sleep(SLEEP); }
  CBAR();
}
template <int SLEEP> __device__ __forceinline__ void wait_vals8(unsigned off, v4u& a, v4u& b) {
#pragma nounroll
  for (unsigned s = 0; s < LSPIN_CAP; ++s) {
    a = lds_vld4(off); b = lds_vld4(off + 16);
    if (__builtin_amdgcn_readfirstlane(max(max(max(a.x, a.y), max(a.z, a.w)), max(max(b.x, b.y), max(b.z, b.w)))) != SENT) break;
    if (SLEEP) __builtin_amdgcn_s_sleep(SLEEP);
  }
  CBAR();
}
#define lds_inc(off) asm volatile("ds_add_u32 %0, %1 offset:%2" :: "v"(0u), "v"(1u), "i"(off) : "memory")
#define LOOP_END(n) "s_sub_u32 " n ", " n ", 1\n\ts_cmp_lg_u32 " n ", 0\n\ts_cbranch_scc1 1b"
template <int N, int DEP> __device__ __forceinline__ void dma1(unsigned& voff, unsigned lds, const void* base, unsigned gstep) {
  static_assert(N % 2 == 0, "");
  unsigned n = N / 2;
  asm volatile("s_mov_b32 m0, %2\n\t1:\n\t.rept 2\n\ts_waitcnt vmcnt(%5)\n\tglobal_load_lds_dwordx4 %0, %3 nt\n\ts_add_u32 m0, m0, 0x400\n\tv_add_u32 %0, %4, %0\n\t.endr\n\t" LOOP_END("%1")
               : "+v"(voff), "+s"(n) : "s"(lds), "s"(base), "s"(gstep), "i"(DEP - 1) : "memory", "scc");
}
template <int NG, int DEP> __device__ __forceinline__ void dma4(unsigned (&v)[4], unsigned lds, const void* base) {
  unsigned n = NG;
  if (NG > 0)
    asm volatile("s_mov_b32 m0, %5\n\t1:\n\t"
                 "s_waitcnt vmcnt(%7)\n\tglobal_load_lds_dwordx4 %0, %6 nt\n\tv_add_u32 %0, 0x1000, %0\n\t"
                 "s_waitcnt vmcnt(%7)\n\tglobal_load_lds_dwordx4 %1, %6 offset:1024 nt\n\tv_add_u32 %1, 0x1000, %1\n\t"
                 "s_waitcnt vmcnt(%7)\n\tglobal_load_lds_dwordx4 %2, %6 offset:2048 nt\n\tv_add_u32 %2, 0x1000, %2\n\t"
                 "s_waitcnt vmcnt(%7)\n\tglobal_load_lds_dwordx4 %3, %6 offset:3072 nt\n\tv_add_u32 %3, 0x1000, %3\n\t"
                 "s_add_u32 m0, m0, 0x1000\n\t" LOOP_END("%4")
                 : "+v"(v[0]), "+v"(v[1]), "+v"(v[2]), "+v"(v[3]), "+s"(n) : "s"(lds), "s"(base), "i"(DEP - 1) : "memory", "scc");
}
template <int NG, int DEP> __device__ __forceinline__ void dma3(unsigned (&v)[3], unsigned lds, const void* base, unsigned gstep) {
  unsigned n = NG;
  asm volatile("s_mov_b32 m0, %4\n\t1:\n\t"
               "s_waitcnt vmcnt(%7)\n\tglobal_load_lds_dwordx4 %0, %5 nt\n\ts_add_u32 m0, m0, 0x400\n\tv_add_u32 %0, %6, %0\n\t"
               "s_waitcnt vmcnt(%7)\n\tglobal_load_lds_dwordx4 %1, %5 nt\n\ts_add_u32 m0, m0, 0x400\n\tv_add_u32 %1, %6, %1\n\t"
               "s_waitcnt vmcnt(%7)\n\tglobal_load_lds_dwordx4 %2, %5 nt\n\ts_add_u32 m0, m0, 0x400\n\tv_add_u32 %2, %6, %2\n\t" LOOP_END("%3")
               : "+v"(v[0]), "+v"(v[1]), "+v"(v[2]), "+s"(n) : "s"(lds), "s"(base), "s"(gstep), "i"(DEP - 1) : "memory", "scc");
}
template <int N, int A2, int STEP> __device__ __forceinline__ void ramp1(unsigned& voff, unsigned lds, const void* base, unsigned gstep) {
  asm volatile("s_mov_b32 m0, %1\n\t.set rk_, %4\n\t.rept %6\n\ts_waitcnt vmcnt(rk_ / 2)\n\tglobal_load_lds_dwordx4 %0, %2 nt\n\ts_add_u32 m0, m0, 0x400\n\tv_add_u32 %0, %3, %0\n\t"
               ".set rk_, rk_ + (%5)\n\t.endr" : "+v"(voff) : "s"(lds), "s"(base), "s"(gstep), "i"(A2), "i"(STEP), "i"(N) : "memory", "scc");
}
template <int NG, int A> __device__ __forceinline__ void ramp3(unsigned (&v)[3], unsigned lds, const void* base, unsigned gstep) {
  asm volatile("s_mov_b32 m0, %3\n\t.set rk_, %6\n\t.rept %7\n\ts_waitcnt vmcnt(rk_)\n\t"
               "global_load_lds_dwordx4 %0, %4 nt\n\ts_add_u32 m0, m0, 0x400\n\tv_add_u32 %0, %5, %0\n\t"
               "global_load_lds_dwordx4 %1, %4 nt\n\ts_add_u32 m0, m0, 0x400\n\tv_add_u32 %1, %5, %1\n\t"
               "global_load_lds_dwordx4 %2, %4 nt\n\ts_add_u32 m0, m0, 0x400\n\tv_add_u32 %2, %5, %2\n\t.set rk_, rk_ - 1\n\t.endr"
               : "+v"(v[0]), "+v"(v[1]), "+v"(v[2]) : "s"(lds), "s"(base), "s"(gstep), "i"(A), "i"(NG) : "memory", "scc");
}
#define VMW(n) asm volatile("s_waitcnt vmcnt(%0)" :: "i"(n) : "memory")

#define ST(i) do { if (PROBE == (i) && wave == PROBEW) { SB(); t_ev = __builtin_amdgcn_s_memrealtime(); SB(); } } while (0)
#define LDW(off, soff) __builtin_amdgcn_raw_buffer_load_b128(ws, off, soff, AUX_SC1)
#define STW(v, off) __builtin_amdgcn_raw_buffer_store_b128(v, ws, off, 0, AUX_SC1)
__device__ __forceinline__ v4u tag4(v4f v, unsigned T0, unsigned T1) {
  return v4u{(asu(v[0]) & 0xfffff000u) | T0, asu(v[1]), (asu(v[2]) & 0xfffff000u) | T1, asu(v[3])};
}
__device__ __forceinline__ v4f untag4(v4u v, unsigned T0, unsigned T1, unsigned& bad) {
  const unsigned a = v.x ^ T0, c = v.z ^ T1; bad |= a | c;
  return v4f{asf(a), asf(v.y), asf(c), asf(v.w)};
}
#define ALL_OK(bad) (!__any(((bad) & 0xfffu) != 0u))
#define GET_TAGS() const v2u tg_ = lds_ld<v2u>(TG_OFF); const unsigned T0 = __builtin_amdgcn_readfirstlane(tg_.x), T1 = __builtin_amdgcn_readfirstlane(tg_.y)
#define F4(v) __builtin_bit_cast(v4f, v)
#define U4(v) __builtin_bit_cast(v4u, v)

__global__ void __launch_bounds__(NTHR, 3) fused(Params p) {
  __shared__ __attribute__((aligned(16))) char lds_raw[LDS_BYTES];
  u64 pc0; asm volatile("s_getpc_b64 %0" : "=s"(pc0));
  const int b = blockIdx.x, tid = threadIdx.x, wave = __builtin_amdgcn_readfirstlane(tid >> 6), lane = tid & 63;
  const int g = (b & 7) * 2 + ((b >> 3) & 1), j = b >> 4;
  const bool is_wo = j < 6, is_mlp = j >= 6 && j < 15;
  const int u = g * 9 + (j - 6);
  const int red = ((j + 1) & 15) * 16 + g;
  const __amdgpu_buffer_rsrc_t ws = __builtin_amdgcn_make_buffer_rsrc(p.ws, 0, WS_END, 0x00020000);
  const int r = lane >> 4, c = lane & 15;
  const int q4 = c >> 2, p4 = c & 3;
  const v4f zero = {0.f, 0.f, 0.f, 0.f};
  asm volatile("" :: "v"((LAS char*)lds_raw));
  u64 t_ev = 0;
  ST(0);
  const int qt = j >> 2, qc = j & 3;
  if (wave == 0) {
    const unsigned vx = lane * 16, vy = min(lane * 16u, 112u);
    asm volatile("s_mov_b32 m0, %2\n\ts_nop 0\n\tglobal_load_lds_dwordx4 %0, %3\n\tglobal_load_lds_dwordx4 %1, %3 offset:1024"
                 :: "v"(vx), "v"(vy), "s"(W_OFF + 18 * 1024), "s"(p.x) : "memory");
    unsigned vq = (144 * qt + (lane >> 3)) * (QW * 2) + (256 * g + 64 * qc) * 2 + (lane & 7) * 16;
    dma1<18, 63>(vq, W_OFF, p.wq, 8u * QW * 2);
    const size_t hb = ((size_t)g * SK + (SK - WIN) + 256 * j) * DH;
    const u16 *kb = p.k + hb, *vb = p.v + hb;
    unsigned vk[4], vv[4];
#pragma unroll
    for (int n = 0; n < 4; ++n) { vk[n] = r * 256 + ((c ^ (4 * n + r)) << 4); vv[n] = r * 256 + ((c ^ ((r << 2) | n)) << 4); }
    static_assert(DB % 4 == 0 && DB >= 20 && DB <= 32 && DC % 4 == 0 && DC >= DB && DC <= 32, "stage positions");
    ST(10);
    bar();
    dma4<1, 20>(vk, K_OFF, kb);
    lds_vst(F_L, 1u);
    dma4<4, 20>(vk, K_OFF + 4096, kb);
    VMW(20); lds_vst(F_L, 2u); ST(12);
    dma4<(32 + DB - 20) / 4, DB>(vk, K_OFF + 20 * 1024, kb);
    VMW(DB); lds_vst(F_L, 3u);
    dma4<(32 - DB) / 4, DB>(vk, K_OFF + (32 + DB) * 1024, kb);
    dma4<DC / 4, DC>(vv, V_OFF, vb);
    VMW(DC); lds_vst(F_L, 4u); ST(1);
    dma4<4, DC>(vv, V_OFF + DC * 1024, vb);
    VMW(DC); lds_vst(F_L, 5u);
    dma4<(48 - DC) / 4, DC>(vv, V_OFF + (DC + 16) * 1024, vb);
    ST(2);
    VMW(32); lds_vst(F_L, 6u); ST(3);
    static_assert(DC == 32 || DC == 28 || DC == 24, "ramps");
    if (is_mlp) {
      unsigned v1 = (lane >> 2) * (FF * 2) + 64 * u + (lane & 3) * 16, v2 = 32 * u * (D * 2) + lane * 16;
      wait_cnt<0>(F_PUB, 4);
      constexpr int K7 = (DC - 16) / 2, K8 = (DC - 8) / 2, K9 = DC / 2;
      ramp1<K7 - 1, 2 * (DC - 2), -2>(v1, W1_OFF, p.w1, 16u * FF * 2);
      VMW(16 + K7 - 1); lds_vst(F_L, 7u);
      ramp1<K8 - K7, 2 * (DC - 1 - K7), -2>(v1, W1_OFF + (K7 - 1) * 1024, p.w1, 16u * FF * 2);
      VMW(8 + K8 - 1); lds_vst(F_L, 8u);
      ramp1<K9 - K8, 2 * (DC - 1 - K8), -2>(v1, W1_OFF + (K8 - 1) * 1024, p.w1, 16u * FF * 2);
      VMW(K9 - 1); lds_vst(F_L, 9u); ST(4);
      wait_cnt<0>(F_KD, 4);
      ramp1<1, 2 * (DC / 2 - 1), 0>(v1, W1_OFF + (K9 - 1) * 1024, p.w1, 16u * FF * 2);
      dma1<36 - K9, DC / 2>(v1, W1_OFF + K9 * 1024, p.w1, 16u * FF * 2);
      ramp1<DC - 2, DC - 1, 1>(v2, W2_OFF, p.w2, 1024u);
      VMW(DC - 2); lds_vst(F_W, 1u); ST(13);
      dma1<38 - DC, DC>(v2, W2_OFF + (DC - 2) * 1024, p.w2, 1024u);
      VMW(HEARLY); lds_vst(F_W, 2u);
      VMW(0); lds_vst(F_W, 3u); ST(14);
    } else if (is_wo) {
      unsigned vo[3];
#pragma unroll
      for (int i = 0; i < 3; ++i) { const int pi = i * 64 + lane; vo[i] = (256 * g + pi / 12) * (D * 2) + 192 * j + (pi % 12) * 16; }
      wait_cnt<0>(F_PUB, 4);
      constexpr int K7 = (DC - 16) / 4, K8 = (DC - 8) / 4, K9 = DC / 4;
      ramp3<K7 - 1, DC - 4>(vo, W_OFF, p.wo, 16u * D * 2);
      VMW(16 + 3 * (K7 - 1)); lds_vst(F_L, 7u);
      ramp3<K8 - K7, DC - 3 - K7>(vo, W_OFF + (K7 - 1) * 3072, p.wo, 16u * D * 2);
      VMW(8 + 3 * (K8 - 1)); lds_vst(F_L, 8u);
      ramp3<K9 - K8, DC - 3 - K8>(vo, W_OFF + (K8 - 1) * 3072, p.wo, 16u * D * 2);
      VMW(3 * (K9 - 1)); lds_vst(F_L, 9u); ST(4);
      wait_cnt<0>(F_KD, 4);
      dma3<17 - K9, 3 * K9>(vo, W_OFF + (K9 - 1) * 3072, p.wo, 16u * D * 2);
      VMW(WOEARLY); lds_vst(F_W, 2u); ST(13);
      VMW(0); lds_vst(F_W, 3u); ST(14);
    } else {
      VMW(16); lds_vst(F_L, 7u);
      VMW(8); lds_vst(F_L, 8u);
      VMW(0); lds_vst(F_L, 9u); ST(4);
    }
  } else {
  if (wave == 1) lds_st<unsigned>((lane & 31) * 4, (lane & 16) ? SENT : 0u);
  if (L2WARM && j == 15 && wave >= 9) {
    v4u t; const unsigned co = (wave - 9) * 4096 + lane * 16;
    asm volatile("global_load_dwordx4 %0, %1, %2\n\tglobal_load_dwordx4 %0, %1, %2 offset:1024\n\tglobal_load_dwordx4 %0, %1, %2 offset:2048\n\t"
                 "global_load_dwordx4 %0, %1, %2 offset:3072\n\tglobal_load_dwordx4 %0, %3, %2\n\ts_barrier\n\ts_waitcnt vmcnt(0)"
                 : "=&v"(t) : "v"(co), "s"(pc0), "v"(min(12288u + (wave - 9) * 1024 + lane * 16, CODE_BYTES - 16u)) : "memory");
  } else {
    ST(10);
    bar();
  }
  }
  if (wave == 0) {
  } else if (wave == 1) {
    wait_cnt<1>(F_L, 1);
    const v4u xa = lds_ld<v4u>(W_OFF + 18 * 1024 + lane * 16);
    const unsigned xb = lds_ld<u16>(W_OFF + 19 * 1024 + lane * 2);
    float xv[9];
#pragma unroll
    for (int k = 0; k < 4; ++k) { xv[2 * k] = lo16(xa[k]); xv[2 * k + 1] = hi16(xa[k]); }
    xv[8] = lo16(xb);
    lds_st<v4f>(X_OFF + lane * 32, v4f{xv[0], xv[1], xv[2], xv[3]}); lds_st<v4f>(X_OFF + lane * 32 + 16, v4f{xv[4], xv[5], xv[6], xv[7]});
    lds_st<float>(X_OFF + 2048 + lane * 4, xv[8]);
    {
      const float c0 = asf(__builtin_amdgcn_readfirstlane(asu(xv[0])));
      float s = 0.f, q = 0.f;
#pragma unroll
      for (int i = 0; i < 9; ++i) { const float d = xv[i] - c0; s += d; q = fmaf(d, d, q); }
      s = wave_sum(s); q = wave_sum(q);
      const float md = s * (1.f / D), var = fmaxf(q * (1.f / D) - md * md, 0.f);
      const float mb = rb(c0 + md), rden = 1.f / rb(__builtin_sqrtf(rb(rb(var) + 1e-5f)));
#pragma unroll
      for (int i = 0; i < 9; ++i) xv[i] = rb(xv[i] - mb) * rden;
      lds_st<v4u>(Z_OFF + lane * 16, v4u{pk(xv[0], xv[1]), pk(xv[2], xv[3]), pk(xv[4], xv[5]), pk(xv[6], xv[7])});
      lds_st<u16>(Z_OFF + 1024 + lane * 2, (u16)pk(xv[8], xv[8]));
    }
    lds_vst(F_Y, 1u);
    ST(13);
    wait_cnt<1>(F_PUB, 4);
    {
      GET_TAGS();
      const v4u tq = {T0 & 63u, T0 >> 6, T1 & 63u, T1 >> 6};
      unsigned qa = QP_OFF + g * 4096 + lane * 16;
      PIN(qa);
      v4u ga[4], gb[4];
#define QLD(G_) { CBAR(); _Pragma("unroll") for (int t = 0; t < 4; ++t) G_[t] = LDW(qa, t * 1024); SB(); }
#define QCK(G_, O_) { unsigned bad = 0; _Pragma("unroll") for (int t = 0; t < 4; ++t) { G_[t] ^= tq; bad |= G_[t].x | G_[t].y | G_[t].z | G_[t].w; } \
      const bool ok = !__any((bad & 63u) != 0u) || spin + 1 == cap; \
      const v4f qs = (F4(G_[0]) + F4(G_[1])) + (F4(G_[2]) + F4(G_[3])); \
      lds_st<v2u>(Q_OFF + lane * 8, v2u{pk(qs[0], qs[1]), pk(qs[2], qs[3])}); lds_vst(F_Q, ok ? 1u : 0u); ST(2); SB(); \
      if (ok) break; }
      const unsigned cap = TRUNC >= 3 ? 1 : SPIN_CAP;
      __builtin_amdgcn_s_sleep(QDELAY);
      QLD(ga)
      __builtin_amdgcn_s_sleep(QSTAG);
#pragma nounroll
      for (unsigned spin = 0;; ++spin) {
        QLD(gb) QCK(ga, gb) QLD(ga) QCK(gb, ga)
      }
#undef QLD
#undef QCK
    }
  } else if (wave >= 4 && wave <= 7) {
    const int w = wave - 4;
    if (w == 0) {
      const unsigned ep = *(const __attribute__((address_space(4))) unsigned*)(p.ws + EP_OFF);
      lds_st<v2u>(TG_OFF, v2u{(ep + 1u) & 0xfffu, ((ep >> 16) + 1u) & 0xfffu});
    }
    unsigned ka[4], va[2][2];
#pragma unroll
    for (int s = 0; s < 4; ++s) { ka[s] = K_OFF + (16 * w + c) * 256 + (((4 * s + r) ^ c) << 4); PIN(ka[s]); }
#pragma unroll
    for (int tt = 0; tt < 2; ++tt)
#pragma unroll
      for (int t = 0; t < 2; ++t) { va[tt][t] = V_OFF + 256 * (8 * r + 4 * t + q4) + 16 * ((2 * (2 * w + tt) + (p4 >> 1)) ^ ((q4 << 2) | ((2 * r + t) & 3))) + 8 * (p4 & 1); PIN(va[tt][t]); }
    unsigned opo = oob(c < 4, OP_OFF + (g * 16 + j) * 1024 + ((c & 1) * 128 + 32 * w + 8 * (c & 2) + 4 * r) * 4);
    PIN(opo);
    {
      unsigned wa = W_OFF + (8 * r + q4) * 128 + p4 * 8 + w * 32, wb = W_OFF + (128 + 4 * r + q4) * 128 + p4 * 8 + w * 32;
      unsigned qo = oob(c == 0 && TRUNC < 3, QP_OFF + ((g * 4 + qt) * 256 + 64 * qc + 16 * w + 4 * r) * 4);
      PIN(wa); PIN(wb); PIN(qo);
#pragma nounroll
      for (unsigned s = 0; s < LSPIN_CAP; ++s) { if (__builtin_amdgcn_readfirstlane(lds_vld(TG_OFF)) != SENT) break; __builtin_amdgcn_s_sleep(1); }
      CBAR();
      GET_TAGS();
      wait_cnt<1>(F_Y, 1);
      v4u yf[4];
#pragma unroll
      for (int s = 0; s < 4; ++s) yf[s] = lds_ld<v4u>(Z_OFF + 288 * qt + 16 * r + 64 * s);
      const v2u yl = lds_ld<v2u>(Z_OFF + 288 * qt + 256 + 8 * r);
      wait_cnt<0>(F_L, 2);
      ST(13);
      v4u wf[4];
#pragma unroll
      for (int s = 0; s < 4; ++s) wf[s] = tr8(wa + s * 4096, wa + s * 4096 + 512);
      const v2u wl = __builtin_bit_cast(v2u, __builtin_amdgcn_ds_read_tr16_b64_v4i16((LAS v4s*)(size_t)wb));
      SB();
      const v4f qa = mfma(wf[3], yf[3], mfma(wf[2], yf[2], mfma(wf[1], yf[1], mfma(wf[0], yf[0], zero)))) +
                     __builtin_amdgcn_mfma_f32_16x16x16bf16_1k(__builtin_bit_cast(v4s, wl), __builtin_bit_cast(v4s, yl), zero, 0, 0, 0);
      const v4u tq = {T0 & 63u, T0 >> 6, T1 & 63u, T1 >> 6};
      STW(((U4(qa) + 32u) & ~63u) | tq, qo);
      if (lane == 0) lds_inc(F_PUB);
      ST(14);
    }
    GET_TAGS();
    v4u kf[2][4], qf[4]; v4f dt[4];
    wait_cnt<2>(F_L, 3);
#pragma unroll
    for (int t = 0; t < 2; ++t)
#pragma unroll
      for (int s = 0; s < 4; ++s) kf[t][s] = lds_ld<v4u>(ka[s] + t * 16384);
    SB();
    wait_cnt<1>(F_Q, 1);
    ST(11);
#pragma unroll
    for (int s = 0; s < 4; ++s) qf[s] = lds_ld<v4u>(Q_OFF + (c & 1) * 256 + r * 16 + s * 64);
    SB();
#pragma unroll
    for (int t = 0; t < 2; ++t) dt[t] = mfma(qf[3], kf[t][3], mfma(qf[2], kf[t][2], mfma(qf[1], kf[t][1], mfma(qf[0], kf[t][0], zero))));
    SB();
    wait_cnt<0>(F_L, 4);
    ST(1);
#pragma unroll
    for (int t = 0; t < 2; ++t)
#pragma unroll
      for (int s = 0; s < 4; ++s) kf[t][s] = lds_ld<v4u>(ka[s] + (2 + t) * 16384);
    SB();
    if (lane == 0) lds_inc(F_KD);
#pragma unroll
    for (int t = 0; t < 2; ++t) dt[2 + t] = mfma(qf[3], kf[t][3], mfma(qf[2], kf[t][2], mfma(qf[1], kf[t][1], mfma(qf[0], kf[t][0], zero))));
    const bool r1 = r & 1, r2 = r & 2;
    float s0 = r2 ? (r1 ? dt[3][0] : dt[2][0]) : (r1 ? dt[1][0] : dt[0][0]);
    float s1 = r2 ? (r1 ? dt[3][1] : dt[2][1]) : (r1 ? dt[1][1] : dt[0][1]);
    s0 = rb(rb(s0) * 0.08838834764831845f); s1 = rb(rb(s1) * 0.08838834764831845f);
    {
      auto sw = __builtin_amdgcn_permlane32_swap(asu(s0), asu(s1), false, false);
      float m = fmaxf(asf(sw[0]), asf(sw[1]));
      auto s2 = __builtin_amdgcn_permlane16_swap(asu(m), asu(m), false, false);
      m = vmax(asf(s2[0]), asf(s2[1]));
      asm volatile("s_nop 1\n\tv_max_f32_dpp %0, %0, %0 quad_perm:[1,0,3,2] row_mask:0xf bank_mask:0xf\n\t"
                   "s_nop 1\n\tv_max_f32_dpp %0, %0, %0 quad_perm:[2,3,0,1] row_mask:0xf bank_mask:0xf\n\t"
                   "s_nop 1\n\tv_max_f32_dpp %0, %0, %0 row_half_mirror row_mask:0xf bank_mask:0xf\n\t"
                   "s_nop 1\n\tv_max_f32_dpp %0, %0, %0 row_ror:8 row_mask:0xf bank_mask:0xf" : "+v"(m));
      lds_st<unsigned>(MX_OFF + (lane >> 5) * 16 + w * 4, clean(m));
    }
    v4u ea, eb;
    wait_vals8<0>(MX_OFF, ea, eb);
    const float M0 = vmax(vmax(asf(ea.x), asf(ea.y)), vmax(asf(ea.z), asf(ea.w))), M1 = vmax(vmax(asf(eb.x), asf(eb.y)), vmax(asf(eb.z), asf(eb.w)));
    const unsigned pp = pk(__builtin_amdgcn_exp2f((s0 - M0) * 1.4426950408889634f), __builtin_amdgcn_exp2f((s1 - M1) * 1.4426950408889634f));
    lds_st<u16>(P_OFF + (64 * r + 16 * w + c) * 2, (u16)pp); lds_st<u16>(P_OFF + 512 + (64 * r + 16 * w + c) * 2, (u16)(pp >> 16));
    if (lane == 0) lds_inc(F_P);
    wait_cnt<0>(F_P, 4);
    v4u pf[8];
#pragma unroll
    for (int s = 0; s < 8; ++s) pf[s] = lds_ld<v4u>(P_OFF + (c & 1) * 512 + r * 16 + s * 64);
    if (w == 0) {
      const v4u ones = {0x3f803f80u, 0x3f803f80u, 0x3f803f80u, 0x3f803f80u};
      v4f ls = zero;
#pragma unroll
      for (int s = 0; s < 8; ++s) ls = mfma(pf[s], ones, ls);
      STW(tag4(v4f{M0, ls[0], M1, ls[1]}, T0, T1), oob(lane < 8 && TRUNC < 2, ST_OFF + (g * 16 + j) * 128 + lane * 16));
    }
    ST(2);
    v4f oa = zero, ob = zero;
#define PV(s0_, n_) { v4u fa[n_], fb[n_]; \
      _Pragma("unroll") for (int s = 0; s < n_; ++s) { fa[s] = tr8(va[0][0] + (s0_ + s) * 8192, va[0][1] + (s0_ + s) * 8192); fb[s] = tr8(va[1][0] + (s0_ + s) * 8192, va[1][1] + (s0_ + s) * 8192); } \
      SB(); _Pragma("unroll") for (int s = 0; s < n_; ++s) { oa = mfma(fa[s], pf[s0_ + s], oa); ob = mfma(fb[s], pf[s0_ + s], ob); } SB(); }
    wait_cnt<0>(F_L, 6); PV(0, 4)
    wait_cnt<0>(F_L, 7); PV(4, 2)
    wait_cnt<0>(F_L, 8); PV(6, 1)
    {
      v4u fa, fb;
#pragma nounroll
      for (unsigned sp = 0; sp < LSPIN_CAP; ++sp) {
        const unsigned f = lds_vld(F_L);
        CBAR();
        fa = tr8(va[0][0] + 7 * 8192, va[0][1] + 7 * 8192); fb = tr8(va[1][0] + 7 * 8192, va[1][1] + 7 * 8192);
        SB();
        if (__builtin_amdgcn_readfirstlane(f) >= 9u) break;
      }
      oa = mfma(fa, pf[7], oa); ob = mfma(fb, pf[7], ob);
    }
#undef PV
    STW(tag4((c & 2) ? ob : oa, T0, T1), TRUNC >= 2 ? opo | 0x40000000u : opo);
    ST(3);
  }

  if (TRUNC) {} else
  if (is_wo) {
    if (wave >= 4 && wave <= 7) {
      const int w = wave - 4;
      unsigned oo = OP_OFF + (g * 16 + 4 * r) * 1024 + (64 * w + 4 * c) * 4, ol = O_OFF + (64 * w + 4 * c + r) * 2;
      unsigned h2o = oob(lane < 8 && w == 0, H2_OFF + (g * 6 + j) * 128 + lane * 16);
      PIN(oo); PIN(ol); PIN(h2o);
      GET_TAGS();
      const unsigned so = ST_OFF + (g * 16 + ((c + 4 * r) & 15)) * 128 + (w >> 1) * 8, Th = (w >> 1) ? T1 : T0;
      v2u st = __builtin_amdgcn_raw_buffer_load_b64(ws, so, 0, AUX_SC1);
      SB();
      wait_cnt<0>(F_W, 2);
      ST(12);
      v4u t[4];
#pragma unroll
      for (int i = 0; i < 4; ++i) t[i] = LDW(oo, i * 1024);
      SB();
#pragma nounroll
      for (unsigned spin = 0; spin < SPIN_CAP; ++spin) {
        st.x ^= Th;
        if (ALL_OK(st.x)) break;
        CBAR();
        st = __builtin_amdgcn_raw_buffer_load_b64(ws, so, 0, AUX_SC1);
      }
      float wgt = __builtin_amdgcn_exp2f((asf(st.x) - row_max(asf(st.x))) * 1.4426950408889634f);
      wgt *= __builtin_amdgcn_rcpf(row_sum(wgt * asf(st.y)));
      float wg0 = dpp<NEWB + 0>(wgt), wg1 = dpp<NEWB + 1>(wgt), wg2 = dpp<NEWB + 2>(wgt), wg3 = dpp<NEWB + 3>(wgt);
      PIN(wg0); PIN(wg1); PIN(wg2); PIN(wg3);
      SB();
      v4f a;
#pragma nounroll
      for (unsigned spin = 0; spin < SPIN_CAP; ++spin) {
        unsigned bad = 0;
        a = (untag4(t[0], T0, T1, bad) * wg0 + untag4(t[1], T0, T1, bad) * wg1) + (untag4(t[2], T0, T1, bad) * wg2 + untag4(t[3], T0, T1, bad) * wg3);
        if (ALL_OK(bad)) break;
        CBAR();
#pragma unroll
        for (int i = 0; i < 4; ++i) t[i] = LDW(oo, i * 1024);
      }
      STW((v4u{T0, 0u, T1, 0u}), h2o);
      ST(4);
      const float o = t16(t32(a[0], a[2]), t32(a[1], a[3]));
      lds_st<u16>(ol, (u16)pk(o, o));
      ST(5);
      bar();
    } else if (wave >= 8 || wave == 2 || wave == 3) {
      const int t = wave >= 8 ? wave - 8 : wave + 2;
      unsigned ho = oob(c == 0, HP_OFF + (g * 576 + j * 96 + 16 * t + 4 * r) * 4), oa = O_OFF + r * 16;
      PIN(ho); PIN(oa);
      wait_cnt<8>(F_W, 2);
      wait_cnt<0>(F_W, 3);
      GET_TAGS();
      v4u wf[8];
#pragma unroll
      for (int s = 0; s < 8; ++s) { const unsigned a = W_OFF + (32 * s + 8 * r + q4) * 192 + t * 32 + p4 * 8; wf[s] = tr8(a, a + 4 * 192); }
#pragma unroll
      for (int s = 0; s < 8; ++s) PIN(wf[s]);
      ST(5);
      bar();
      v4u of[8];
#pragma unroll
      for (int s = 0; s < 8; ++s) of[s] = lds_ld<v4u>(oa + s * 64);
      SB();
      v4f acc = zero;
#pragma unroll
      for (int s = 0; s < 8; ++s) acc = mfma(wf[s], of[s], acc);
      STW(tag4(acc, T0, T1), ho);
      ST(6);
    } else {
      bar();
    }
  } else if (is_mlp) {
    v4u w2f[9]; float b1v = 0.f;
    unsigned za = Z_OFF + (c >> 3) * 128 + (8 * r + (c & 7)) * 4, mo = oob(p4 < 2 || c == 2, MP_OFF + u * 2304 + (wave - 8) * 576 + (4 * p4 + q4) * 64 + r * 16);
    PIN(za); PIN(mo);
    if (wave >= 8) { b1v = lo16(((const GAS u16*)p.b1)[32 * u + 8 * r + (c & 7)]); PIN(b1v); }
    wait_cnt<8>(F_L, 9);
    wait_cnt<2>(F_W, 1);
    GET_TAGS();
    const int ch = wave == 9 ? 0 : wave;
    const unsigned w2a = W2_OFF + (8 * r + q4) * 1152 + 9 * (wave - 8) * 32 + p4 * 8;
    if (wave >= 10) {
      wait_cnt<1>(F_W, 3);
#pragma unroll
      for (int t = 0; t < 9; ++t) w2f[t] = tr8(w2a + t * 32, w2a + t * 32 + 4 * 1152);
    } else if (wave != 0) {
      unsigned ho = HP_OFF + (r * 576 + 64 * ch + 4 * c) * 4, zw = Z_OFF + ch * 256 + r * 16, sw = S_OFF + r * 128 + ch * 8;
      unsigned hs_o = oob(u == 0, MP_OFF + 144 * 2304 + (64 * ch + 4 * c + r) * 4);
      PIN(ho); PIN(zw); PIN(sw); PIN(hs_o);
      v4u w1f[2][2];
#pragma unroll
      for (int s = 0; s < 2; ++s)
#pragma unroll
        for (int t = 0; t < 2; ++t) { const unsigned a = W1_OFF + (64 * ch + 4 * (8 * s + q4) + r) * 64 + t * 32 + p4 * 8; w1f[s][t] = tr8(a, a + 16 * 64); }
      float xv = lds_ld<float>(X_OFF + (64 * ch + 4 * c + r) * 4);
      const v4u ones = {0x3f803f80u, 0x3f803f80u, 0x3f803f80u, 0x3f803f80u};
#pragma unroll
      for (int t = 0; t < 2; ++t) {
        const v4f cs = mfma(w1f[1][t], ones, mfma(w1f[0][t], ones, zero));
        if (c == 0) lds_st<v4f>(zw + 128 + t * 64, cs);
      }
      PIN(xv);
      if (wave == 1) {
        unsigned hq[2] = {H2_OFF + lane * 128, oob(lane < 32, H2_OFF + (64 + lane) * 128)};
        wait_cnt<1>(F_W, 2);
        ST(12);
#pragma nounroll
        for (unsigned spin = 0; spin < SPIN_CAP; ++spin) {
          v2u t[2]; bool more = false;
          CBAR();
#pragma unroll
          for (int i = 0; i < 2; ++i) t[i] = __builtin_amdgcn_raw_buffer_load_b64(ws, hq[i], 0, AUX_SC1);
#pragma unroll
          for (int i = 0; i < 2; ++i) { hq[i] = (t[i].x == T0 && t[i].y == 0u) ? WS_END : hq[i]; more |= hq[i] != WS_END; }
          if (!__any(more)) break;
        }
        lds_vst(F_GO, 1u);
        STW((v4u{T0, (unsigned)__builtin_amdgcn_s_memrealtime(), T1, 0u}), oob(lane < 8, H3_OFF + u * 128 + lane * 16));
      } else wait_cnt<0>(F_GO, 1);
      ST(5);
      v4u hv[4];
#pragma unroll
      for (int i = 0; i < 4; ++i) hv[i] = LDW(ho, i * 4 * 2304);
      SB();
      if (wave >= 8) {
        wait_cnt<0>(F_W, 3);
#pragma unroll
        for (int t = 0; t < 9; ++t) w2f[t] = tr8(w2a + t * 32, w2a + t * 32 + 4 * 1152);
      }
      SB();
      float at;
#pragma nounroll
      for (unsigned spin = 0; spin < SPIN_CAP; ++spin) {
        unsigned bad = 0;
        const v4f a = (untag4(hv[0], T0, T1, bad) + untag4(hv[1], T0, T1, bad)) + (untag4(hv[2], T0, T1, bad) + untag4(hv[3], T0, T1, bad));
        at = t16(t32(a[0], a[2]), t32(a[1], a[3]));
        if (ALL_OK(bad)) break;
        CBAR();
#pragma unroll
        for (int i = 0; i < 4; ++i) hv[i] = LDW(ho, i * 4 * 2304);
      }
      ST(6);
      const float hs = xv + rb(at);
      const unsigned pr = pk(hs, dpp<XOR1>(hs));
      const v4u f0 = {dppu<NEWB + 0>(pr), dppu<NEWB + 2>(pr), dppu<NEWB + 4>(pr), dppu<NEWB + 6>(pr)};
      const v4u f1 = {dppu<NEWB + 8>(pr), dppu<NEWB + 10>(pr), dppu<NEWB + 12>(pr), dppu<NEWB + 14>(pr)};
      const v4f z0 = mfma(w1f[1][0], f1, mfma(w1f[0][0], f0, zero)), z1 = mfma(w1f[1][1], f1, mfma(w1f[0][1], f0, zero));
      const v4f sq = mfma(c == 1 ? ones : f1, f1, mfma(c == 1 ? ones : f0, f0, zero));
      if (c == 0) { lds_st<v4f>(zw, z0); lds_st<v4f>(zw + 64, z1); lds_st<v2f>(sw, v2f{sq[0], sq[1]}); }
      __builtin_amdgcn_raw_buffer_store_b32((pr << 16) | T0, ws, hs_o, 0, AUX_SC1);
    }
    if (wave >= 8) {
#pragma unroll
      for (int t = 0; t < 9; ++t) PIN(w2f[t]);
    }
    ST(7);
    bar();
    if (wave >= 8) {
      ST(11);
      const v4f sa = lds_ld<v4f>(S_OFF), sb = lds_ld<v4f>(S_OFF + 16), sc = lds_ld<v4f>(S_OFF + 32), sd = lds_ld<v4f>(S_OFF + 48);
      const v2f se = lds_ld<v2f>(S_OFF + 64);
      float zz[9];
#pragma unroll
      for (int i = 0; i < 9; ++i) zz[i] = lds_ld<float>(za + i * 256);
      SB();
      const v4f s4 = (sa + sb) + (sc + sd);
      const float mean = ((s4[1] + s4[3]) + se[1]) * (1.f / D), var = fmaxf(((s4[0] + s4[2]) + se[0]) * (1.f / D) - mean * mean, 0.f);
      const float r1 = __builtin_amdgcn_rsqf(var + 1e-5f);
      const float r2 = __builtin_amdgcn_rsqf(var * r1 * r1 + 1e-5f);
      const float t = (((zz[0] + zz[1]) + (zz[2] + zz[3])) + ((zz[4] + zz[5]) + (zz[6] + zz[7]))) + zz[8];
      const float z = (t - mean * dpp<XOR8>(t)) * (r1 * r2);
      const float zb = rb(rb(z) + b1v);
      const float e = __builtin_amdgcn_exp2f(2.302208198144325f * (zb + 0.044715f * zb * zb * zb));
      const float gl = zb - zb * __builtin_amdgcn_rcpf(e + 1.f);
      const unsigned gp = pk(gl, dpp<XOR1>(gl));
#define GB(bk) v4u{(unsigned)__builtin_amdgcn_update_dpp(0, (int)gp, NEWB + 0, 0xf, 1 << (bk), false), (unsigned)__builtin_amdgcn_update_dpp(0, (int)gp, NEWB + 2, 0xf, 1 << (bk), false), \
                   (unsigned)__builtin_amdgcn_update_dpp(0, (int)gp, NEWB + 4, 0xf, 1 << (bk), false), (unsigned)__builtin_amdgcn_update_dpp(0, (int)gp, NEWB + 6, 0xf, 1 << (bk), false)}
      const v4u g0 = GB(0), g1 = GB(1), g2 = GB(2), g3 = GB(3);
#undef GB
      const v4f o2 = mfma(w2f[8], g0, zero);
      const v4f o0 = mfma(w2f[3], g3, mfma(w2f[2], g2, mfma(w2f[1], g1, mfma(w2f[0], g0, zero))));
      const v4f o1 = mfma(w2f[7], g3, mfma(w2f[6], g2, mfma(w2f[5], g1, mfma(w2f[4], g0, zero))));
      v4f o;
#pragma unroll
      for (int k = 0; k < 4; ++k) { const float a0 = o0[k], a1 = o1[k], a2 = o2[k]; const float lo_ = (c & 2) ? a2 : a0; o[k] = (c & 1) ? a1 : lo_; }
      STW(tag4(o, T0, T1), mo);
      ST(8);
    }
  }
  if (!TRUNC && red < 36 && wave == 1) {
    const int pq = lane >> 2, c4 = lane & 3, col = 16 * red + 4 * c4 + r;
    float b2v = lo16(((const GAS u16*)p.b2)[col]);
    PIN(b2v);
    float t, hf;
    wait_cnt<8>(F_L, 9);
    GET_TAGS();
    {
      const unsigned tref = (unsigned)__builtin_amdgcn_s_memrealtime();
      unsigned hq[3]; int late = 0;
#pragma unroll
      for (int i = 0; i < 3; ++i) hq[i] = oob(i * 64 + lane < 144, H3_OFF + (i * 64 + lane) * 128);
#pragma nounroll
      for (unsigned spin = 0; spin < SPIN_CAP; ++spin) {
        v2u t[3]; bool more = false;
        CBAR();
#pragma unroll
        for (int i = 0; i < 3; ++i) t[i] = __builtin_amdgcn_raw_buffer_load_b64(ws, hq[i], 0, AUX_SC1);
#pragma unroll
        for (int i = 0; i < 3; ++i) { const bool ok = hq[i] != WS_END && t[i].x == T0; late = ok ? max(late, (int)(t[i].y - tref)) : late; hq[i] = ok ? WS_END : hq[i]; more |= hq[i] != WS_END; }
        if (!__any(more)) break;
      }
      ST(7);
      float lf = wave_max((float)late);
      const int go = min((int)asf(__builtin_amdgcn_readfirstlane(asu(lf))) + DELTA3, (int)((unsigned)__builtin_amdgcn_s_memrealtime() - tref) + DELTA3);
      while ((int)((unsigned)__builtin_amdgcn_s_memrealtime() - tref) < go) {}
    }
    ST(8);
#pragma nounroll
    for (unsigned spin = 0; spin < SPIN_CAP; ++spin) {
      v4u mv[9];
      CBAR();
      const unsigned hu = __builtin_amdgcn_raw_buffer_load_b32(ws, MP_OFF + 144 * 2304 + col * 4, 0, AUX_SC1) ^ T0;
#pragma unroll
      for (int i = 0; i < 9; ++i) mv[i] = LDW(MP_OFF + pq * 2304 + red * 64 + c4 * 16, i * 16 * 2304);
      unsigned bad = hu; hf = asf(hu);
#define UT(i) untag4(mv[i], T0, T1, bad)
      const v4f a = (((UT(0) + UT(1)) + (UT(2) + UT(3))) + ((UT(4) + UT(5)) + (UT(6) + UT(7)))) + UT(8);
#undef UT
      t = t16(t32(a[0], a[2]), t32(a[1], a[3]));
      t += dpp<XOR8>(t); t += dpp<XOR7>(dpp<XOR3>(t));
      if (ALL_OK(bad)) break;
    }
    const float res = hf + rb(rb(t) + b2v);
    if ((lane & 12) == 0) ((GAS u16*)p.out)[col] = (u16)pk(res, res);
    if (red == 0 && lane == 0) __builtin_amdgcn_raw_buffer_store_b32(T0 | (T1 << 16), ws, EP_OFF, 0, AUX_SC1);
    ST(9);
  }
  if (t_ev) while (__builtin_amdgcn_s_memrealtime() < t_ev + 4000) __builtin_amdgcn_s_sleep(1);
}

extern "C" void kernel_launch(void* const* d_in, const int* in_sizes, int n_in, void* d_out, int out_size, void* d_ws, size_t ws_size, hipStream_t stream) {
  Params p;
  p.x = (const u16*)d_in[0]; p.k = (const u16*)d_in[1]; p.v = (const u16*)d_in[2]; p.wq = (const u16*)d_in[3]; p.wo = (const u16*)d_in[4];
  p.w1 = (const u16*)d_in[6]; p.b1 = (const u16*)d_in[7]; p.w2 = (const u16*)d_in[8]; p.b2 = (const u16*)d_in[9];
  p.out = (u16*)d_out; p.ws = (char*)d_ws + WSPOS(ws_size);
  hipLaunchKernelGGL(fused, dim3(NBLK), dim3(NTHR), 0, stream, p);
}
```

```cpp
#define MARG 50
#define LEAD 45
#define DELTA3 165
#define CODEB 18432
#include <hip/hip_runtime.h>
#include <hip/hip_bf16.h>

typedef unsigned short u16;
typedef unsigned long long u64;
typedef unsigned v4u __attribute__((ext_vector_type(4)));
typedef unsigned v2u __attribute__((ext_vector_type(2)));
typedef float v4f __attribute__((ext_vector_type(4)));
typedef float v2f __attribute__((ext_vector_type(2)));
typedef short v4s __attribute__((ext_vector_type(4)));
typedef __bf16 bf16x2 __attribute__((ext_vector_type(2)));
typedef __bf16 bf16x8 __attribute__((ext_vector_type(8)));
#define GAS __attribute__((address_space(1)))
#define LAS __attribute__((address_space(3)))
#define STAMP 0
#define WSPOS(sz) 0
#define L2WARM 1
#define MPT 1
#define DEEP 1
#define DBD 32
#define DCD 32
#define DV DC
#define DA 20
#define DB 24
#define DC 28
#define SL1 2
#define SL2 3
#define SL3 6
#define DE 25
#define DF 24
#define PRED 0
#define CWO 100
#define HCOMPACT 1
#define AHEAD 60
#define SLH 4
#define WOEARLY 12
#define HEARLY 20
#define TRUNC 0
#define QSTAG 16
#define DELTAW 120
#define CW1 225
#define SLR 10
#define HREAD 2
#define HWAIT 40
#define QDELAY 16
#define WTHR_WO 0
#define WTHR_MLP 0

constexpr int D = 576, SK = 65536, DH = 128, WIN = 4096, FF = 4608, QW = 4096;
constexpr int NWAVE = 15, NTHR = NWAVE * 64, NBLK = 256;
constexpr unsigned EP_OFF = 0, QP_OFF = 4096, OP_OFF = QP_OFF + 16 * 4 * 256 * 4, ST_OFF = OP_OFF + 16 * 16 * 256 * 4,
                   HP_OFF = ST_OFF + 256 * 128, MP_OFF = HP_OFF + 16 * 576 * 4, H2_OFF = MP_OFF + 145 * 576 * 4, H1_OFF = H2_OFF + 96 * 128, H3_OFF = H1_OFF + 16 * 128, WS_END = H3_OFF + 144 * 128, DBG_OFF = 1u << 20;
constexpr unsigned SENT = 0xFFFFFFFFu;
constexpr unsigned SPIN_CAP = 1u << 16, LSPIN_CAP = 1u << 22;
constexpr int AUX_SC1 = 16;
constexpr unsigned CODE_BYTES = CODEB;
constexpr unsigned F_L = 0, F_I = 4, F_W = 8, F_Z = 12, F_Q = 40, F_P = 16, F_KD = 20, F_PUB = 24, F_Y = 28, F_GO = 32, F_T = 36, MX_OFF = 64, TG_OFF = 96,
                   Q_OFF = 128, P_OFF = Q_OFF + 512, TT_OFF = P_OFF + 1024, O_OFF = TT_OFF + 512, S_OFF = O_OFF + 512, X_OFF = S_OFF + 512,
                   Z_OFF = X_OFF + 2304, M_END = Z_OFF + 2304;
constexpr unsigned W_OFF = 8192, K_OFF = W_OFF + 24576, V_OFF = K_OFF + 65536, LDS_BYTES = V_OFF + 65536;
static_assert(M_END <= W_OFF && LDS_BYTES <= 160 * 1024, "LDS");
constexpr unsigned W1_OFF = W_OFF, W2_OFF = W_OFF + 36864;

struct Params { const u16 *x, *k, *v, *wq, *wo, *w1, *b1, *w2, *b2; u16* out; char* ws; };

__device__ __forceinline__ float asf(unsigned u) { return __builtin_bit_cast(float, u); }
__device__ __forceinline__ unsigned asu(float f) { return __builtin_bit_cast(unsigned, f); }
__device__ __forceinline__ float lo16(unsigned u) { return asf(u << 16); }
__device__ __forceinline__ float hi16(unsigned u) { return asf(u & 0xffff0000u); }
__device__ __forceinline__ unsigned pk(float lo, float hi) { bf16x2 r; r.x = (__bf16)lo; r.y = (__bf16)hi; return __builtin_bit_cast(unsigned, r); }
__device__ __forceinline__ float rb(float f) { return lo16(pk(f, f)); }
__device__ __forceinline__ float dot2(unsigned a, unsigned b, float c) {
  return __builtin_amdgcn_fdot2_f32_bf16(__builtin_bit_cast(bf16x2, a), __builtin_bit_cast(bf16x2, b), c, false);
}
__device__ __forceinline__ v4f mfma(v4u a, v4u b, v4f c) {
  return __builtin_amdgcn_mfma_f32_16x16x32_bf16(__builtin_bit_cast(bf16x8, a), __builtin_bit_cast(bf16x8, b), c, 0, 0, 0);
}
template <int CTRL> __device__ __forceinline__ float dpp(float x) { return asf(__builtin_amdgcn_mov_dpp(asu(x), CTRL, 0xf, 0xf, true)); }
template <int CTRL> __device__ __forceinline__ unsigned dppu(unsigned x) { return __builtin_amdgcn_mov_dpp(x, CTRL, 0xf, 0xf, true); }
constexpr int XOR1 = 0xB1, XOR2 = 0x4E, XOR7 = 0x141, XOR8 = 0x128, XOR3 = 0x1B, NEWB = 0x150;
__device__ __forceinline__ float vmax(float a, float b) { float r; asm("v_max_f32 %0, %1, %2" : "=v"(r) : "v"(a), "v"(b)); return r; }
__device__ __forceinline__ float x16_sum(float x) { auto s = __builtin_amdgcn_permlane16_swap(asu(x), asu(x), false, false); return asf(s[0]) + asf(s[1]); }
__device__ __forceinline__ float x32_sum(float x) { auto s = __builtin_amdgcn_permlane32_swap(asu(x), asu(x), false, false); return asf(s[0]) + asf(s[1]); }
__device__ __forceinline__ float x16_max(float x) { auto s = __builtin_amdgcn_permlane16_swap(asu(x), asu(x), false, false); return fmaxf(asf(s[0]), asf(s[1])); }
__device__ __forceinline__ float x32_max(float x) { auto s = __builtin_amdgcn_permlane32_swap(asu(x), asu(x), false, false); return fmaxf(asf(s[0]), asf(s[1])); }
__device__ __forceinline__ float t32(float a, float b) { auto s = __builtin_amdgcn_permlane32_swap(asu(a), asu(b), false, false); return asf(s[0]) + asf(s[1]); }
__device__ __forceinline__ float t16(float a, float b) { auto s = __builtin_amdgcn_permlane16_swap(asu(a), asu(b), false, false); return asf(s[0]) + asf(s[1]); }
template <int CTRL> __device__ __forceinline__ float tdpp(float a, float b, bool hi) { const float s = hi ? a : b, k = hi ? b : a; return k + dpp<CTRL>(s); }
__device__ __forceinline__ float row_sum(float x) { x += dpp<XOR1>(x); x += dpp<XOR2>(x); x += dpp<XOR7>(x); x += dpp<XOR8>(x); return x; }
__device__ __forceinline__ float row_max(float x) { x = fmaxf(x, dpp<XOR1>(x)); x = fmaxf(x, dpp<XOR2>(x)); x = fmaxf(x, dpp<XOR7>(x)); x = fmaxf(x, dpp<XOR8>(x)); return x; }
__device__ __forceinline__ float wave_sum(float x) { return x32_sum(x16_sum(row_sum(x))); }
__device__ __forceinline__ float wave_max(float x) { return x32_max(x16_max(row_max(x))); }
__device__ __forceinline__ v4u ldnt4(const void* p) { return __builtin_nontemporal_load((const GAS v4u*)p); }
__device__ __forceinline__ unsigned clean(float f) { const unsigned u = asu(f); return u < SENT ? u : SENT - 1u; }
__device__ __forceinline__ bool has_sent(v4u v) { return max(max(v.x, v.y), max(v.z, v.w)) == SENT; }
#define DRAIN() asm volatile("s_waitcnt vmcnt(0)" ::: "memory")
#define CBAR() asm volatile("" ::: "memory")
#define LGKM0() asm volatile("s_waitcnt lgkmcnt(0)" ::: "memory")
#define SB() __builtin_amdgcn_sched_barrier(0)
__device__ __forceinline__ void bar() { LGKM0(); __builtin_amdgcn_s_barrier(); CBAR(); }
template <typename T> __device__ __forceinline__ T lds_ld(unsigned off) { return *(const LAS T*)(size_t)off; }
template <typename T> __device__ __forceinline__ void lds_st(unsigned off, T v) { *(LAS T*)(size_t)off = v; }
__device__ __forceinline__ v4u lds_vld4(unsigned off) { return *(const volatile LAS v4u*)(size_t)off; }
__device__ __forceinline__ unsigned lds_vld(unsigned off) { return *(const volatile LAS unsigned*)(size_t)off; }
__device__ __forceinline__ void lds_vst(unsigned off, unsigned v) { *(volatile LAS unsigned*)(size_t)off = v; }
__device__ __forceinline__ v4u tr8(unsigned a0, unsigned a1) {
  const v2u x = __builtin_bit_cast(v2u, __builtin_amdgcn_ds_read_tr16_b64_v4i16((LAS v4s*)(size_t)a0));
  const v2u y = __builtin_bit_cast(v2u, __builtin_amdgcn_ds_read_tr16_b64_v4i16((LAS v4s*)(size_t)a1));
  return v4u{x.x, x.y, y.x, y.y};
}

#define PIN(x) asm volatile("" : "+v"(x))
__device__ __forceinline__ unsigned oob(bool keep, unsigned off) { PIN(off); return keep ? off : WS_END; }
template <int SLEEP> __device__ __forceinline__ void wait_cnt(unsigned off, unsigned n) {
#pragma nounroll
  for (unsigned s = 0; s < LSPIN_CAP; ++s) { if (__builtin_amdgcn_readfirstlane(lds_vld(off)) >= n) break; if (SLEEP) __builtin_amdgcn_s_sleep(SLEEP); }
  CBAR();
}
template <int SLEEP> __device__ __forceinline__ void wait_vals8(unsigned off, v4u& a, v4u& b) {
#pragma nounroll
  for (unsigned s = 0; s < LSPIN_CAP; ++s) {
    a = lds_vld4(off); b = lds_vld4(off + 16);
    if (__builtin_amdgcn_readfirstlane(max(max(max(a.x, a.y), max(a.z, a.w)), max(max(b.x, b.y), max(b.z, b.w)))) != SENT) break;
    if (SLEEP) __builtin_amdgcn_s_sleep(SLEEP);
  }
  CBAR();
}
#define lds_inc(off) asm volatile("ds_add_u32 %0, %1 offset:%2" :: "v"(0u), "v"(1u), "i"(off) : "memory")
#define LOOP_END(n) "s_sub_u32 " n ", " n ", 1\n\ts_cmp_lg_u32 " n ", 0\n\ts_cbranch_scc1 1b"
template <int N, int DEP> __device__ __forceinline__ void dma1(unsigned& voff, unsigned lds, const void* base, unsigned gstep) {
  static_assert(N % 2 == 0, "");
  unsigned n = N / 2;
  asm volatile("s_mov_b32 m0, %2\n\t1:\n\t.rept 2\n\ts_waitcnt vmcnt(%5)\n\tglobal_load_lds_dwordx4 %0, %3 nt\n\ts_add_u32 m0, m0, 0x400\n\tv_add_u32 %0, %4, %0\n\t.endr\n\t" LOOP_END("%1")
               : "+v"(voff), "+s"(n) : "s"(lds), "s"(base), "s"(gstep), "i"(DEP - 1) : "memory", "scc");
}
template <int NG, int DEP> __device__ __forceinline__ void dma4(unsigned (&v)[4], unsigned lds, const void* base) {
  unsigned n = NG;
  if (NG > 0)
    asm volatile("s_mov_b32 m0, %5\n\t1:\n\t"
                 "s_waitcnt vmcnt(%7)\n\tglobal_load_lds_dwordx4 %0, %6 nt\n\tv_add_u32 %0, 0x1000, %0\n\t"
                 "s_waitcnt vmcnt(%7)\n\tglobal_load_lds_dwordx4 %1, %6 offset:1024 nt\n\tv_add_u32 %1, 0x1000, %1\n\t"
                 "s_waitcnt vmcnt(%7)\n\tglobal_load_lds_dwordx4 %2, %6 offset:2048 nt\n\tv_add_u32 %2, 0x1000, %2\n\t"
                 "s_waitcnt vmcnt(%7)\n\tglobal_load_lds_dwordx4 %3, %6 offset:3072 nt\n\tv_add_u32 %3, 0x1000, %3\n\t"
                 "s_add_u32 m0, m0, 0x1000\n\t" LOOP_END("%4")
                 : "+v"(v[0]), "+v"(v[1]), "+v"(v[2]), "+v"(v[3]), "+s"(n) : "s"(lds), "s"(base), "i"(DEP - 1) : "memory", "scc");
}
template <int NG, int DEP> __device__ __forceinline__ void dma3(unsigned (&v)[3], unsigned lds, const void* base, unsigned gstep) {
  unsigned n = NG;
  asm volatile("s_mov_b32 m0, %4\n\t1:\n\t"
               "s_waitcnt vmcnt(%7)\n\tglobal_load_lds_dwordx4 %0, %5 nt\n\ts_add_u32 m0, m0, 0x400\n\tv_add_u32 %0, %6, %0\n\t"
               "s_waitcnt vmcnt(%7)\n\tglobal_load_lds_dwordx4 %1, %5 nt\n\ts_add_u32 m0, m0, 0x400\n\tv_add_u32 %1, %6, %1\n\t"
               "s_waitcnt vmcnt(%7)\n\tglobal_load_lds_dwordx4 %2, %5 nt\n\ts_add_u32 m0, m0, 0x400\n\tv_add_u32 %2, %6, %2\n\t" LOOP_END("%3")
               : "+v"(v[0]), "+v"(v[1]), "+v"(v[2]), "+s"(n) : "s"(lds), "s"(base), "s"(gstep), "i"(DEP - 1) : "memory", "scc");
}
template <int N, int A2, int STEP> __device__ __forceinline__ void ramp1(unsigned& voff, unsigned lds, const void* base, unsigned gstep) {
  asm volatile("s_mov_b32 m0, %1\n\t.set rk_, %4\n\t.rept %6\n\ts_waitcnt vmcnt(rk_ / 2)\n\tglobal_load_lds_dwordx4 %0, %2 nt\n\ts_add_u32 m0, m0, 0x400\n\tv_add_u32 %0, %3, %0\n\t"
               ".set rk_, rk_ + (%5)\n\t.endr" : "+v"(voff) : "s"(lds), "s"(base), "s"(gstep), "i"(A2), "i"(STEP), "i"(N) : "memory", "scc");
}
template <int NG, int A> __device__ __forceinline__ void ramp3(unsigned (&v)[3], unsigned lds, const void* base, unsigned gstep) {
  asm volatile("s_mov_b32 m0, %3\n\t.set rk_, %6\n\t.rept %7\n\ts_waitcnt vmcnt(rk_)\n\t"
               "global_load_lds_dwordx4 %0, %4 nt\n\ts_add_u32 m0, m0, 0x400\n\tv_add_u32 %0, %5, %0\n\t"
               "global_load_lds_dwordx4 %1, %4 nt\n\ts_add_u32 m0, m0, 0x400\n\tv_add_u32 %1, %5, %1\n\t"
               "global_load_lds_dwordx4 %2, %4 nt\n\ts_add_u32 m0, m0, 0x400\n\tv_add_u32 %2, %5, %2\n\t.set rk_, rk_ - 1\n\t.endr"
               : "+v"(v[0]), "+v"(v[1]), "+v"(v[2]) : "s"(lds), "s"(base), "s"(gstep), "i"(A), "i"(NG) : "memory", "scc");
}
__device__ __forceinline__ unsigned now32() { return (unsigned)__builtin_amdgcn_s_memrealtime(); }
#define VMW(n) asm volatile("s_waitcnt vmcnt(%0)" :: "i"(n) : "memory")

#define ST(i) do {} while (0)
#define STE(i) do {} while (0)
#define LDW(off, soff) __builtin_amdgcn_raw_buffer_load_b128(ws, off, soff, AUX_SC1)
#define STW(v, off) __builtin_amdgcn_raw_buffer_store_b128(v, ws, off, 0, AUX_SC1)
__device__ __forceinline__ v4u tag4(v4f v, unsigned T0, unsigned T1) {
  unsigned m = 0xfffff000u; asm("" : "+v"(m));
  return v4u{(asu(v[0]) & m) | T0, asu(v[1]), (asu(v[2]) & m) | T1, asu(v[3])};
}
__device__ __forceinline__ v4f untag4(v4u v, unsigned T0, unsigned T1, unsigned& bad) {
  const unsigned a = v.x ^ T0, c = v.z ^ T1; bad |= a | c;
  return v4f{asf(a), asf(v.y), asf(c), asf(v.w)};
}
template <int N> __device__ __forceinline__ unsigned flip(v4u (&t)[N], unsigned T0, unsigned T1) {
  unsigned bad = 0;
#pragma unroll
  for (int i = 0; i < N; ++i) { t[i].x ^= T0; t[i].z ^= T1; asm("" : "+v"(t[i])); bad |= t[i].x | t[i].z; }
  return bad;
}
#define ALL_OK(bad) (!__any(((bad) & 0xfffu) != 0u))
#define GET_TAGS() const v2u tg_ = lds_ld<v2u>(TG_OFF); const unsigned T0 = __builtin_amdgcn_readfirstlane(tg_.x), T1 = __builtin_amdgcn_readfirstlane(tg_.y)
#define CHK(x, z, m, T0, T1) "s_mov_b64 exec, " m "\n\tv_xor_b32 " x ", " T0 ", " x "\n\tv_xor_b32 " z ", " T1 ", " z "\n\tv_or_b32 v83, " x ", " z "\n\tv_and_b32 v83, 0xfff, v83\n\tv_cmp_ne_u32 " m ", 0, v83\n\t"
#define ASK(t, m, off, rs, so) "s_mov_b64 exec, " m "\n\ts_mov_b32 s59, " so "\n\tbuffer_load_dwordx4 " t ", " off ", " rs ", s59 offen sc1\n\t"
#define SETTLE4(off, rs, T0, T1, S1, S2, S3) \
  "s_mov_b64 s[60:61], -1\n\ts_mov_b64 s[62:63], -1\n\ts_mov_b64 s[64:65], -1\n\ts_mov_b64 s[66:67], -1\n\ts_mov_b32 s58, 0x10000\n\t" \
  "2:\n\ts_waitcnt vmcnt(0)\n\t" \
  CHK("v84", "v86", "s[60:61]", T0, T1) CHK("v88", "v90", "s[62:63]", T0, T1) CHK("v92", "v94", "s[64:65]", T0, T1) CHK("v96", "v98", "s[66:67]", T0, T1) \
  "s_or_b64 s[56:57], s[60:61], s[62:63]\n\ts_or_b64 s[56:57], s[56:57], s[64:65]\n\ts_or_b64 s[56:57], s[56:57], s[66:67]\n\ts_cbranch_scc0 3f\n\t" \
  "s_sub_u32 s58, s58, 1\n\ts_cbranch_scc1 3f\n\ts_sleep 6\n\t" \
  ASK("v[84:87]", "s[60:61]", off, rs, "0") ASK("v[88:91]", "s[62:63]", off, rs, S1) ASK("v[92:95]", "s[64:65]", off, rs, S2) ASK("v[96:99]", "s[66:67]", off, rs, S3) \
  "s_branch 2b\n\t3:\n\ts_mov_b64 exec, -1"
#define CLOB4 "memory", "scc", "vcc", "v83", "s56", "s57", "s58", "s59", "s60", "s61", "s62", "s63", "s64", "s65", "s66", "s67"
template <int STEP> __device__ __forceinline__ void settle4(v4u (&t)[4], unsigned off, __amdgpu_buffer_rsrc_t ws, unsigned T0, unsigned T1) {
  asm volatile(SETTLE4("%4", "%5", "%6", "%7", "%8", "%9", "%10")
               : "+{v[84:87]}"(t[0]), "+{v[88:91]}"(t[1]), "+{v[92:95]}"(t[2]), "+{v[96:99]}"(t[3])
               : "v"(off), "s"(ws), "s"(T0), "s"(T1), "i"(STEP), "i"(2 * STEP), "i"(3 * STEP) : CLOB4);
}
#define ASK2(t, m, so) "s_andn2_b64 exec, -1, " m "\n\ts_cbranch_execz 4f\n\tbuffer_load_dwordx4 " t ", %4, %5, " so " offen sc1\n\t4:\n\t"
template <int STEP> __device__ __forceinline__ void two_batches(v4u (&t)[4], const u64 (&m)[4], unsigned off, __amdgpu_buffer_rsrc_t ws) {
  asm volatile("s_mov_b64 exec, %9\n\tbuffer_load_dwordx4 %0, %4, %5, 0 offen sc1\n\ts_mov_b64 exec, %10\n\tbuffer_load_dwordx4 %1, %4, %5, %6 offen sc1\n\t"
               "s_mov_b64 exec, %11\n\tbuffer_load_dwordx4 %2, %4, %5, %7 offen sc1\n\ts_mov_b64 exec, %12\n\tbuffer_load_dwordx4 %3, %4, %5, %8 offen sc1\n\t"
               "s_mov_b64 exec, -1\n\tv_mov_b32 v82, 0\n\ts_mov_b32 s58, 0x400000\n\t"
               "1:\n\tds_read_b32 v83, v82 offset:%13\n\ts_sub_u32 s58, s58, 1\n\ts_cbranch_scc1 2f\n\ts_waitcnt lgkmcnt(0)\n\tv_readfirstlane_b32 s59, v83\n\ts_cmp_lt_u32 s59, 2\n\ts_cbranch_scc1 1b\n\t2:\n\t"
               ASK2("%0", "%9", "0") ASK2("%1", "%10", "%6") ASK2("%2", "%11", "%7") ASK2("%3", "%12", "%8")
               "s_mov_b64 exec, -1\n\ts_waitcnt vmcnt(0) lgkmcnt(0)"
               : "=&v"(t[0]), "=&v"(t[1]), "=&v"(t[2]), "=&v"(t[3])
               : "v"(off), "s"(ws), "s"(STEP), "s"(2 * STEP), "s"(3 * STEP), "s"(m[0]), "s"(m[1]), "s"(m[2]), "s"(m[3]), "i"(F_GO)
               : "memory", "scc", "v82", "v83", "s58", "s59");
}
template <int STEP> __device__ __forceinline__ void gather9(v4u (&t)[9], unsigned off, __amdgpu_buffer_rsrc_t ws, unsigned T0, unsigned T1) {
  asm volatile("s_mov_b64 s[60:61], -1\n\ts_mov_b64 s[62:63], -1\n\ts_mov_b64 s[64:65], -1\n\ts_mov_b64 s[66:67], -1\n\ts_mov_b64 s[68:69], -1\n\t"
               "s_mov_b64 s[70:71], -1\n\ts_mov_b64 s[72:73], -1\n\ts_mov_b64 s[74:75], -1\n\ts_mov_b64 s[76:77], -1\n\ts_mov_b32 s58, 0x10000\n\t"
               "2:\n\t"
               ASK("v[84:87]", "s[60:61]", "%9", "%10", "0") ASK("v[88:91]", "s[62:63]", "%9", "%10", "%13") ASK("v[92:95]", "s[64:65]", "%9", "%10", "2*%13")
               ASK("v[96:99]", "s[66:67]", "%9", "%10", "3*%13") ASK("v[100:103]", "s[68:69]", "%9", "%10", "4*%13") ASK("v[104:107]", "s[70:71]", "%9", "%10", "5*%13")
               ASK("v[108:111]", "s[72:73]", "%9", "%10", "6*%13") ASK("v[112:115]", "s[74:75]", "%9", "%10", "7*%13") ASK("v[116:119]", "s[76:77]", "%9", "%10", "8*%13")
               "s_waitcnt vmcnt(8)\n\t" CHK("v84", "v86", "s[60:61]", "%11", "%12") "s_waitcnt vmcnt(7)\n\t" CHK("v88", "v90", "s[62:63]", "%11", "%12")
               "s_waitcnt vmcnt(6)\n\t" CHK("v92", "v94", "s[64:65]", "%11", "%12") "s_waitcnt vmcnt(5)\n\t" CHK("v96", "v98", "s[66:67]", "%11", "%12")
               "s_waitcnt vmcnt(4)\n\t" CHK("v100", "v102", "s[68:69]", "%11", "%12") "s_waitcnt vmcnt(3)\n\t" CHK("v104", "v106", "s[70:71]", "%11", "%12")
               "s_waitcnt vmcnt(2)\n\t" CHK("v108", "v110", "s[72:73]", "%11", "%12") "s_waitcnt vmcnt(1)\n\t" CHK("v112", "v114", "s[74:75]", "%11", "%12")
               "s_or_b64 s[56:57], s[60:61], s[62:63]\n\ts_or_b64 s[56:57], s[56:57], s[64:65]\n\ts_or_b64 s[56:57], s[56:57], s[66:67]\n\ts_or_b64 s[56:57], s[56:57], s[68:69]\n\t"
               "s_or_b64 s[56:57], s[56:57], s[70:71]\n\ts_or_b64 s[56:57], s[56:57], s[72:73]\n\ts_or_b64 s[56:57], s[56:57], s[74:75]\n\t"
               "s_waitcnt vmcnt(0)\n\t" CHK("v116", "v118", "s[76:77]", "%11", "%12")
               "s_nop 0\n\ts_or_b64 s[56:57], s[56:57], s[76:77]\n\ts_cbranch_scc0 3f\n\ts_sub_u32 s58, s58, 1\n\ts_cbranch_scc0 2b\n\t3:\n\ts_mov_b64 exec, -1"
               : "=&{v[84:87]}"(t[0]), "=&{v[88:91]}"(t[1]), "=&{v[92:95]}"(t[2]), "=&{v[96:99]}"(t[3]), "=&{v[100:103]}"(t[4]), "=&{v[104:107]}"(t[5]),
                 "=&{v[108:111]}"(t[6]), "=&{v[112:115]}"(t[7]), "=&{v[116:119]}"(t[8])
               : "v"(off), "s"(ws), "s"(T0), "s"(T1), "i"(STEP)
               : CLOB4, "s68", "s69", "s70", "s71", "s72", "s73", "s74", "s75", "s76", "s77");
}
template <int N, int SL> __device__ __forceinline__ unsigned latest(unsigned (&hq)[N], __amdgpu_buffer_rsrc_t ws, unsigned T0, unsigned (&tm)[N]) {
  const unsigned tref = now32();
  int late = 0;
#pragma unroll
  for (int i = 0; i < N; ++i) tm[i] = tref - 1000u;
#pragma nounroll
  for (unsigned spin = 0; spin < SPIN_CAP; ++spin) {
    v2u t[N]; bool more = false;
    CBAR();
#pragma unroll
    for (int i = 0; i < N; ++i) t[i] = __builtin_amdgcn_raw_buffer_load_b64(ws, hq[i], 0, AUX_SC1);
#pragma unroll
    for (int i = 0; i < N; ++i) { const bool ok = hq[i] != WS_END && t[i].x == T0; late = ok ? max(late, (int)(t[i].y - tref)) : late; tm[i] = ok ? t[i].y : tm[i]; hq[i] = ok ? WS_END : hq[i]; more |= hq[i] != WS_END; }
    if (!__any(more)) break;
    __builtin_amdgcn_s_sleep(SL);
  }
  const int l = (int)asf(__builtin_amdgcn_readfirstlane(asu(wave_max((float)late))));
  return tref + min(l, (int)(now32() - tref) + 400);
}
#define F4(v) __builtin_bit_cast(v4f, v)
#define U4(v) __builtin_bit_cast(v4u, v)

__global__ void __launch_bounds__(NTHR, 4) fused(Params p) {
  __shared__ __attribute__((aligned(16))) char lds_raw[LDS_BYTES];
  u64 pc0; asm volatile("s_getpc_b64 %0" : "=s"(pc0));
  const int b = blockIdx.x, tid = threadIdx.x, wave = __builtin_amdgcn_readfirstlane(tid >> 6), lane = tid & 63;
  const int g = (b & 7) * 2 + ((b >> 3) & 1), j = b >> 4;
  const bool is_wo = j < 6, is_mlp = j >= 6 && j < 15;
  const int u = g * 9 + (j - 6);
  const int red = ((j + 1) & 15) * 16 + g;
  const __amdgpu_buffer_rsrc_t ws = __builtin_amdgcn_make_buffer_rsrc(p.ws, 0, WS_END, 0x00020000);
  const int r = lane >> 4, c = lane & 15;
  const int q4 = c >> 2, p4 = c & 3;
  const v4f zero = {0.f, 0.f, 0.f, 0.f};
  asm volatile("" :: "v"((LAS char*)lds_raw));
  STE(0);
  const int qt = j >> 2, qc = j & 3;
  if (__builtin_expect(wave == 0, 1)) {
    const unsigned vx = lane * 16, vy = min(lane * 16u, 112u);
    asm volatile("s_mov_b32 m0, %2\n\ts_nop 0\n\tglobal_load_lds_dwordx4 %0, %3\n\tglobal_load_lds_dwordx4 %1, %3 offset:1024"
                 :: "v"(vx), "v"(vy), "s"(W_OFF + 18 * 1024), "s"(p.x) : "memory");
    unsigned vq = (144 * qt + (lane >> 3)) * (QW * 2) + (256 * g + 64 * qc) * 2 + (lane & 7) * 16;
    dma1<18, 63>(vq, W_OFF, p.wq, 8u * QW * 2);
    const size_t hb = ((size_t)g * SK + (SK - WIN) + 256 * j) * DH;
    const u16 *kb = p.k + hb, *vb = p.v + hb;
    unsigned vk[4], vv[4];
#pragma unroll
    for (int n = 0; n < 4; ++n) { vk[n] = r * 256 + ((c ^ (4 * n + r)) << 4); vv[n] = r * 256 + ((c ^ ((r << 2) | n)) << 4); }
    static_assert(DB % 4 == 0 && DB >= 20 && DB <= 32 && DC % 4 == 0 && DC >= DB && DC <= 32, "stage positions");
    ST(10);
    bar();
#define KV(DA_, DB_, DC_) { \
    dma4<1, DA_>(vk, K_OFF, kb); \
    VMW(22); lds_vst(F_L, 1u);                                           \
    dma4<DA_ / 4 - 1, DA_>(vk, K_OFF + 4096, kb); \
    VMW(DA_); lds_vst(F_L, 2u); ST(12);                                  \
    dma4<(32 + DB_ - DA_) / 4, DB_>(vk, K_OFF + DA_ * 1024, kb); \
    VMW(DB_); lds_vst(F_L, 3u);                                          \
    dma4<(32 - DB_) / 4, DB_>(vk, K_OFF + (32 + DB_) * 1024, kb); \
    dma4<DC_ / 4, DC_>(vv, V_OFF, vb); \
    VMW(DC_); lds_vst(F_L, 4u); ST(1);                                   \
    dma4<(64 - DC_) / 4, DC_>(vv, V_OFF + DC_ * 1024, vb); }
    if ((DEEP == 2 && is_wo) || (DEEP && is_mlp && ((((unsigned)(size_t)p.w1 + 64 * u) >> 7) & 7) == 3)) KV(DA, DBD, DCD) else KV(DA, DB, DV)
#undef KV
    ST(2);
    lds_vst(F_L, 6u);
    static_assert(DC == 32 || DC == 28 || DC == 24, "ramps");
    if (is_mlp) {
      unsigned v1 = (lane >> 2) * (FF * 2) + 64 * u + (lane & 3) * 16, v2 = 32 * u * (D * 2) + lane * 16;
      wait_cnt<0>(F_PUB, 4);
      constexpr int K7 = (DC - 16) / 2, K8 = (DC - 8) / 2, K9 = DC / 2;
      ramp1<K7 - 1, 2 * (DC - 2), -2>(v1, W1_OFF, p.w1, 16u * FF * 2);
      VMW(16 + K7 - 1); lds_vst(F_L, 7u);
      ramp1<K8 - K7, 2 * (DC - 1 - K7), -2>(v1, W1_OFF + (K7 - 1) * 1024, p.w1, 16u * FF * 2);
      VMW(8 + K8 - 1); lds_vst(F_L, 8u);
      ramp1<K9 - K8, 2 * (DC - 1 - K8), -2>(v1, W1_OFF + (K8 - 1) * 1024, p.w1, 16u * FF * 2);
      VMW(K9 - 1); lds_vst(F_L, 9u); STE(4);
      wait_cnt<0>(F_KD, 4);
      ramp1<1, 2 * (DC / 2 - 1), 0>(v1, W1_OFF + (K9 - 1) * 1024, p.w1, 16u * FF * 2);
      dma1<36 - K9, DC / 2>(v1, W1_OFF + K9 * 1024, p.w1, 16u * FF * 2);
      ramp1<DC - 2, DC - 1, 1>(v2, W2_OFF, p.w2, 1024u);
      VMW(DC - 2); lds_vst(F_W, 1u);
      dma1<38 - DC, DC>(v2, W2_OFF + (DC - 2) * 1024, p.w2, 1024u);
      lds_vst(F_T, now32()); lds_vst(F_W, 2u); ST(13);
      VMW(0); lds_vst(F_W, 3u); STE(14);
    } else if (is_wo) {
      unsigned vo[3];
#pragma unroll
      for (int i = 0; i < 3; ++i) { const int pi = i * 64 + lane; vo[i] = (256 * g + pi / 12) * (D * 2) + 192 * j + (pi % 12) * 16; }
      wait_cnt<0>(F_PUB, 4);
      constexpr int K7 = (DC - 16) / 4, K8 = (DC - 8) / 4, K9 = DC / 4;
      ramp3<K7 - 1, DC - 4>(vo, W_OFF, p.wo, 16u * D * 2);
      VMW(16 + 3 * (K7 - 1)); lds_vst(F_L, 7u);
      ramp3<K8 - K7, DC - 3 - K7>(vo, W_OFF + (K7 - 1) * 3072, p.wo, 16u * D * 2);
      VMW(8 + 3 * (K8 - 1)); lds_vst(F_L, 8u);
      ramp3<K9 - K8, DC - 3 - K8>(vo, W_OFF + (K8 - 1) * 3072, p.wo, 16u * D * 2);
      VMW(3 * (K9 - 1)); lds_vst(F_L, 9u); STE(4);
      lds_vst(F_W, 1u);
      wait_cnt<0>(F_KD, 4);
      dma3<17 - K9, 3 * K9>(vo, W_OFF + (K9 - 1) * 3072, p.wo, 16u * D * 2);
      VMW(WOEARLY); lds_vst(F_W, 2u); ST(13);
      VMW(0); lds_vst(F_W, 3u); STE(14);
    } else {
      VMW(16); lds_vst(F_L, 7u);
      VMW(8); lds_vst(F_L, 8u);
      VMW(0); lds_vst(F_L, 9u); STE(4);
    }
  } else {
  if (wave == 1) lds_st<unsigned>((lane & 31) * 4, (lane & 16) ? SENT : 0u);
  if (L2WARM && j == 15 && wave >= 8 && wave <= 12) {
    v4u t;
    asm volatile("global_load_dwordx4 %0, %1, %2\n\tglobal_load_dwordx4 %0, %1, %2 offset:1024\n\tglobal_load_dwordx4 %0, %1, %2 offset:2048\n\t"
                 "global_load_dwordx4 %0, %1, %2 offset:3072\n\ts_barrier\n\ts_waitcnt vmcnt(0)"
                 : "=&v"(t) : "v"(min((wave - 8) * 4096u, CODE_BYTES - 4096u) + lane * 16), "s"(pc0) : "memory");
  } else {
    ST(10);
    bar();
  }
  }
  if (wave == 0) {
  } else if (wave == 1) {
    wait_cnt<1>(F_L, 1);
    const v4u xa = lds_ld<v4u>(W_OFF + 18 * 1024 + lane * 16);
    const unsigned xb = lds_ld<u16>(W_OFF + 19 * 1024 + lane * 2);
    float xv[9];
#pragma unroll
    for (int k = 0; k < 4; ++k) { xv[2 * k] = lo16(xa[k]); xv[2 * k + 1] = hi16(xa[k]); }
    xv[8] = lo16(xb);
    lds_st<v4f>(X_OFF + lane * 32, v4f{xv[0], xv[1], xv[2], xv[3]}); lds_st<v4f>(X_OFF + lane * 32 + 16, v4f{xv[4], xv[5], xv[6], xv[7]});
    lds_st<float>(X_OFF + 2048 + lane * 4, xv[8]);
    {
      const float c0 = asf(__builtin_amdgcn_readfirstlane(asu(xv[0])));
      float s = 0.f, q = 0.f;
#pragma unroll
      for (int i = 0; i < 9; ++i) { const float d = xv[i] - c0; s += d; q = fmaf(d, d, q); }
      s = wave_sum(s); q = wave_sum(q);
      const float md = s * (1.f / D), var = fmaxf(q * (1.f / D) - md * md, 0.f);
      const float mb = rb(c0 + md), rden = 1.f / rb(__builtin_sqrtf(rb(rb(var) + 1e-5f)));
#pragma unroll
      for (int i = 0; i < 9; ++i) xv[i] = rb(xv[i] - mb) * rden;
      lds_st<v4u>(Z_OFF + lane * 16, v4u{pk(xv[0], xv[1]), pk(xv[2], xv[3]), pk(xv[4], xv[5]), pk(xv[6], xv[7])});
      lds_st<u16>(Z_OFF + 1024 + lane * 2, (u16)pk(xv[8], xv[8]));
    }
    lds_vst(F_Y, 1u);
    ST(13);
    wait_cnt<1>(F_PUB, 4);
    {
      GET_TAGS();
      const v4u tq = {T0 & 63u, T0 >> 6, T1 & 63u, T1 >> 6};
      unsigned qa = QP_OFF + g * 4096 + lane * 16;
      PIN(qa);
      v4u ga[4], gb[4];
#define QLD(G_) { CBAR(); _Pragma("unroll") for (int t = 0; t < 4; ++t) G_[t] = LDW(qa, t * 1024); SB(); }
#define QCK(G_, O_) { unsigned bad = 0; _Pragma("unroll") for (int t = 0; t < 4; ++t) { G_[t] ^= tq; bad |= G_[t].x | G_[t].y | G_[t].z | G_[t].w; } \
      const bool ok = !__any((bad & 63u) != 0u) || spin + 1 == cap; \
      const v4f qs = (F4(G_[0]) + F4(G_[1])) + (F4(G_[2]) + F4(G_[3])); \
      lds_st<v2u>(Q_OFF + lane * 8, v2u{pk(qs[0], qs[1]), pk(qs[2], qs[3])}); lds_vst(F_Q, ok ? 1u : 0u); ST(2); SB(); \
      if (ok) break; }
      const unsigned cap = TRUNC >= 3 ? 1 : SPIN_CAP;
      __builtin_amdgcn_s_sleep(QDELAY);
      QLD(ga)
      __builtin_amdgcn_s_sleep(QSTAG);
#pragma nounroll
      for (unsigned spin = 0;; ++spin) {
        QLD(gb) QCK(ga, gb) QLD(ga) QCK(gb, ga)
      }
#undef QLD
#undef QCK
    }
  } else if (wave >= 4 && wave <= 7) {
    const int w = wave - 4;
    if (w == 0) {
      const unsigned ep = *(const __attribute__((address_space(4))) unsigned*)(p.ws + EP_OFF);
      lds_st<v2u>(TG_OFF, v2u{(ep + 1u) & 0xfffu, ((ep >> 16) + 1u) & 0xfffu});
    }
    unsigned ka[4], va[2][2];
#pragma unroll
    for (int s = 0; s < 4; ++s) { ka[s] = K_OFF + (16 * w + c) * 256 + (((4 * s + r) ^ c) << 4); PIN(ka[s]); }
#pragma unroll
    for (int tt = 0; tt < 2; ++tt)
#pragma unroll
      for (int t = 0; t < 2; ++t) { va[tt][t] = V_OFF + 256 * (8 * r + 4 * t + q4) + 16 * ((2 * (2 * w + tt) + (p4 >> 1)) ^ ((q4 << 2) | ((2 * r + t) & 3))) + 8 * (p4 & 1); PIN(va[tt][t]); }
    unsigned opo = oob(c < 4, OP_OFF + (g * 16 + j) * 1024 + ((c & 1) * 128 + 32 * w + 8 * (c & 2) + 4 * r) * 4);
    PIN(opo);
    {
      unsigned wa = W_OFF + (8 * r + q4) * 128 + p4 * 8 + w * 32, wb = W_OFF + (128 + 4 * r + q4) * 128 + p4 * 8 + w * 32;
      unsigned qo = oob(c == 0 && TRUNC < 3, QP_OFF + ((g * 4 + qt) * 256 + 64 * qc + 16 * w + 4 * r) * 4);
      PIN(wa); PIN(wb); PIN(qo);
#pragma nounroll
      for (unsigned s = 0; s < LSPIN_CAP; ++s) { if (__builtin_amdgcn_readfirstlane(lds_vld(TG_OFF)) != SENT) break; __builtin_amdgcn_s_sleep(1); }
      CBAR();
      GET_TAGS();
      wait_cnt<1>(F_Y, 1);
      v4u yf[4];
#pragma unroll
      for (int s = 0; s < 4; ++s) yf[s] = lds_ld<v4u>(Z_OFF + 288 * qt + 16 * r + 64 * s);
      const v2u yl = lds_ld<v2u>(Z_OFF + 288 * qt + 256 + 8 * r);
      wait_cnt<0>(F_L, 2);
      ST(13);
      v4u wf[4];
#pragma unroll
      for (int s = 0; s < 4; ++s) wf[s] = tr8(wa + s * 4096, wa + s * 4096 + 512);
      const v2u wl = __builtin_bit_cast(v2u, __builtin_amdgcn_ds_read_tr16_b64_v4i16((LAS v4s*)(size_t)wb));
      SB();
      const v4f qa = mfma(wf[3], yf[3], mfma(wf[2], yf[2], mfma(wf[1], yf[1], mfma(wf[0], yf[0], zero)))) +
                     __builtin_amdgcn_mfma_f32_16x16x16bf16_1k(__builtin_bit_cast(v4s, wl), __builtin_bit_cast(v4s, yl), zero, 0, 0, 0);
      const v4u tq = {T0 & 63u, T0 >> 6, T1 & 63u, T1 >> 6};
      STW(((U4(qa) + 32u) & ~63u) | tq, qo);
      if (lane == 0) lds_inc(F_PUB);
      ST(14);
    }
    GET_TAGS();
    v4u kf[2][4], qf[4]; v4f dt[4];
    wait_cnt<2>(F_L, 3);
#pragma unroll
    for (int t = 0; t < 2; ++t)
#pragma unroll
      for (int s = 0; s < 4; ++s) kf[t][s] = lds_ld<v4u>(ka[s] + t * 16384);
    SB();
    wait_cnt<1>(F_Q, 1);
    ST(11);
#pragma unroll
    for (int s = 0; s < 4; ++s) qf[s] = lds_ld<v4u>(Q_OFF + (c & 1) * 256 + r * 16 + s * 64);
    SB();
#pragma unroll
    for (int t = 0; t < 2; ++t) dt[t] = mfma(qf[3], kf[t][3], mfma(qf[2], kf[t][2], mfma(qf[1], kf[t][1], mfma(qf[0], kf[t][0], zero))));
    SB();
    wait_cnt<0>(F_L, 4);
    ST(1);
#pragma unroll
    for (int t = 0; t < 2; ++t)
#pragma unroll
      for (int s = 0; s < 4; ++s) kf[t][s] = lds_ld<v4u>(ka[s] + (2 + t) * 16384);
    SB();
    if (lane == 0) lds_inc(F_KD);
#pragma unroll
    for (int t = 0; t < 2; ++t) dt[2 + t] = mfma(qf[3], kf[t][3], mfma(qf[2], kf[t][2], mfma(qf[1], kf[t][1], mfma(qf[0], kf[t][0], zero))));
    const bool r1 = r & 1, r2 = r & 2;
    float s0 = r2 ? (r1 ? dt[3][0] : dt[2][0]) : (r1 ? dt[1][0] : dt[0][0]);
    float s1 = r2 ? (r1 ? dt[3][1] : dt[2][1]) : (r1 ? dt[1][1] : dt[0][1]);
    s0 = rb(rb(s0) * 0.08838834764831845f); s1 = rb(rb(s1) * 0.08838834764831845f);
    {
      auto sw = __builtin_amdgcn_permlane32_swap(asu(s0), asu(s1), false, false);
      float m = fmaxf(asf(sw[0]), asf(sw[1]));
      auto s2 = __builtin_amdgcn_permlane16_swap(asu(m), asu(m), false, false);
      m = vmax(asf(s2[0]), asf(s2[1]));
      asm volatile("s_nop 1\n\tv_max_f32_dpp %0, %0, %0 quad_perm:[1,0,3,2] row_mask:0xf bank_mask:0xf\n\t"
                   "s_nop 1\n\tv_max_f32_dpp %0, %0, %0 quad_perm:[2,3,0,1] row_mask:0xf bank_mask:0xf\n\t"
                   "s_nop 1\n\tv_max_f32_dpp %0, %0, %0 row_half_mirror row_mask:0xf bank_mask:0xf\n\t"
                   "s_nop 1\n\tv_max_f32_dpp %0, %0, %0 row_ror:8 row_mask:0xf bank_mask:0xf" : "+v"(m));
      lds_st<unsigned>(MX_OFF + (lane >> 5) * 16 + w * 4, clean(m));
    }
    v4u ea, eb;
    wait_vals8<0>(MX_OFF, ea, eb);
    const float M0 = vmax(vmax(asf(ea.x), asf(ea.y)), vmax(asf(ea.z), asf(ea.w))), M1 = vmax(vmax(asf(eb.x), asf(eb.y)), vmax(asf(eb.z), asf(eb.w)));
    const unsigned pp = pk(__builtin_amdgcn_exp2f((s0 - M0) * 1.4426950408889634f), __builtin_amdgcn_exp2f((s1 - M1) * 1.4426950408889634f));
    lds_st<u16>(P_OFF + (64 * r + 16 * w + c) * 2, (u16)pp); lds_st<u16>(P_OFF + 512 + (64 * r + 16 * w + c) * 2, (u16)(pp >> 16));
    if (lane == 0) lds_inc(F_P);
    wait_cnt<0>(F_P, 4);
    v4u pf[8];
#pragma unroll
    for (int s = 0; s < 8; ++s) pf[s] = lds_ld<v4u>(P_OFF + (c & 1) * 512 + r * 16 + s * 64);
    if (w == 0) {
      const v4u ones = {0x3f803f80u, 0x3f803f80u, 0x3f803f80u, 0x3f803f80u};
      v4f ls = zero;
#pragma unroll
      for (int s = 0; s < 8; ++s) ls = mfma(pf[s], ones, ls);
      STW(tag4(v4f{M0, ls[0], M1, ls[1]}, T0, T1), oob(lane < 8 && TRUNC < 2, ST_OFF + (g * 16 + j) * 128 + lane * 16));
    }
    ST(2);
    v4f oa = zero, ob = zero;
#define PV(s0_, n_) { v4u fa[n_], fb[n_]; \
      _Pragma("unroll") for (int s = 0; s < n_; ++s) { fa[s] = tr8(va[0][0] + (s0_ + s) * 8192, va[0][1] + (s0_ + s) * 8192); fb[s] = tr8(va[1][0] + (s0_ + s) * 8192, va[1][1] + (s0_ + s) * 8192); } \
      SB(); _Pragma("unroll") for (int s = 0; s < n_; ++s) { oa = mfma(fa[s], pf[s0_ + s], oa); ob = mfma(fb[s], pf[s0_ + s], ob); } SB(); }
    wait_cnt<0>(F_L, 6); PV(0, 4)
    wait_cnt<0>(F_L, 7); PV(4, 2)
    wait_cnt<0>(F_L, 8); PV(6, 1)
    {
      v4u fa, fb;
#pragma nounroll
      for (unsigned sp = 0; sp < LSPIN_CAP; ++sp) {
        const unsigned f = lds_vld(F_L);
        CBAR();
        fa = tr8(va[0][0] + 7 * 8192, va[0][1] + 7 * 8192); fb = tr8(va[1][0] + 7 * 8192, va[1][1] + 7 * 8192);
        SB();
        if (__builtin_amdgcn_readfirstlane(f) >= 9u) break;
      }
      oa = mfma(fa, pf[7], oa); ob = mfma(fb, pf[7], ob);
    }
#undef PV
    STW(tag4((c & 2) ? ob : oa, T0, T1), TRUNC >= 2 ? opo | 0x40000000u : opo);
    ST(3);
  }

  if (TRUNC) {} else
  if (is_wo) {
    if (wave >= 4 && wave <= 7) {
      const int w = wave - 4;
      unsigned oo = OP_OFF + (g * 16 + 4 * r) * 1024 + (64 * w + 4 * c) * 4, ol = O_OFF + (64 * w + 4 * c + r) * 2;
      PIN(oo); PIN(ol);
      GET_TAGS();
      wait_cnt<1>(F_W, 2);
      ST(12);
      v4u t[4];
#pragma unroll
      for (int i = 0; i < 4; ++i) t[i] = LDW(oo, i * 1024);
      SB();
      wait_cnt<0>(F_GO, 1);
      v4f wg = lds_ld<v4f>(TT_OFF + (w >> 1) * 64 + r * 16);
      PIN(wg);
      SB();
      if (__builtin_expect(!ALL_OK(flip(t, T0, T1)), 0)) { flip(t, T0, T1); settle4<1024>(t, oo, ws, T0, T1); }
      const v4f a = (F4(t[0]) * wg[0] + F4(t[1]) * wg[1]) + (F4(t[2]) * wg[2] + F4(t[3]) * wg[3]);
      ST(4);
      const float o = t16(t32(a[0], a[2]), t32(a[1], a[3]));
      lds_st<u16>(ol, (u16)pk(o, o));
      ST(5);
      bar();
    } else if (wave >= 8 && wave <= 13) {
      const int t = wave - 8;
      unsigned ho = oob(c == 0, HP_OFF + (g * 576 + j * 96 + 16 * t + 4 * r) * 4), oa = O_OFF + r * 16;
      PIN(ho); PIN(oa);
      unsigned wa = W_OFF + (8 * r + q4) * 192 + t * 32 + p4 * 8;
      PIN(wa);
      wait_cnt<8>(F_W, 1);
      wait_cnt<2>(F_W, 2);
      GET_TAGS();
      v4u wf[8];
      constexpr int S1 = 8 - (WOEARLY + 5) / 6;
#pragma unroll
      for (int s = 0; s < S1; ++s) wf[s] = tr8(wa + s * 6144, wa + s * 6144 + 4 * 192);
      wait_cnt<0>(F_W, 3);
#pragma unroll
      for (int s = S1; s < 8; ++s) wf[s] = tr8(wa + s * 6144, wa + s * 6144 + 4 * 192);
#pragma unroll
      for (int s = 0; s < 8; ++s) PIN(wf[s]);
      ST(5);
      bar();
      v4u of[8];
#pragma unroll
      for (int s = 0; s < 8; ++s) of[s] = lds_ld<v4u>(oa + s * 64);
      SB();
      v4f acc = zero;
#pragma unroll
      for (int s = 0; s < 8; ++s) acc = mfma(wf[s], of[s], acc);
      STW(tag4(acc, T0, T1), ho);
      STE(6);
    } else {
      if (wave == 1) {
        wait_cnt<2>(F_W, 1);
        GET_TAGS();
        STW((v4u{T0, now32() + CW1, T1, 0u}), oob(lane < (j == 5 ? 3 : 1), H1_OFF + g * 128 + (j + lane) * 16));
        v4f st;
#pragma nounroll
        for (unsigned spin = 0; spin < SPIN_CAP; ++spin) {
          unsigned bad = 0;
          CBAR();
          st = untag4(LDW(ST_OFF + (g * 16 + c) * 128, 0), T0, T1, bad);
          if (ALL_OK(bad)) break;
          __builtin_amdgcn_s_sleep(4);
        }
        const float mp = (r & 1) ? st[2] : st[0], lp = (r & 1) ? st[3] : st[1];
        float wgt = __builtin_amdgcn_exp2f((mp - row_max(mp)) * 1.4426950408889634f);
        wgt *= __builtin_amdgcn_rcpf(row_sum(wgt * lp));
        lds_st<float>(TT_OFF + lane * 4, wgt);
        lds_vst(F_GO, 1u);
        ST(4);
        unsigned h2o = HCOMPACT ? oob(lane < (j == 5 ? 3 : 1), H2_OFF + g * 128 + (j + lane) * 16) : oob(lane < 8, H2_OFF + (g * 6 + j) * 128 + lane * 16);
        PIN(h2o);
        wait_cnt<0>(F_W, 2);
        const unsigned due = now32() + CWO;
        STW((v4u{T0, due, T1, 0u}), h2o);
      }
      bar();
    }
  } else if (is_mlp) {
    v4u w2f[9]; float b1v = 0.f;
    unsigned za = Z_OFF + (c >> 3) * 128 + (8 * r + (c & 7)) * 4, mo = oob(p4 < 2 || c == 2, MPT ? MP_OFF + (9 * (wave - 8) + 4 * p4 + q4) * 9216 + u * 64 + r * 16 : MP_OFF + u * 2304 + (9 * (wave - 8) + 4 * p4 + q4) * 64 + r * 16);
    PIN(za); PIN(mo);
    const bool second = wave >= 8 && wave <= 11;
    if (second) { b1v = lo16(((const GAS u16*)p.b1)[32 * u + 8 * r + (c & 7)]); PIN(b1v); }
    wait_cnt<8>(F_L, 9);
    GET_TAGS();
    const int ch = wave >= 12 ? (wave == 14 ? 8 : wave - 12) : wave;
    unsigned w2a = W2_OFF + (8 * r + q4) * 1152 + 9 * (wave - 8) * 32 + p4 * 8;
    PIN(w2a);
    bool have = false;
    if (second) {
      wait_cnt<4>(F_W, 1);
#pragma nounroll
      for (unsigned sp = 0; sp < LSPIN_CAP; ++sp) {
        const v2u f = *(const volatile LAS v2u*)(size_t)F_W;
        have = __builtin_amdgcn_readfirstlane(f.x) >= 3u;
        if (have || __builtin_amdgcn_readfirstlane(f.y) >= 9u) break;
        __builtin_amdgcn_s_sleep(1);
      }
      CBAR();
      if (have) {
#pragma unroll
        for (int t = 0; t < 9; ++t) w2f[t] = tr8(w2a + t * 32, w2a + t * 32 + 4 * 1152);
      }
    } else if (wave == 1) {
      unsigned hq[2] = {HCOMPACT ? oob((lane & 7) < 6, H2_OFF + lane * 16) : H2_OFF + lane * 128, HCOMPACT ? oob((lane & 7) < 6, H2_OFF + 1024 + lane * 16) : oob(lane < 32, H2_OFF + (64 + lane) * 128)};
      unsigned h3o = oob(lane < 8, H3_OFF + u * 128 + lane * 16);
      PIN(h3o);
      unsigned h1[2] = {oob((lane & 7) < 6, H1_OFF + lane * 16), oob((lane & 7) < 6, H1_OFF + 1024 + lane * 16)}, tm[2], tn[2];
      __builtin_amdgcn_s_sleep(HWAIT);
      const unsigned rough = latest<2, SLR>(h1, ws, T0, tm);
      unsigned ta;
      do ta = now32(); while ((int)(ta - (rough - LEAD)) < 0);
      const u64 l0 = __builtin_amdgcn_ballot_w64((int)(tm[0] + MARG - ta) > 0), l1 = __builtin_amdgcn_ballot_w64((int)(tm[1] + MARG - ta) > 0);
      lds_st<v4u>(TT_OFF, v4u{(unsigned)l0, (unsigned)(l0 >> 32), (unsigned)l1, (unsigned)(l1 >> 32)});
      lds_vst(F_GO, 1u);
      const unsigned due = latest<2, SLH>(hq, ws, T0, tn);
      ST(12);
      ta = now32();
      const bool late = (int)(due - ta) <= 0;
      if (late) lds_vst(F_GO, 2u);
      const unsigned all = (late ? ta : due) + DELTA3;
      bool sent = false;
      for (unsigned sp = 0; sp < LSPIN_CAP; ++sp) {
        const bool out = __builtin_amdgcn_readfirstlane(lds_vld(F_W)) >= 2u, now = (int)(now32() - due) >= 0;
        if (now) lds_vst(F_GO, 2u);
        if (out && !sent) {
          const unsigned own = __builtin_amdgcn_readfirstlane(lds_vld(F_T)) + DELTAW;
          STW((v4u{T0, (int)(own - all) > 0 ? own : all, T1, 0u}), h3o);
          sent = true;
        }
        if (now && sent) break;
      }
    } else if (wave != 0) {
      unsigned ho = HP_OFF + (r * 576 + 64 * ch + 4 * c) * 4, zw = Z_OFF + ch * 256 + r * 16, sw = S_OFF + r * 128 + ch * 8;
      unsigned hs_o = oob(u == 0, MP_OFF + 144 * 2304 + (64 * ch + 4 * c + r) * 4);
      PIN(ho); PIN(zw); PIN(sw); PIN(hs_o);
      wait_cnt<2>(F_W, 1);
      v4u w1f[2][2];
#pragma unroll
      for (int s = 0; s < 2; ++s)
#pragma unroll
        for (int t = 0; t < 2; ++t) { const unsigned a = W1_OFF + (64 * ch + 4 * (8 * s + q4) + r) * 64 + t * 32 + p4 * 8; w1f[s][t] = tr8(a, a + 16 * 64); }
      float xv = lds_ld<float>(X_OFF + (64 * ch + 4 * c + r) * 4);
      const v4u ones = {0x3f803f80u, 0x3f803f80u, 0x3f803f80u, 0x3f803f80u};
#pragma unroll
      for (int t = 0; t < 2; ++t) {
        const v4f cs = mfma(w1f[1][t], ones, mfma(w1f[0][t], ones, zero));
        if (c == 0) lds_st<v4f>(zw + 128 + t * 64, cs);
      }
      PIN(xv);
      wait_cnt<1>(F_GO, 1);
      const v4u ny = lds_ld<v4u>(TT_OFF);
      const u64 ma[4] = {__builtin_amdgcn_ballot_w64(((ny[0] >> (8 * r)) & 255u) == 0u), __builtin_amdgcn_ballot_w64(((ny[1] >> (8 * r)) & 255u) == 0u),
                         __builtin_amdgcn_ballot_w64(((ny[2] >> (8 * r)) & 255u) == 0u), __builtin_amdgcn_ballot_w64(((ny[3] >> (8 * r)) & 255u) == 0u)};
      ST(5);
      v4u hv[4];
      two_batches<4 * 2304>(hv, ma, ho, ws);
      if (__builtin_expect(!ALL_OK(flip(hv, T0, T1)), 0)) { flip(hv, T0, T1); settle4<4 * 2304>(hv, ho, ws, T0, T1); }
      const v4f a = (F4(hv[0]) + F4(hv[1])) + (F4(hv[2]) + F4(hv[3]));
      const float at = t16(t32(a[0], a[2]), t32(a[1], a[3]));
      ST(6);
      const float hs = xv + rb(at);
      const unsigned pr = pk(hs, dpp<XOR1>(hs));
      const v4u f0 = {dppu<NEWB + 0>(pr), dppu<NEWB + 2>(pr), dppu<NEWB + 4>(pr), dppu<NEWB + 6>(pr)};
      const v4u f1 = {dppu<NEWB + 8>(pr), dppu<NEWB + 10>(pr), dppu<NEWB + 12>(pr), dppu<NEWB + 14>(pr)};
      const v4f z0 = mfma(w1f[1][0], f1, mfma(w1f[0][0], f0, zero)), z1 = mfma(w1f[1][1], f1, mfma(w1f[0][1], f0, zero));
      const v4f sq = mfma(c == 1 ? ones : f1, f1, mfma(c == 1 ? ones : f0, f0, zero));
      if (c == 0) { lds_st<v4f>(zw, z0); lds_st<v4f>(zw + 64, z1); lds_st<v2f>(sw, v2f{sq[0], sq[1]}); }
      if (lane == 0) lds_inc(F_Z);
      __builtin_amdgcn_raw_buffer_store_b32((pr << 16) | T0, ws, hs_o, 0, AUX_SC1);
    }
    if (second) {
      v4u g0 = {0u, 0u, 0u, 0u}, g1 = g0, g2 = g0, g3 = g0;
      PIN(g0); PIN(g1); PIN(g2); PIN(g3);
      wait_cnt<2>(F_Z, 1);
      wait_cnt<0>(F_Z, 9);
      ST(11);
      const v4f sa = lds_ld<v4f>(S_OFF), sb = lds_ld<v4f>(S_OFF + 16), sc = lds_ld<v4f>(S_OFF + 32), sd = lds_ld<v4f>(S_OFF + 48);
      const v2f se = lds_ld<v2f>(S_OFF + 64);
      float zz[9];
#pragma unroll
      for (int i = 0; i < 9; ++i) zz[i] = lds_ld<float>(za + i * 256);
      SB();
      const v4f s4 = (sa + sb) + (sc + sd);
      const float mean = ((s4[1] + s4[3]) + se[1]) * (1.f / D), var = fmaxf(((s4[0] + s4[2]) + se[0]) * (1.f / D) - mean * mean, 0.f);
      const float rr = __builtin_amdgcn_rsqf(fmaf(var, 1.f + 1e-5f, 1e-10f));
      const v2f zp = (v2f{zz[0], zz[1]} + v2f{zz[2], zz[3]}) + (v2f{zz[4], zz[5]} + v2f{zz[6], zz[7]});
      const float t = (zp[0] + zz[8]) + zp[1];
      const float z = (t - mean * dpp<XOR8>(t)) * rr;
      const float zb = rb(rb(z) + b1v);
      const float e = __builtin_amdgcn_exp2f(zb * fmaf(zb * zb, 2.302208198144325f * 0.044715f, 2.302208198144325f));
      const float gl = zb - zb * __builtin_amdgcn_rcpf(e + 1.f);
      const unsigned gp = pk(gl, dpp<XOR1>(gl));
      if (!have) {
        wait_cnt<0>(F_W, 3);
#pragma unroll
        for (int t = 0; t < 9; ++t) w2f[t] = tr8(w2a + t * 32, w2a + t * 32 + 4 * 1152);
      }
#define GB(G_, bk) _Pragma("unroll") for (int k = 0; k < 1; ++k) { \
        G_[0] = __builtin_amdgcn_update_dpp(G_[0], gp, NEWB + 0, 0xf, 1 << (bk), false); G_[1] = __builtin_amdgcn_update_dpp(G_[1], gp, NEWB + 2, 0xf, 1 << (bk), false); \
        G_[2] = __builtin_amdgcn_update_dpp(G_[2], gp, NEWB + 4, 0xf, 1 << (bk), false); G_[3] = __builtin_amdgcn_update_dpp(G_[3], gp, NEWB + 6, 0xf, 1 << (bk), false); }
      GB(g0, 0) GB(g1, 1) GB(g2, 2) GB(g3, 3)
#undef GB
      const v4f o2 = mfma(w2f[8], g0, zero);
      const v4f o0 = mfma(w2f[3], g3, mfma(w2f[2], g2, mfma(w2f[1], g1, mfma(w2f[0], g0, zero))));
      const v4f o1 = mfma(w2f[7], g3, mfma(w2f[6], g2, mfma(w2f[5], g1, mfma(w2f[4], g0, zero))));
      v4f o;
#pragma unroll
      for (int k = 0; k < 4; ++k) { const float a0 = o0[k], a1 = o1[k], a2 = o2[k]; const float lo_ = (c & 2) ? a2 : a0; o[k] = (c & 1) ? a1 : lo_; }
      STW(tag4(o, T0, T1), mo);
      STE(8);
    }
  }
  if (!TRUNC && red < 36 && wave == 1) {
    const int c4 = lane & 3, col = 16 * red + 4 * c4 + r;
    float b2v = lo16(((const GAS u16*)p.b2)[col]);
    PIN(b2v);
    wait_cnt<8>(F_L, 9);
    GET_TAGS();
    unsigned hu, mo = MPT ? MP_OFF + red * 9216 + lane * 16 : MP_OFF + (lane >> 2) * 2304 + red * 64 + c4 * 16;
    unsigned hro = MP_OFF + 144 * 2304 + col * 4, oo = col * 2;
    PIN(mo); PIN(hro); PIN(oo);
    {
      unsigned hq[3];
#pragma unroll
      for (int i = 0; i < 3; ++i) hq[i] = oob(i * 64 + lane < 144, H3_OFF + (i * 64 + lane) * 128);
      if (j == 15) __builtin_amdgcn_s_sleep(64);
      unsigned tm[3];
      const unsigned go = latest<3, SL3>(hq, ws, T0, tm);
      ST(6);
      while ((int)(now32() - go) < 0) {}
    }
    constexpr int MS = MPT ? 1024 : 16 * 2304;
    hu = __builtin_amdgcn_raw_buffer_load_b32(ws, hro, 0, AUX_SC1);
    SB();
    v4u mv[9];
#pragma unroll
    for (int i = 0; i < 9; ++i) { mv[i] = LDW(mo + (MPT ? (i & 3) * MS : 0), (MPT ? (i & ~3) : i) * MS); SB(); }
    hu ^= T0;
    unsigned bad = hu;
    v4f a = untag4(mv[0], T0, T1, bad);
#pragma unroll
    for (int i = 1; i < 9; ++i) { a += untag4(mv[i], T0, T1, bad); PIN(a); SB(); }
    if (__builtin_expect(!ALL_OK(bad), 0)) {
      gather9<MS>(mv, mo, ws, T0, T1);
#pragma nounroll
      for (unsigned spin = 0; spin < SPIN_CAP && !ALL_OK(hu); ++spin) { CBAR(); hu = __builtin_amdgcn_raw_buffer_load_b32(ws, hro, 0, AUX_SC1) ^ T0; }
      a = F4(mv[0]);
#pragma unroll
      for (int i = 1; i < 9; ++i) a += F4(mv[i]);
    }
    float t = t16(t32(a[0], a[2]), t32(a[1], a[3]));
    t += dpp<XOR8>(t); t += dpp<XOR7>(dpp<XOR3>(t));
    const float hf = asf(hu);
    const float res = hf + rb(rb(t) + b2v);
    *(GAS u16*)((GAS char*)p.out + oo) = (u16)pk(res, res);
    if (red == 0 && lane == 0) __builtin_amdgcn_raw_buffer_store_b32(T0 | (T1 << 16), ws, EP_OFF, 0, AUX_SC1);
    STE(9);
  }
}

extern "C" void kernel_launch(void* const* d_in, const int* in_sizes, int n_in, void* d_out, int out_size, void* d_ws, size_t ws_size, hipStream_t stream) {
  Params p;
  p.x = (const u16*)d_in[0]; p.k = (const u16*)d_in[1]; p.v = (const u16*)d_in[2]; p.wq = (const u16*)d_in[3]; p.wo = (const u16*)d_in[4];
  p.w1 = (const u16*)d_in[6]; p.b1 = (const u16*)d_in[7]; p.w2 = (const u16*)d_in[8]; p.b2 = (const u16*)d_in[9];
  p.out = (u16*)d_out; p.ws = (char*)d_ws + WSPOS(ws_size);
  hipLaunchKernelGGL(fused, dim3(NBLK), dim3(NTHR), 0, stream, p);
}
```

```cpp
#define CWO 240
#define DELTA3 190
#define CODEB 17920
#include <hip/hip_runtime.h>
#include <hip/hip_bf16.h>

typedef unsigned short u16;
typedef unsigned long long u64;
typedef unsigned v4u __attribute__((ext_vector_type(4)));
typedef unsigned v2u __attribute__((ext_vector_type(2)));
typedef float v4f __attribute__((ext_vector_type(4)));
typedef float v2f __attribute__((ext_vector_type(2)));
typedef short v4s __attribute__((ext_vector_type(4)));
typedef __bf16 bf16x2 __attribute__((ext_vector_type(2)));
typedef __bf16 bf16x8 __attribute__((ext_vector_type(8)));
#define GAS __attribute__((address_space(1)))
#define LAS __attribute__((address_space(3)))
#define STAMP 0
#define WSPOS(sz) 0
#define L2WARM 1
#define MPT 1
#define DEEP 1
#define DBD 32
#define DCD 32
#define DV DC
#define DA 20
#define DB 24
#define DC 28
#define SL1 2
#define SL2 3
#define SL3 6
#define DE 25
#define DF 24
#define PRED 0
#define HCOMPACT 1
#define AHEAD 60
#define MARG 40
#define SLH 10
#define WOEARLY 12
#define HEARLY 20
#define TRUNC 0
#define QSTAG 16
#define DELTAW 120
#define HWAIT 40
#define QDELAY 16
#define WTHR_WO 0
#define WTHR_MLP 0

constexpr int D = 576, SK = 65536, DH = 128, WIN = 4096, FF = 4608, QW = 4096;
constexpr int NWAVE = 15, NTHR = NWAVE * 64, NBLK = 256;
constexpr unsigned EP_OFF = 0, QP_OFF = 4096, OP_OFF = QP_OFF + 16 * 4 * 256 * 4, ST_OFF = OP_OFF + 16 * 16 * 256 * 4,
                   HP_OFF = ST_OFF + 256 * 128, MP_OFF = HP_OFF + 16 * 576 * 4, H2_OFF = MP_OFF + 145 * 576 * 4, H3_OFF = H2_OFF + 96 * 128, WS_END = H3_OFF + 144 * 128, DBG_OFF = 1u << 20;
constexpr unsigned SENT = 0xFFFFFFFFu;
constexpr unsigned SPIN_CAP = 1u << 16, LSPIN_CAP = 1u << 22;
constexpr int AUX_SC1 = 16;
constexpr unsigned CODE_BYTES = CODEB;
constexpr unsigned F_L = 0, F_I = 4, F_W = 8, F_Z = 12, F_Q = 40, F_P = 16, F_KD = 20, F_PUB = 24, F_Y = 28, F_GO = 32, F_T = 36, MX_OFF = 64, TG_OFF = 96,
                   Q_OFF = 128, P_OFF = Q_OFF + 512, TT_OFF = P_OFF + 1024, O_OFF = TT_OFF + 512, S_OFF = O_OFF + 512, X_OFF = S_OFF + 512,
                   Z_OFF = X_OFF + 2304, M_END = Z_OFF + 2304;
constexpr unsigned W_OFF = 8192, K_OFF = W_OFF + 24576, V_OFF = K_OFF + 65536, LDS_BYTES = V_OFF + 65536;
static_assert(M_END <= W_OFF && LDS_BYTES <= 160 * 1024, "LDS");
constexpr unsigned W1_OFF = W_OFF, W2_OFF = W_OFF + 36864;

struct Params { const u16 *x, *k, *v, *wq, *wo, *w1, *b1, *w2, *b2; u16* out; char* ws; };

__device__ __forceinline__ float asf(unsigned u) { return __builtin_bit_cast(float, u); }
__device__ __forceinline__ unsigned asu(float f) { return __builtin_bit_cast(unsigned, f); }
__device__ __forceinline__ float lo16(unsigned u) { return asf(u << 16); }
__device__ __forceinline__ float hi16(unsigned u) { return asf(u & 0xffff0000u); }
__device__ __forceinline__ unsigned pk(float lo, float hi) { bf16x2 r; r.x = (__bf16)lo; r.y = (__bf16)hi; return __builtin_bit_cast(unsigned, r); }
__device__ __forceinline__ float rb(float f) { return lo16(pk(f, f)); }
__device__ __forceinline__ float dot2(unsigned a, unsigned b, float c) {
  return __builtin_amdgcn_fdot2_f32_bf16(__builtin_bit_cast(bf16x2, a), __builtin_bit_cast(bf16x2, b), c, false);
}
__device__ __forceinline__ v4f mfma(v4u a, v4u b, v4f c) {
  return __builtin_amdgcn_mfma_f32_16x16x32_bf16(__builtin_bit_cast(bf16x8, a), __builtin_bit_cast(bf16x8, b), c, 0, 0, 0);
}
template <int CTRL> __device__ __forceinline__ float dpp(float x) { return asf(__builtin_amdgcn_mov_dpp(asu(x), CTRL, 0xf, 0xf, true)); }
template <int CTRL> __device__ __forceinline__ unsigned dppu(unsigned x) { return __builtin_amdgcn_mov_dpp(x, CTRL, 0xf, 0xf, true); }
constexpr int XOR1 = 0xB1, XOR2 = 0x4E, XOR7 = 0x141, XOR8 = 0x128, XOR3 = 0x1B, NEWB = 0x150;
__device__ __forceinline__ float vmax(float a, float b) { float r; asm("v_max_f32 %0, %1, %2" : "=v"(r) : "v"(a), "v"(b)); return r; }
__device__ __forceinline__ float x16_sum(float x) { auto s = __builtin_amdgcn_permlane16_swap(asu(x), asu(x), false, false); return asf(s[0]) + asf(s[1]); }
__device__ __forceinline__ float x32_sum(float x) { auto s = __builtin_amdgcn_permlane32_swap(asu(x), asu(x), false, false); return asf(s[0]) + asf(s[1]); }
__device__ __forceinline__ float x16_max(float x) { auto s = __builtin_amdgcn_permlane16_swap(asu(x), asu(x), false, false); return fmaxf(asf(s[0]), asf(s[1])); }
__device__ __forceinline__ float x32_max(float x) { auto s = __builtin_amdgcn_permlane32_swap(asu(x), asu(x), false, false); return fmaxf(asf(s[0]), asf(s[1])); }
__device__ __forceinline__ float t32(float a, float b) { auto s = __builtin_amdgcn_permlane32_swap(asu(a), asu(b), false, false); return asf(s[0]) + asf(s[1]); }
__device__ __forceinline__ float t16(float a, float b) { auto s = __builtin_amdgcn_permlane16_swap(asu(a), asu(b), false, false); return asf(s[0]) + asf(s[1]); }
template <int CTRL> __device__ __forceinline__ float tdpp(float a, float b, bool hi) { const float s = hi ? a : b, k = hi ? b : a; return k + dpp<CTRL>(s); }
__device__ __forceinline__ float row_sum(float x) { x += dpp<XOR1>(x); x += dpp<XOR2>(x); x += dpp<XOR7>(x); x += dpp<XOR8>(x); return x; }
__device__ __forceinline__ float row_max(float x) { x = fmaxf(x, dpp<XOR1>(x)); x = fmaxf(x, dpp<XOR2>(x)); x = fmaxf(x, dpp<XOR7>(x)); x = fmaxf(x, dpp<XOR8>(x)); return x; }
__device__ __forceinline__ float wave_sum(float x) { return x32_sum(x16_sum(row_sum(x))); }
__device__ __forceinline__ float wave_max(float x) { return x32_max(x16_max(row_max(x))); }
__device__ __forceinline__ v4u ldnt4(const void* p) { return __builtin_nontemporal_load((const GAS v4u*)p); }
__device__ __forceinline__ unsigned clean(float f) { const unsigned u = asu(f); return u < SENT ? u : SENT - 1u; }
__device__ __forceinline__ bool has_sent(v4u v) { return max(max(v.x, v.y), max(v.z, v.w)) == SENT; }
#define DRAIN() asm volatile("s_waitcnt vmcnt(0)" ::: "memory")
#define CBAR() asm volatile("" ::: "memory")
#define LGKM0() asm volatile("s_waitcnt lgkmcnt(0)" ::: "memory")
#define SB() __builtin_amdgcn_sched_barrier(0)
__device__ __forceinline__ void bar() { LGKM0(); __builtin_amdgcn_s_barrier(); CBAR(); }
template <typename T> __device__ __forceinline__ T lds_ld(unsigned off) { return *(const LAS T*)(size_t)off; }
template <typename T> __device__ __forceinline__ void lds_st(unsigned off, T v) { *(LAS T*)(size_t)off = v; }
__device__ __forceinline__ v4u lds_vld4(unsigned off) { return *(const volatile LAS v4u*)(size_t)off; }
__device__ __forceinline__ unsigned lds_vld(unsigned off) { return *(const volatile LAS unsigned*)(size_t)off; }
__device__ __forceinline__ void lds_vst(unsigned off, unsigned v) { *(volatile LAS unsigned*)(size_t)off = v; }
__device__ __forceinline__ v4u tr8(unsigned a0, unsigned a1) {
  const v2u x = __builtin_bit_cast(v2u, __builtin_amdgcn_ds_read_tr16_b64_v4i16((LAS v4s*)(size_t)a0));
  const v2u y = __builtin_bit_cast(v2u, __builtin_amdgcn_ds_read_tr16_b64_v4i16((LAS v4s*)(size_t)a1));
  return v4u{x.x, x.y, y.x, y.y};
}

#define PIN(x) asm volatile("" : "+v"(x))
__device__ __forceinline__ unsigned oob(bool keep, unsigned off) { PIN(off); return keep ? off : WS_END; }
template <int SLEEP> __device__ __forceinline__ void wait_cnt(unsigned off, unsigned n) {
#pragma nounroll
  for (unsigned s = 0; s < LSPIN_CAP; ++s) { if (__builtin_amdgcn_readfirstlane(lds_vld(off)) >= n) break; if (SLEEP) __builtin_amdgcn_s_sleep(SLEEP); }
  CBAR();
}
template <int SLEEP> __device__ __forceinline__ void wait_vals8(unsigned off, v4u& a, v4u& b) {
#pragma nounroll
  for (unsigned s = 0; s < LSPIN_CAP; ++s) {
    a = lds_vld4(off); b = lds_vld4(off + 16);
    if (__builtin_amdgcn_readfirstlane(max(max(max(a.x, a.y), max(a.z, a.w)), max(max(b.x, b.y), max(b.z, b.w)))) != SENT) break;
    if (SLEEP) __builtin_amdgcn_s_sleep(SLEEP);
  }
  CBAR();
}
#define lds_inc(off) asm volatile("ds_add_u32 %0, %1 offset:%2" :: "v"(0u), "v"(1u), "i"(off) : "memory")
#define LOOP_END(n) "s_sub_u32 " n ", " n ", 1\n\ts_cmp_lg_u32 " n ", 0\n\ts_cbranch_scc1 1b"
template <int N, int DEP> __device__ __forceinline__ void dma1(unsigned& voff, unsigned lds, const void* base, unsigned gstep) {
  static_assert(N % 2 == 0, "");
  unsigned n = N / 2;
  asm volatile("s_mov_b32 m0, %2\n\t1:\n\t.rept 2\n\ts_waitcnt vmcnt(%5)\n\tglobal_load_lds_dwordx4 %0, %3 nt\n\ts_add_u32 m0, m0, 0x400\n\tv_add_u32 %0, %4, %0\n\t.endr\n\t" LOOP_END("%1")
               : "+v"(voff), "+s"(n) : "s"(lds), "s"(base), "s"(gstep), "i"(DEP - 1) : "memory", "scc");
}
template <int NG, int DEP> __device__ __forceinline__ void dma4(unsigned (&v)[4], unsigned lds, const void* base) {
  unsigned n = NG;
  if (NG > 0)
    asm volatile("s_mov_b32 m0, %5\n\t1:\n\t"
                 "s_waitcnt vmcnt(%7)\n\tglobal_load_lds_dwordx4 %0, %6 nt\n\tv_add_u32 %0, 0x1000, %0\n\t"
                 "s_waitcnt vmcnt(%7)\n\tglobal_load_lds_dwordx4 %1, %6 offset:1024 nt\n\tv_add_u32 %1, 0x1000, %1\n\t"
                 "s_waitcnt vmcnt(%7)\n\tglobal_load_lds_dwordx4 %2, %6 offset:2048 nt\n\tv_add_u32 %2, 0x1000, %2\n\t"
                 "s_waitcnt vmcnt(%7)\n\tglobal_load_lds_dwordx4 %3, %6 offset:3072 nt\n\tv_add_u32 %3, 0x1000, %3\n\t"
                 "s_add_u32 m0, m0, 0x1000\n\t" LOOP_END("%4")
                 : "+v"(v[0]), "+v"(v[1]), "+v"(v[2]), "+v"(v[3]), "+s"(n) : "s"(lds), "s"(base), "i"(DEP - 1) : "memory", "scc");
}
template <int NG, int DEP> __device__ __forceinline__ void dma3(unsigned (&v)[3], unsigned lds, const void* base, unsigned gstep) {
  unsigned n = NG;
  asm volatile("s_mov_b32 m0, %4\n\t1:\n\t"
               "s_waitcnt vmcnt(%7)\n\tglobal_load_lds_dwordx4 %0, %5 nt\n\ts_add_u32 m0, m0, 0x400\n\tv_add_u32 %0, %6, %0\n\t"
               "s_waitcnt vmcnt(%7)\n\tglobal_load_lds_dwordx4 %1, %5 nt\n\ts_add_u32 m0, m0, 0x400\n\tv_add_u32 %1, %6, %1\n\t"
               "s_waitcnt vmcnt(%7)\n\tglobal_load_lds_dwordx4 %2, %5 nt\n\ts_add_u32 m0, m0, 0x400\n\tv_add_u32 %2, %6, %2\n\t" LOOP_END("%3")
               : "+v"(v[0]), "+v"(v[1]), "+v"(v[2]), "+s"(n) : "s"(lds), "s"(base), "s"(gstep), "i"(DEP - 1) : "memory", "scc");
}
template <int N, int A2, int STEP> __device__ __forceinline__ void ramp1(unsigned& voff, unsigned lds, const void* base, unsigned gstep) {
  asm volatile("s_mov_b32 m0, %1\n\t.set rk_, %4\n\t.rept %6\n\ts_waitcnt vmcnt(rk_ / 2)\n\tglobal_load_lds_dwordx4 %0, %2 nt\n\ts_add_u32 m0, m0, 0x400\n\tv_add_u32 %0, %3, %0\n\t"
               ".set rk_, rk_ + (%5)\n\t.endr" : "+v"(voff) : "s"(lds), "s"(base), "s"(gstep), "i"(A2), "i"(STEP), "i"(N) : "memory", "scc");
}
template <int NG, int A> __device__ __forceinline__ void ramp3(unsigned (&v)[3], unsigned lds, const void* base, unsigned gstep) {
  asm volatile("s_mov_b32 m0, %3\n\t.set rk_, %6\n\t.rept %7\n\ts_waitcnt vmcnt(rk_)\n\t"
               "global_load_lds_dwordx4 %0, %4 nt\n\ts_add_u32 m0, m0, 0x400\n\tv_add_u32 %0, %5, %0\n\t"
               "global_load_lds_dwordx4 %1, %4 nt\n\ts_add_u32 m0, m0, 0x400\n\tv_add_u32 %1, %5, %1\n\t"
               "global_load_lds_dwordx4 %2, %4 nt\n\ts_add_u32 m0, m0, 0x400\n\tv_add_u32 %2, %5, %2\n\t.set rk_, rk_ - 1\n\t.endr"
               : "+v"(v[0]), "+v"(v[1]), "+v"(v[2]) : "s"(lds), "s"(base), "s"(gstep), "i"(A), "i"(NG) : "memory", "scc");
}
__device__ __forceinline__ unsigned now32() { return (unsigned)__builtin_amdgcn_s_memrealtime(); }
#define VMW(n) asm volatile("s_waitcnt vmcnt(%0)" :: "i"(n) : "memory")

#define ST(i) do {} while (0)
#define STE(i) do {} while (0)
#define LDW(off, soff) __builtin_amdgcn_raw_buffer_load_b128(ws, off, soff, AUX_SC1)
#define STW(v, off) __builtin_amdgcn_raw_buffer_store_b128(v, ws, off, 0, AUX_SC1)
__device__ __forceinline__ v4u tag4(v4f v, unsigned T0, unsigned T1) {
  unsigned m = 0xfffff000u; asm("" : "+v"(m));
  return v4u{(asu(v[0]) & m) | T0, asu(v[1]), (asu(v[2]) & m) | T1, asu(v[3])};
}
__device__ __forceinline__ v4f untag4(v4u v, unsigned T0, unsigned T1, unsigned& bad) {
  const unsigned a = v.x ^ T0, c = v.z ^ T1; bad |= a | c;
  return v4f{asf(a), asf(v.y), asf(c), asf(v.w)};
}
template <int N> __device__ __forceinline__ unsigned flip(v4u (&t)[N], unsigned T0, unsigned T1) {
  unsigned bad = 0;
#pragma unroll
  for (int i = 0; i < N; ++i) { t[i].x ^= T0; t[i].z ^= T1; asm("" : "+v"(t[i])); bad |= t[i].x | t[i].z; }
  return bad;
}
#define ALL_OK(bad) (!__any(((bad) & 0xfffu) != 0u))
#define GET_TAGS() const v2u tg_ = lds_ld<v2u>(TG_OFF); const unsigned T0 = __builtin_amdgcn_readfirstlane(tg_.x), T1 = __builtin_amdgcn_readfirstlane(tg_.y)
#define CHK(x, z, m, T0, T1) "s_mov_b64 exec, " m "\n\tv_xor_b32 " x ", " T0 ", " x "\n\tv_xor_b32 " z ", " T1 ", " z "\n\tv_or_b32 v83, " x ", " z "\n\tv_and_b32 v83, 0xfff, v83\n\tv_cmp_ne_u32 " m ", 0, v83\n\t"
#define ASK(t, m, off, rs, so) "s_mov_b64 exec, " m "\n\ts_mov_b32 s59, " so "\n\tbuffer_load_dwordx4 " t ", " off ", " rs ", s59 offen sc1\n\t"
#define SETTLE4(off, rs, T0, T1, S1, S2, S3) \
  "s_mov_b64 s[60:61], -1\n\ts_mov_b64 s[62:63], -1\n\ts_mov_b64 s[64:65], -1\n\ts_mov_b64 s[66:67], -1\n\ts_mov_b32 s58, 0x10000\n\t" \
  "2:\n\ts_waitcnt vmcnt(0)\n\t" \
  CHK("v84", "v86", "s[60:61]", T0, T1) CHK("v88", "v90", "s[62:63]", T0, T1) CHK("v92", "v94", "s[64:65]", T0, T1) CHK("v96", "v98", "s[66:67]", T0, T1) \
  "s_or_b64 s[56:57], s[60:61], s[62:63]\n\ts_or_b64 s[56:57], s[56:57], s[64:65]\n\ts_or_b64 s[56:57], s[56:57], s[66:67]\n\ts_cbranch_scc0 3f\n\t" \
  "s_sub_u32 s58, s58, 1\n\ts_cbranch_scc1 3f\n\ts_sleep 6\n\t" \
  ASK("v[84:87]", "s[60:61]", off, rs, "0") ASK("v[88:91]", "s[62:63]", off, rs, S1) ASK("v[92:95]", "s[64:65]", off, rs, S2) ASK("v[96:99]", "s[66:67]", off, rs, S3) \
  "s_branch 2b\n\t3:\n\ts_mov_b64 exec, -1"
#define CLOB4 "memory", "scc", "vcc", "v83", "s56", "s57", "s58", "s59", "s60", "s61", "s62", "s63", "s64", "s65", "s66", "s67"
template <int STEP> __device__ __forceinline__ void settle4(v4u (&t)[4], unsigned off, __amdgpu_buffer_rsrc_t ws, unsigned T0, unsigned T1) {
  asm volatile(SETTLE4("%4", "%5", "%6", "%7", "%8", "%9", "%10")
               : "+{v[84:87]}"(t[0]), "+{v[88:91]}"(t[1]), "+{v[92:95]}"(t[2]), "+{v[96:99]}"(t[3])
               : "v"(off), "s"(ws), "s"(T0), "s"(T1), "i"(STEP), "i"(2 * STEP), "i"(3 * STEP) : CLOB4);
}
#define ASK2(t, m, so) "s_andn2_b64 exec, -1, " m "\n\ts_cbranch_execz 4f\n\tbuffer_load_dwordx4 " t ", %4, %5, " so " offen sc1\n\t4:\n\t"
template <int STEP> __device__ __forceinline__ void two_batches(v4u (&t)[4], const u64 (&m)[4], unsigned off, __amdgpu_buffer_rsrc_t ws) {
  asm volatile("s_mov_b64 exec, %9\n\tbuffer_load_dwordx4 %0, %4, %5, 0 offen sc1\n\ts_mov_b64 exec, %10\n\tbuffer_load_dwordx4 %1, %4, %5, %6 offen sc1\n\t"
               "s_mov_b64 exec, %11\n\tbuffer_load_dwordx4 %2, %4, %5, %7 offen sc1\n\ts_mov_b64 exec, %12\n\tbuffer_load_dwordx4 %3, %4, %5, %8 offen sc1\n\t"
               "s_mov_b64 exec, -1\n\tv_mov_b32 v82, 0\n\ts_mov_b32 s58, 0x400000\n\t"
               "1:\n\tds_read_b32 v83, v82 offset:%13\n\ts_sub_u32 s58, s58, 1\n\ts_cbranch_scc1 2f\n\ts_waitcnt lgkmcnt(0)\n\tv_readfirstlane_b32 s59, v83\n\ts_cmp_lt_u32 s59, 2\n\ts_cbranch_scc1 1b\n\t2:\n\t"
               ASK2("%0", "%9", "0") ASK2("%1", "%10", "%6") ASK2("%2", "%11", "%7") ASK2("%3", "%12", "%8")
               "s_mov_b64 exec, -1\n\ts_waitcnt vmcnt(0) lgkmcnt(0)"
               : "=&v"(t[0]), "=&v"(t[1]), "=&v"(t[2]), "=&v"(t[3])
               : "v"(off), "s"(ws), "s"(STEP), "s"(2 * STEP), "s"(3 * STEP), "s"(m[0]), "s"(m[1]), "s"(m[2]), "s"(m[3]), "i"(F_GO)
               : "memory", "scc", "v82", "v83", "s58", "s59");
}
template <int STEP> __device__ __forceinline__ void gather9(v4u (&t)[9], unsigned off, __amdgpu_buffer_rsrc_t ws, unsigned T0, unsigned T1) {
  asm volatile("s_mov_b64 s[60:61], -1\n\ts_mov_b64 s[62:63], -1\n\ts_mov_b64 s[64:65], -1\n\ts_mov_b64 s[66:67], -1\n\ts_mov_b64 s[68:69], -1\n\t"
               "s_mov_b64 s[70:71], -1\n\ts_mov_b64 s[72:73], -1\n\ts_mov_b64 s[74:75], -1\n\ts_mov_b64 s[76:77], -1\n\ts_mov_b32 s58, 0x10000\n\t"
               "2:\n\t"
               ASK("v[84:87]", "s[60:61]", "%9", "%10", "0") ASK("v[88:91]", "s[62:63]", "%9", "%10", "%13") ASK("v[92:95]", "s[64:65]", "%9", "%10", "2*%13")
               ASK("v[96:99]", "s[66:67]", "%9", "%10", "3*%13") ASK("v[100:103]", "s[68:69]", "%9", "%10", "4*%13") ASK("v[104:107]", "s[70:71]", "%9", "%10", "5*%13")
               ASK("v[108:111]", "s[72:73]", "%9", "%10", "6*%13") ASK("v[112:115]", "s[74:75]", "%9", "%10", "7*%13") ASK("v[116:119]", "s[76:77]", "%9", "%10", "8*%13")
               "s_waitcnt vmcnt(8)\n\t" CHK("v84", "v86", "s[60:61]", "%11", "%12") "s_waitcnt vmcnt(7)\n\t" CHK("v88", "v90", "s[62:63]", "%11", "%12")
               "s_waitcnt vmcnt(6)\n\t" CHK("v92", "v94", "s[64:65]", "%11", "%12") "s_waitcnt vmcnt(5)\n\t" CHK("v96", "v98", "s[66:67]", "%11", "%12")
               "s_waitcnt vmcnt(4)\n\t" CHK("v100", "v102", "s[68:69]", "%11", "%12") "s_waitcnt vmcnt(3)\n\t" CHK("v104", "v106", "s[70:71]", "%11", "%12")
               "s_waitcnt vmcnt(2)\n\t" CHK("v108", "v110", "s[72:73]", "%11", "%12") "s_waitcnt vmcnt(1)\n\t" CHK("v112", "v114", "s[74:75]", "%11", "%12")
               "s_or_b64 s[56:57], s[60:61], s[62:63]\n\ts_or_b64 s[56:57], s[56:57], s[64:65]\n\ts_or_b64 s[56:57], s[56:57], s[66:67]\n\ts_or_b64 s[56:57], s[56:57], s[68:69]\n\t"
               "s_or_b64 s[56:57], s[56:57], s[70:71]\n\ts_or_b64 s[56:57], s[56:57], s[72:73]\n\ts_or_b64 s[56:57], s[56:57], s[74:75]\n\t"
               "s_waitcnt vmcnt(0)\n\t" CHK("v116", "v118", "s[76:77]", "%11", "%12")
               "s_nop 0\n\ts_or_b64 s[56:57], s[56:57], s[76:77]\n\ts_cbranch_scc0 3f\n\ts_sub_u32 s58, s58, 1\n\ts_cbranch_scc0 2b\n\t3:\n\ts_mov_b64 exec, -1"
               : "=&{v[84:87]}"(t[0]), "=&{v[88:91]}"(t[1]), "=&{v[92:95]}"(t[2]), "=&{v[96:99]}"(t[3]), "=&{v[100:103]}"(t[4]), "=&{v[104:107]}"(t[5]),
                 "=&{v[108:111]}"(t[6]), "=&{v[112:115]}"(t[7]), "=&{v[116:119]}"(t[8])
               : "v"(off), "s"(ws), "s"(T0), "s"(T1), "i"(STEP)
               : CLOB4, "s68", "s69", "s70", "s71", "s72", "s73", "s74", "s75", "s76", "s77");
}
template <int N, int SL> __device__ __forceinline__ unsigned latest(unsigned (&hq)[N], __amdgpu_buffer_rsrc_t ws, unsigned T0, unsigned (&tm)[N]) {
  const unsigned tref = now32();
  int late = 0;
#pragma unroll
  for (int i = 0; i < N; ++i) tm[i] = tref - 1000u;
#pragma nounroll
  for (unsigned spin = 0; spin < SPIN_CAP; ++spin) {
    v2u t[N]; bool more = false;
    CBAR();
#pragma unroll
    for (int i = 0; i < N; ++i) t[i] = __builtin_amdgcn_raw_buffer_load_b64(ws, hq[i], 0, AUX_SC1);
#pragma unroll
    for (int i = 0; i < N; ++i) { const bool ok = hq[i] != WS_END && t[i].x == T0; late = ok ? max(late, (int)(t[i].y - tref)) : late; tm[i] = ok ? t[i].y : tm[i]; hq[i] = ok ? WS_END : hq[i]; more |= hq[i] != WS_END; }
    if (!__any(more)) break;
    __builtin_amdgcn_s_sleep(SL);
  }
  const int l = (int)asf(__builtin_amdgcn_readfirstlane(asu(wave_max((float)late))));
  return tref + min(l, (int)(now32() - tref) + 400);
}
#define F4(v) __builtin_bit_cast(v4f, v)
#define U4(v) __builtin_bit_cast(v4u, v)

__global__ void __launch_bounds__(NTHR, 4) fused(Params p) {
  __shared__ __attribute__((aligned(16))) char lds_raw[LDS_BYTES];
  u64 pc0; asm volatile("s_getpc_b64 %0" : "=s"(pc0));
  const int b = blockIdx.x, tid = threadIdx.x, wave = __builtin_amdgcn_readfirstlane(tid >> 6), lane = tid & 63;
  const int g = (b & 7) * 2 + ((b >> 3) & 1), j = b >> 4;
  const bool is_wo = j < 6, is_mlp = j >= 6 && j < 15;
  const int u = g * 9 + (j - 6);
  const int red = ((j + 1) & 15) * 16 + g;
  const __amdgpu_buffer_rsrc_t ws = __builtin_amdgcn_make_buffer_rsrc(p.ws, 0, WS_END, 0x00020000);
  const int r = lane >> 4, c = lane & 15;
  const int q4 = c >> 2, p4 = c & 3;
  const v4f zero = {0.f, 0.f, 0.f, 0.f};
  asm volatile("" :: "v"((LAS char*)lds_raw));
  STE(0);
  const int qt = j >> 2, qc = j & 3;
  if (__builtin_expect(wave == 0, 1)) {
    const unsigned vx = lane * 16, vy = min(lane * 16u, 112u);
    asm volatile("s_mov_b32 m0, %2\n\ts_nop 0\n\tglobal_load_lds_dwordx4 %0, %3\n\tglobal_load_lds_dwordx4 %1, %3 offset:1024"
                 :: "v"(vx), "v"(vy), "s"(W_OFF + 18 * 1024), "s"(p.x) : "memory");
    unsigned vq = (144 * qt + (lane >> 3)) * (QW * 2) + (256 * g + 64 * qc) * 2 + (lane & 7) * 16;
    dma1<18, 63>(vq, W_OFF, p.wq, 8u * QW * 2);
    const size_t hb = ((size_t)g * SK + (SK - WIN) + 256 * j) * DH;
    const u16 *kb = p.k + hb, *vb = p.v + hb;
    unsigned vk[4], vv[4];
#pragma unroll
    for (int n = 0; n < 4; ++n) { vk[n] = r * 256 + ((c ^ (4 * n + r)) << 4); vv[n] = r * 256 + ((c ^ ((r << 2) | n)) << 4); }
    static_assert(DB % 4 == 0 && DB >= 20 && DB <= 32 && DC % 4 == 0 && DC >= DB && DC <= 32, "stage positions");
    ST(10);
    bar();
#define KV(DA_, DB_, DC_) { \
    dma4<1, DA_>(vk, K_OFF, kb); \
    VMW(22); lds_vst(F_L, 1u);                                           \
    dma4<DA_ / 4 - 1, DA_>(vk, K_OFF + 4096, kb); \
    VMW(DA_); lds_vst(F_L, 2u); ST(12);                                  \
    dma4<(32 + DB_ - DA_) / 4, DB_>(vk, K_OFF + DA_ * 1024, kb); \
    VMW(DB_); lds_vst(F_L, 3u);                                          \
    dma4<(32 - DB_) / 4, DB_>(vk, K_OFF + (32 + DB_) * 1024, kb); \
    dma4<DC_ / 4, DC_>(vv, V_OFF, vb); \
    VMW(DC_); lds_vst(F_L, 4u); ST(1);                                   \
    dma4<(64 - DC_) / 4, DC_>(vv, V_OFF + DC_ * 1024, vb); }
    if ((DEEP == 2 && is_wo) || (DEEP && is_mlp && ((((unsigned)(size_t)p.w1 + 64 * u) >> 7) & 7) == 3)) KV(DA, DBD, DCD) else KV(DA, DB, DV)
#undef KV
    ST(2);
    lds_vst(F_L, 6u);
    static_assert(DC == 32 || DC == 28 || DC == 24, "ramps");
    if (is_mlp) {
      unsigned v1 = (lane >> 2) * (FF * 2) + 64 * u + (lane & 3) * 16, v2 = 32 * u * (D * 2) + lane * 16;
      wait_cnt<0>(F_PUB, 4);
      constexpr int K7 = (DC - 16) / 2, K8 = (DC - 8) / 2, K9 = DC / 2;
      ramp1<K7 - 1, 2 * (DC - 2), -2>(v1, W1_OFF, p.w1, 16u * FF * 2);
      VMW(16 + K7 - 1); lds_vst(F_L, 7u);
      ramp1<K8 - K7, 2 * (DC - 1 - K7), -2>(v1, W1_OFF + (K7 - 1) * 1024, p.w1, 16u * FF * 2);
      VMW(8 + K8 - 1); lds_vst(F_L, 8u);
      ramp1<K9 - K8, 2 * (DC - 1 - K8), -2>(v1, W1_OFF + (K8 - 1) * 1024, p.w1, 16u * FF * 2);
      VMW(K9 - 1); lds_vst(F_L, 9u); STE(4);
      wait_cnt<0>(F_KD, 4);
      ramp1<1, 2 * (DC / 2 - 1), 0>(v1, W1_OFF + (K9 - 1) * 1024, p.w1, 16u * FF * 2);
      dma1<36 - K9, DC / 2>(v1, W1_OFF + K9 * 1024, p.w1, 16u * FF * 2);
      ramp1<DC - 2, DC - 1, 1>(v2, W2_OFF, p.w2, 1024u);
      VMW(DC - 2); lds_vst(F_W, 1u);
      dma1<38 - DC, DC>(v2, W2_OFF + (DC - 2) * 1024, p.w2, 1024u);
      lds_vst(F_T, now32()); lds_vst(F_W, 2u); ST(13);
      VMW(0); lds_vst(F_W, 3u); STE(14);
    } else if (is_wo) {
      const unsigned ta = now32();
      unsigned vo[3];
#pragma unroll
      for (int i = 0; i < 3; ++i) { const int pi = i * 64 + lane; vo[i] = (256 * g + pi / 12) * (D * 2) + 192 * j + (pi % 12) * 16; }
      wait_cnt<0>(F_PUB, 4);
      constexpr int K7 = (DC - 16) / 4, K8 = (DC - 8) / 4, K9 = DC / 4;
      ramp3<K7 - 1, DC - 4>(vo, W_OFF, p.wo, 16u * D * 2);
      VMW(16 + 3 * (K7 - 1)); lds_vst(F_L, 7u);
      ramp3<K8 - K7, DC - 3 - K7>(vo, W_OFF + (K7 - 1) * 3072, p.wo, 16u * D * 2);
      VMW(8 + 3 * (K8 - 1)); lds_vst(F_L, 8u);
      ramp3<K9 - K8, DC - 3 - K8>(vo, W_OFF + (K8 - 1) * 3072, p.wo, 16u * D * 2);
      VMW(3 * (K9 - 1)); lds_vst(F_L, 9u); STE(4);
      const unsigned tb = now32();
      lds_vst(F_T, tb + (tb - ta) * PRED / DC + CWO);
      lds_vst(F_W, 1u);
      wait_cnt<0>(F_KD, 4);
      dma3<17 - K9, 3 * K9>(vo, W_OFF + (K9 - 1) * 3072, p.wo, 16u * D * 2);
      VMW(WOEARLY); lds_vst(F_W, 2u); ST(13);
      VMW(0); lds_vst(F_W, 3u); STE(14);
    } else {
      VMW(16); lds_vst(F_L, 7u);
      VMW(8); lds_vst(F_L, 8u);
      VMW(0); lds_vst(F_L, 9u); STE(4);
    }
  } else {
  if (wave == 1) lds_st<unsigned>((lane & 31) * 4, (lane & 16) ? SENT : 0u);
  if (L2WARM && j == 15 && wave >= 8 && wave <= 12) {
    v4u t;
    asm volatile("global_load_dwordx4 %0, %1, %2\n\tglobal_load_dwordx4 %0, %1, %2 offset:1024\n\tglobal_load_dwordx4 %0, %1, %2 offset:2048\n\t"
                 "global_load_dwordx4 %0, %1, %2 offset:3072\n\ts_barrier\n\ts_waitcnt vmcnt(0)"
                 : "=&v"(t) : "v"(min((wave - 8) * 4096u, CODE_BYTES - 4096u) + lane * 16), "s"(pc0) : "memory");
  } else {
    ST(10);
    bar();
  }
  }
  if (wave == 0) {
  } else if (wave == 1) {
    wait_cnt<1>(F_L, 1);
    const v4u xa = lds_ld<v4u>(W_OFF + 18 * 1024 + lane * 16);
    const unsigned xb = lds_ld<u16>(W_OFF + 19 * 1024 + lane * 2);
    float xv[9];
#pragma unroll
    for (int k = 0; k < 4; ++k) { xv[2 * k] = lo16(xa[k]); xv[2 * k + 1] = hi16(xa[k]); }
    xv[8] = lo16(xb);
    lds_st<v4f>(X_OFF + lane * 32, v4f{xv[0], xv[1], xv[2], xv[3]}); lds_st<v4f>(X_OFF + lane * 32 + 16, v4f{xv[4], xv[5], xv[6], xv[7]});
    lds_st<float>(X_OFF + 2048 + lane * 4, xv[8]);
    {
      const float c0 = asf(__builtin_amdgcn_readfirstlane(asu(xv[0])));
      float s = 0.f, q = 0.f;
#pragma unroll
      for (int i = 0; i < 9; ++i) { const float d = xv[i] - c0; s += d; q = fmaf(d, d, q); }
      s = wave_sum(s); q = wave_sum(q);
      const float md = s * (1.f / D), var = fmaxf(q * (1.f / D) - md * md, 0.f);
      const float mb = rb(c0 + md), rden = 1.f / rb(__builtin_sqrtf(rb(rb(var) + 1e-5f)));
#pragma unroll
      for (int i = 0; i < 9; ++i) xv[i] = rb(xv[i] - mb) * rden;
      lds_st<v4u>(Z_OFF + lane * 16, v4u{pk(xv[0], xv[1]), pk(xv[2], xv[3]), pk(xv[4], xv[5]), pk(xv[6], xv[7])});
      lds_st<u16>(Z_OFF + 1024 + lane * 2, (u16)pk(xv[8], xv[8]));
    }
    lds_vst(F_Y, 1u);
    ST(13);
    wait_cnt<1>(F_PUB, 4);
    {
      GET_TAGS();
      const v4u tq = {T0 & 63u, T0 >> 6, T1 & 63u, T1 >> 6};
      unsigned qa = QP_OFF + g * 4096 + lane * 16;
      PIN(qa);
      v4u ga[4], gb[4];
#define QLD(G_) { CBAR(); _Pragma("unroll") for (int t = 0; t < 4; ++t) G_[t] = LDW(qa, t * 1024); SB(); }
#define QCK(G_, O_) { unsigned bad = 0; _Pragma("unroll") for (int t = 0; t < 4; ++t) { G_[t] ^= tq; bad |= G_[t].x | G_[t].y | G_[t].z | G_[t].w; } \
      const bool ok = !__any((bad & 63u) != 0u) || spin + 1 == cap; \
      const v4f qs = (F4(G_[0]) + F4(G_[1])) + (F4(G_[2]) + F4(G_[3])); \
      lds_st<v2u>(Q_OFF + lane * 8, v2u{pk(qs[0], qs[1]), pk(qs[2], qs[3])}); lds_vst(F_Q, ok ? 1u : 0u); ST(2); SB(); \
      if (ok) break; }
      const unsigned cap = TRUNC >= 3 ? 1 : SPIN_CAP;
      __builtin_amdgcn_s_sleep(QDELAY);
      QLD(ga)
      __builtin_amdgcn_s_sleep(QSTAG);
#pragma nounroll
      for (unsigned spin = 0;; ++spin) {
        QLD(gb) QCK(ga, gb) QLD(ga) QCK(gb, ga)
      }
#undef QLD
#undef QCK
    }
  } else if (wave >= 4 && wave <= 7) {
    const int w = wave - 4;
    if (w == 0) {
      const unsigned ep = *(const __attribute__((address_space(4))) unsigned*)(p.ws + EP_OFF);
      lds_st<v2u>(TG_OFF, v2u{(ep + 1u) & 0xfffu, ((ep >> 16) + 1u) & 0xfffu});
    }
    unsigned ka[4], va[2][2];
#pragma unroll
    for (int s = 0; s < 4; ++s) { ka[s] = K_OFF + (16 * w + c) * 256 + (((4 * s + r) ^ c) << 4); PIN(ka[s]); }
#pragma unroll
    for (int tt = 0; tt < 2; ++tt)
#pragma unroll
      for (int t = 0; t < 2; ++t) { va[tt][t] = V_OFF + 256 * (8 * r + 4 * t + q4) + 16 * ((2 * (2 * w + tt) + (p4 >> 1)) ^ ((q4 << 2) | ((2 * r + t) & 3))) + 8 * (p4 & 1); PIN(va[tt][t]); }
    unsigned opo = oob(c < 4, OP_OFF + (g * 16 + j) * 1024 + ((c & 1) * 128 + 32 * w + 8 * (c & 2) + 4 * r) * 4);
    PIN(opo);
    {
      unsigned wa = W_OFF + (8 * r + q4) * 128 + p4 * 8 + w * 32, wb = W_OFF + (128 + 4 * r + q4) * 128 + p4 * 8 + w * 32;
      unsigned qo = oob(c == 0 && TRUNC < 3, QP_OFF + ((g * 4 + qt) * 256 + 64 * qc + 16 * w + 4 * r) * 4);
      PIN(wa); PIN(wb); PIN(qo);
#pragma nounroll
      for (unsigned s = 0; s < LSPIN_CAP; ++s) { if (__builtin_amdgcn_readfirstlane(lds_vld(TG_OFF)) != SENT) break; __builtin_amdgcn_s_sleep(1); }
      CBAR();
      GET_TAGS();
      wait_cnt<1>(F_Y, 1);
      v4u yf[4];
#pragma unroll
      for (int s = 0; s < 4; ++s) yf[s] = lds_ld<v4u>(Z_OFF + 288 * qt + 16 * r + 64 * s);
      const v2u yl = lds_ld<v2u>(Z_OFF + 288 * qt + 256 + 8 * r);
      wait_cnt<0>(F_L, 2);
      ST(13);
      v4u wf[4];
#pragma unroll
      for (int s = 0; s < 4; ++s) wf[s] = tr8(wa + s * 4096, wa + s * 4096 + 512);
      const v2u wl = __builtin_bit_cast(v2u, __builtin_amdgcn_ds_read_tr16_b64_v4i16((LAS v4s*)(size_t)wb));
      SB();
      const v4f qa = mfma(wf[3], yf[3], mfma(wf[2], yf[2], mfma(wf[1], yf[1], mfma(wf[0], yf[0], zero)))) +
                     __builtin_amdgcn_mfma_f32_16x16x16bf16_1k(__builtin_bit_cast(v4s, wl), __builtin_bit_cast(v4s, yl), zero, 0, 0, 0);
      const v4u tq = {T0 & 63u, T0 >> 6, T1 & 63u, T1 >> 6};
      STW(((U4(qa) + 32u) & ~63u) | tq, qo);
      if (lane == 0) lds_inc(F_PUB);
      ST(14);
    }
    GET_TAGS();
    v4u kf[2][4], qf[4]; v4f dt[4];
    wait_cnt<2>(F_L, 3);
#pragma unroll
    for (int t = 0; t < 2; ++t)
#pragma unroll
      for (int s = 0; s < 4; ++s) kf[t][s] = lds_ld<v4u>(ka[s] + t * 16384);
    SB();
    wait_cnt<1>(F_Q, 1);
    ST(11);
#pragma unroll
    for (int s = 0; s < 4; ++s) qf[s] = lds_ld<v4u>(Q_OFF + (c & 1) * 256 + r * 16 + s * 64);
    SB();
#pragma unroll
    for (int t = 0; t < 2; ++t) dt[t] = mfma(qf[3], kf[t][3], mfma(qf[2], kf[t][2], mfma(qf[1], kf[t][1], mfma(qf[0], kf[t][0], zero))));
    SB();
    wait_cnt<0>(F_L, 4);
    ST(1);
#pragma unroll
    for (int t = 0; t < 2; ++t)
#pragma unroll
      for (int s = 0; s < 4; ++s) kf[t][s] = lds_ld<v4u>(ka[s] + (2 + t) * 16384);
    SB();
    if (lane == 0) lds_inc(F_KD);
#pragma unroll
    for (int t = 0; t < 2; ++t) dt[2 + t] = mfma(qf[3], kf[t][3], mfma(qf[2], kf[t][2], mfma(qf[1], kf[t][1], mfma(qf[0], kf[t][0], zero))));
    const bool r1 = r & 1, r2 = r & 2;
    float s0 = r2 ? (r1 ? dt[3][0] : dt[2][0]) : (r1 ? dt[1][0] : dt[0][0]);
    float s1 = r2 ? (r1 ? dt[3][1] : dt[2][1]) : (r1 ? dt[1][1] : dt[0][1]);
    s0 = rb(rb(s0) * 0.08838834764831845f); s1 = rb(rb(s1) * 0.08838834764831845f);
    {
      auto sw = __builtin_amdgcn_permlane32_swap(asu(s0), asu(s1), false, false);
      float m = fmaxf(asf(sw[0]), asf(sw[1]));
      auto s2 = __builtin_amdgcn_permlane16_swap(asu(m), asu(m), false, false);
      m = vmax(asf(s2[0]), asf(s2[1]));
      asm volatile("s_nop 1\n\tv_max_f32_dpp %0, %0, %0 quad_perm:[1,0,3,2] row_mask:0xf bank_mask:0xf\n\t"
                   "s_nop 1\n\tv_max_f32_dpp %0, %0, %0 quad_perm:[2,3,0,1] row_mask:0xf bank_mask:0xf\n\t"
                   "s_nop 1\n\tv_max_f32_dpp %0, %0, %0 row_half_mirror row_mask:0xf bank_mask:0xf\n\t"
                   "s_nop 1\n\tv_max_f32_dpp %0, %0, %0 row_ror:8 row_mask:0xf bank_mask:0xf" : "+v"(m));
      lds_st<unsigned>(MX_OFF + (lane >> 5) * 16 + w * 4, clean(m));
    }
    v4u ea, eb;
    wait_vals8<0>(MX_OFF, ea, eb);
    const float M0 = vmax(vmax(asf(ea.x), asf(ea.y)), vmax(asf(ea.z), asf(ea.w))), M1 = vmax(vmax(asf(eb.x), asf(eb.y)), vmax(asf(eb.z), asf(eb.w)));
    const unsigned pp = pk(__builtin_amdgcn_exp2f((s0 - M0) * 1.4426950408889634f), __builtin_amdgcn_exp2f((s1 - M1) * 1.4426950408889634f));
    lds_st<u16>(P_OFF + (64 * r + 16 * w + c) * 2, (u16)pp); lds_st<u16>(P_OFF + 512 + (64 * r + 16 * w + c) * 2, (u16)(pp >> 16));
    if (lane == 0) lds_inc(F_P);
    wait_cnt<0>(F_P, 4);
    v4u pf[8];
#pragma unroll
    for (int s = 0; s < 8; ++s) pf[s] = lds_ld<v4u>(P_OFF + (c & 1) * 512 + r * 16 + s * 64);
    if (w == 0) {
      const v4u ones = {0x3f803f80u, 0x3f803f80u, 0x3f803f80u, 0x3f803f80u};
      v4f ls = zero;
#pragma unroll
      for (int s = 0; s < 8; ++s) ls = mfma(pf[s], ones, ls);
      STW(tag4(v4f{M0, ls[0], M1, ls[1]}, T0, T1), oob(lane < 8 && TRUNC < 2, ST_OFF + (g * 16 + j) * 128 + lane * 16));
    }
    ST(2);
    v4f oa = zero, ob = zero;
#define PV(s0_, n_) { v4u fa[n_], fb[n_]; \
      _Pragma("unroll") for (int s = 0; s < n_; ++s) { fa[s] = tr8(va[0][0] + (s0_ + s) * 8192, va[0][1] + (s0_ + s) * 8192); fb[s] = tr8(va[1][0] + (s0_ + s) * 8192, va[1][1] + (s0_ + s) * 8192); } \
      SB(); _Pragma("unroll") for (int s = 0; s < n_; ++s) { oa = mfma(fa[s], pf[s0_ + s], oa); ob = mfma(fb[s], pf[s0_ + s], ob); } SB(); }
    wait_cnt<0>(F_L, 6); PV(0, 4)
    wait_cnt<0>(F_L, 7); PV(4, 2)
    wait_cnt<0>(F_L, 8); PV(6, 1)
    {
      v4u fa, fb;
#pragma nounroll
      for (unsigned sp = 0; sp < LSPIN_CAP; ++sp) {
        const unsigned f = lds_vld(F_L);
        CBAR();
        fa = tr8(va[0][0] + 7 * 8192, va[0][1] + 7 * 8192); fb = tr8(va[1][0] + 7 * 8192, va[1][1] + 7 * 8192);
        SB();
        if (__builtin_amdgcn_readfirstlane(f) >= 9u) break;
      }
      oa = mfma(fa, pf[7], oa); ob = mfma(fb, pf[7], ob);
    }
#undef PV
    STW(tag4((c & 2) ? ob : oa, T0, T1), TRUNC >= 2 ? opo | 0x40000000u : opo);
    ST(3);
  }

  if (TRUNC) {} else
  if (is_wo) {
    if (wave >= 4 && wave <= 7) {
      const int w = wave - 4;
      unsigned oo = OP_OFF + (g * 16 + 4 * r) * 1024 + (64 * w + 4 * c) * 4, ol = O_OFF + (64 * w + 4 * c + r) * 2;
      PIN(oo); PIN(ol);
      GET_TAGS();
      wait_cnt<1>(F_W, 2);
      ST(12);
      v4u t[4];
#pragma unroll
      for (int i = 0; i < 4; ++i) t[i] = LDW(oo, i * 1024);
      SB();
      wait_cnt<0>(F_GO, 1);
      v4f wg = lds_ld<v4f>(TT_OFF + (w >> 1) * 64 + r * 16);
      PIN(wg);
      SB();
      if (__builtin_expect(!ALL_OK(flip(t, T0, T1)), 0)) { flip(t, T0, T1); settle4<1024>(t, oo, ws, T0, T1); }
      const v4f a = (F4(t[0]) * wg[0] + F4(t[1]) * wg[1]) + (F4(t[2]) * wg[2] + F4(t[3]) * wg[3]);
      ST(4);
      const float o = t16(t32(a[0], a[2]), t32(a[1], a[3]));
      lds_st<u16>(ol, (u16)pk(o, o));
      ST(5);
      bar();
    } else if (wave >= 8 && wave <= 13) {
      const int t = wave - 8;
      unsigned ho = oob(c == 0, HP_OFF + (g * 576 + j * 96 + 16 * t + 4 * r) * 4), oa = O_OFF + r * 16;
      PIN(ho); PIN(oa);
      unsigned wa = W_OFF + (8 * r + q4) * 192 + t * 32 + p4 * 8;
      PIN(wa);
      wait_cnt<8>(F_W, 1);
      wait_cnt<2>(F_W, 2);
      GET_TAGS();
      v4u wf[8];
      constexpr int S1 = 8 - (WOEARLY + 5) / 6;
#pragma unroll
      for (int s = 0; s < S1; ++s) wf[s] = tr8(wa + s * 6144, wa + s * 6144 + 4 * 192);
      wait_cnt<0>(F_W, 3);
#pragma unroll
      for (int s = S1; s < 8; ++s) wf[s] = tr8(wa + s * 6144, wa + s * 6144 + 4 * 192);
#pragma unroll
      for (int s = 0; s < 8; ++s) PIN(wf[s]);
      ST(5);
      bar();
      v4u of[8];
#pragma unroll
      for (int s = 0; s < 8; ++s) of[s] = lds_ld<v4u>(oa + s * 64);
      SB();
      v4f acc = zero;
#pragma unroll
      for (int s = 0; s < 8; ++s) acc = mfma(wf[s], of[s], acc);
      STW(tag4(acc, T0, T1), ho);
      STE(6);
    } else {
      if (wave == 1) {
        wait_cnt<2>(F_W, 1);
        GET_TAGS();
        STW((v4u{T0, lds_vld(F_T), T1, 0u}), HCOMPACT ? oob(lane < (j == 5 ? 3 : 1), H2_OFF + g * 128 + (j + lane) * 16) : oob(lane < 8, H2_OFF + (g * 6 + j) * 128 + lane * 16));
        v4f st;
#pragma nounroll
        for (unsigned spin = 0; spin < SPIN_CAP; ++spin) {
          unsigned bad = 0;
          CBAR();
          st = untag4(LDW(ST_OFF + (g * 16 + c) * 128, 0), T0, T1, bad);
          if (ALL_OK(bad)) break;
          __builtin_amdgcn_s_sleep(4);
        }
        const float mp = (r & 1) ? st[2] : st[0], lp = (r & 1) ? st[3] : st[1];
        float wgt = __builtin_amdgcn_exp2f((mp - row_max(mp)) * 1.4426950408889634f);
        wgt *= __builtin_amdgcn_rcpf(row_sum(wgt * lp));
        lds_st<float>(TT_OFF + lane * 4, wgt);
        lds_vst(F_GO, 1u);
        ST(4);
      }
      bar();
    }
  } else if (is_mlp) {
    v4u w2f[9]; float b1v = 0.f;
    unsigned za = Z_OFF + (c >> 3) * 128 + (8 * r + (c & 7)) * 4, mo = oob(p4 < 2 || c == 2, MPT ? MP_OFF + (9 * (wave - 8) + 4 * p4 + q4) * 9216 + u * 64 + r * 16 : MP_OFF + u * 2304 + (9 * (wave - 8) + 4 * p4 + q4) * 64 + r * 16);
    PIN(za); PIN(mo);
    const bool second = wave >= 8 && wave <= 11;
    if (second) { b1v = lo16(((const GAS u16*)p.b1)[32 * u + 8 * r + (c & 7)]); PIN(b1v); }
    wait_cnt<8>(F_L, 9);
    GET_TAGS();
    const int ch = wave >= 12 ? (wave == 14 ? 8 : wave - 12) : wave;
    unsigned w2a = W2_OFF + (8 * r + q4) * 1152 + 9 * (wave - 8) * 32 + p4 * 8;
    PIN(w2a);
    bool have = false;
    if (second) {
      wait_cnt<4>(F_W, 1);
#pragma nounroll
      for (unsigned sp = 0; sp < LSPIN_CAP; ++sp) {
        const v2u f = *(const volatile LAS v2u*)(size_t)F_W;
        have = __builtin_amdgcn_readfirstlane(f.x) >= 3u;
        if (have || __builtin_amdgcn_readfirstlane(f.y) >= 9u) break;
        __builtin_amdgcn_s_sleep(1);
      }
      CBAR();
      if (have) {
#pragma unroll
        for (int t = 0; t < 9; ++t) w2f[t] = tr8(w2a + t * 32, w2a + t * 32 + 4 * 1152);
      }
    } else if (wave == 1) {
      unsigned hq[2] = {HCOMPACT ? oob((lane & 7) < 6, H2_OFF + lane * 16) : H2_OFF + lane * 128, HCOMPACT ? oob((lane & 7) < 6, H2_OFF + 1024 + lane * 16) : oob(lane < 32, H2_OFF + (64 + lane) * 128)};
      unsigned h3o = oob(lane < 8, H3_OFF + u * 128 + lane * 16);
      PIN(h3o);
      __builtin_amdgcn_s_sleep(HWAIT);
      unsigned tm[2];
      const unsigned due = latest<2, SLH>(hq, ws, T0, tm);
      ST(12);
      unsigned ta;
      do ta = now32(); while ((int)(ta - (due - AHEAD)) < 0 );
      const bool late = (int)(due - ta) <= 0;
      const u64 l0 = late ? 0ull : __builtin_amdgcn_ballot_w64((int)(tm[0] + MARG - ta) > 0), l1 = late ? 0ull : __builtin_amdgcn_ballot_w64((int)(tm[1] + MARG - ta) > 0);
      lds_st<v4u>(TT_OFF, v4u{(unsigned)l0, (unsigned)(l0 >> 32), (unsigned)l1, (unsigned)(l1 >> 32)});
      lds_vst(F_GO, late ? 2u : 1u);
      const unsigned all = (late ? ta : due) + DELTA3;
      bool sent = false;
      for (unsigned sp = 0; sp < LSPIN_CAP; ++sp) {
        const bool out = __builtin_amdgcn_readfirstlane(lds_vld(F_W)) >= 2u, now = (int)(now32() - due) >= 0;
        if (now) lds_vst(F_GO, 2u);
        if (out && !sent) {
          const unsigned own = __builtin_amdgcn_readfirstlane(lds_vld(F_T)) + DELTAW;
          STW((v4u{T0, (int)(own - all) > 0 ? own : all, T1, 0u}), h3o);
          sent = true;
        }
        if (now && sent) break;
      }
    } else if (wave != 0) {
      unsigned ho = HP_OFF + (r * 576 + 64 * ch + 4 * c) * 4, zw = Z_OFF + ch * 256 + r * 16, sw = S_OFF + r * 128 + ch * 8;
      unsigned hs_o = oob(u == 0, MP_OFF + 144 * 2304 + (64 * ch + 4 * c + r) * 4);
      PIN(ho); PIN(zw); PIN(sw); PIN(hs_o);
      wait_cnt<2>(F_W, 1);
      v4u w1f[2][2];
#pragma unroll
      for (int s = 0; s < 2; ++s)
#pragma unroll
        for (int t = 0; t < 2; ++t) { const unsigned a = W1_OFF + (64 * ch + 4 * (8 * s + q4) + r) * 64 + t * 32 + p4 * 8; w1f[s][t] = tr8(a, a + 16 * 64); }
      float xv = lds_ld<float>(X_OFF + (64 * ch + 4 * c + r) * 4);
      const v4u ones = {0x3f803f80u, 0x3f803f80u, 0x3f803f80u, 0x3f803f80u};
#pragma unroll
      for (int t = 0; t < 2; ++t) {
        const v4f cs = mfma(w1f[1][t], ones, mfma(w1f[0][t], ones, zero));
        if (c == 0) lds_st<v4f>(zw + 128 + t * 64, cs);
      }
      PIN(xv);
      wait_cnt<1>(F_GO, 1);
      const v4u ny = lds_ld<v4u>(TT_OFF);
      const u64 ma[4] = {__builtin_amdgcn_ballot_w64(((ny[0] >> (8 * r)) & 255u) == 0u), __builtin_amdgcn_ballot_w64(((ny[1] >> (8 * r)) & 255u) == 0u),
                         __builtin_amdgcn_ballot_w64(((ny[2] >> (8 * r)) & 255u) == 0u), __builtin_amdgcn_ballot_w64(((ny[3] >> (8 * r)) & 255u) == 0u)};
      ST(5);
      v4u hv[4];
      two_batches<4 * 2304>(hv, ma, ho, ws);
      if (__builtin_expect(!ALL_OK(flip(hv, T0, T1)), 0)) { flip(hv, T0, T1); settle4<4 * 2304>(hv, ho, ws, T0, T1); }
      const v4f a = (F4(hv[0]) + F4(hv[1])) + (F4(hv[2]) + F4(hv[3]));
      const float at = t16(t32(a[0], a[2]), t32(a[1], a[3]));
      ST(6);
      const float hs = xv + rb(at);
      const unsigned pr = pk(hs, dpp<XOR1>(hs));
      const v4u f0 = {dppu<NEWB + 0>(pr), dppu<NEWB + 2>(pr), dppu<NEWB + 4>(pr), dppu<NEWB + 6>(pr)};
      const v4u f1 = {dppu<NEWB + 8>(pr), dppu<NEWB + 10>(pr), dppu<NEWB + 12>(pr), dppu<NEWB + 14>(pr)};
      const v4f z0 = mfma(w1f[1][0], f1, mfma(w1f[0][0], f0, zero)), z1 = mfma(w1f[1][1], f1, mfma(w1f[0][1], f0, zero));
      const v4f sq = mfma(c == 1 ? ones : f1, f1, mfma(c == 1 ? ones : f0, f0, zero));
      if (c == 0) { lds_st<v4f>(zw, z0); lds_st<v4f>(zw + 64, z1); lds_st<v2f>(sw, v2f{sq[0], sq[1]}); }
      if (lane == 0) lds_inc(F_Z);
      __builtin_amdgcn_raw_buffer_store_b32((pr << 16) | T0, ws, hs_o, 0, AUX_SC1);
    }
    if (second) {
      v4u g0 = {0u, 0u, 0u, 0u}, g1 = g0, g2 = g0, g3 = g0;
      PIN(g0); PIN(g1); PIN(g2); PIN(g3);
      wait_cnt<2>(F_Z, 1);
      wait_cnt<0>(F_Z, 9);
      ST(11);
      const v4f sa = lds_ld<v4f>(S_OFF), sb = lds_ld<v4f>(S_OFF + 16), sc = lds_ld<v4f>(S_OFF + 32), sd = lds_ld<v4f>(S_OFF + 48);
      const v2f se = lds_ld<v2f>(S_OFF + 64);
      float zz[9];
#pragma unroll
      for (int i = 0; i < 9; ++i) zz[i] = lds_ld<float>(za + i * 256);
      SB();
      const v4f s4 = (sa + sb) + (sc + sd);
      const float mean = ((s4[1] + s4[3]) + se[1]) * (1.f / D), var = fmaxf(((s4[0] + s4[2]) + se[0]) * (1.f / D) - mean * mean, 0.f);
      const float rr = __builtin_amdgcn_rsqf(fmaf(var, 1.f + 1e-5f, 1e-10f));
      const v2f zp = (v2f{zz[0], zz[1]} + v2f{zz[2], zz[3]}) + (v2f{zz[4], zz[5]} + v2f{zz[6], zz[7]});
      const float t = (zp[0] + zz[8]) + zp[1];
      const float z = (t - mean * dpp<XOR8>(t)) * rr;
      const float zb = rb(rb(z) + b1v);
      const float e = __builtin_amdgcn_exp2f(zb * fmaf(zb * zb, 2.302208198144325f * 0.044715f, 2.302208198144325f));
      const float gl = zb - zb * __builtin_amdgcn_rcpf(e + 1.f);
      const unsigned gp = pk(gl, dpp<XOR1>(gl));
      if (!have) {
        wait_cnt<0>(F_W, 3);
#pragma unroll
        for (int t = 0; t < 9; ++t) w2f[t] = tr8(w2a + t * 32, w2a + t * 32 + 4 * 1152);
      }
#define GB(G_, bk) _Pragma("unroll") for (int k = 0; k < 1; ++k) { \
        G_[0] = __builtin_amdgcn_update_dpp(G_[0], gp, NEWB + 0, 0xf, 1 << (bk), false); G_[1] = __builtin_amdgcn_update_dpp(G_[1], gp, NEWB + 2, 0xf, 1 << (bk), false); \
        G_[2] = __builtin_amdgcn_update_dpp(G_[2], gp, NEWB + 4, 0xf, 1 << (bk), false); G_[3] = __builtin_amdgcn_update_dpp(G_[3], gp, NEWB + 6, 0xf, 1 << (bk), false); }
      GB(g0, 0) GB(g1, 1) GB(g2, 2) GB(g3, 3)
#undef GB
      const v4f o2 = mfma(w2f[8], g0, zero);
      const v4f o0 = mfma(w2f[3], g3, mfma(w2f[2], g2, mfma(w2f[1], g1, mfma(w2f[0], g0, zero))));
      const v4f o1 = mfma(w2f[7], g3, mfma(w2f[6], g2, mfma(w2f[5], g1, mfma(w2f[4], g0, zero))));
      v4f o;
#pragma unroll
      for (int k = 0; k < 4; ++k) { const float a0 = o0[k], a1 = o1[k], a2 = o2[k]; const float lo_ = (c & 2) ? a2 : a0; o[k] = (c & 1) ? a1 : lo_; }
      STW(tag4(o, T0, T1), mo);
      STE(8);
    }
  }
  if (!TRUNC && red < 36 && wave == 1) {
    const int c4 = lane & 3, col = 16 * red + 4 * c4 + r;
    float b2v = lo16(((const GAS u16*)p.b2)[col]);
    PIN(b2v);
    wait_cnt<8>(F_L, 9);
    GET_TAGS();
    unsigned hu, mo = MPT ? MP_OFF + red * 9216 + lane * 16 : MP_OFF + (lane >> 2) * 2304 + red * 64 + c4 * 16;
    unsigned hro = MP_OFF + 144 * 2304 + col * 4, oo = col * 2;
    PIN(mo); PIN(hro); PIN(oo);
    {
      unsigned hq[3];
#pragma unroll
      for (int i = 0; i < 3; ++i) hq[i] = oob(i * 64 + lane < 144, H3_OFF + (i * 64 + lane) * 128);
      if (j == 15) __builtin_amdgcn_s_sleep(64);
      unsigned tm[3];
      const unsigned go = latest<3, SL3>(hq, ws, T0, tm);
      ST(6);
      while ((int)(now32() - go) < 0) {}
    }
    constexpr int MS = MPT ? 1024 : 16 * 2304;
    hu = __builtin_amdgcn_raw_buffer_load_b32(ws, hro, 0, AUX_SC1);
    SB();
    v4u mv[9];
#pragma unroll
    for (int i = 0; i < 9; ++i) { mv[i] = LDW(mo + (MPT ? (i & 3) * MS : 0), (MPT ? (i & ~3) : i) * MS); SB(); }
    hu ^= T0;
    unsigned bad = hu;
    v4f a = untag4(mv[0], T0, T1, bad);
#pragma unroll
    for (int i = 1; i < 9; ++i) { a += untag4(mv[i], T0, T1, bad); PIN(a); SB(); }
    if (__builtin_expect(!ALL_OK(bad), 0)) {
      gather9<MS>(mv, mo, ws, T0, T1);
#pragma nounroll
      for (unsigned spin = 0; spin < SPIN_CAP && !ALL_OK(hu); ++spin) { CBAR(); hu = __builtin_amdgcn_raw_buffer_load_b32(ws, hro, 0, AUX_SC1) ^ T0; }
      a = F4(mv[0]);
#pragma unroll
      for (int i = 1; i < 9; ++i) a += F4(mv[i]);
    }
    float t = t16(t32(a[0], a[2]), t32(a[1], a[3]));
    t += dpp<XOR8>(t); t += dpp<XOR7>(dpp<XOR3>(t));
    const float hf = asf(hu);
    const float res = hf + rb(rb(t) + b2v);
    *(GAS u16*)((GAS char*)p.out + oo) = (u16)pk(res, res);
    if (red == 0 && lane == 0) __builtin_amdgcn_raw_buffer_store_b32(T0 | (T1 << 16), ws, EP_OFF, 0, AUX_SC1);
    STE(9);
  }
}

extern "C" void kernel_launch(void* const* d_in, const int* in_sizes, int n_in, void* d_out, int out_size, void* d_ws, size_t ws_size, hipStream_t stream) {
  Params p;
  p.x = (const u16*)d_in[0]; p.k = (const u16*)d_in[1]; p.v = (const u16*)d_in[2]; p.wq = (const u16*)d_in[3]; p.wo = (const u16*)d_in[4];
  p.w1 = (const u16*)d_in[6]; p.b1 = (const u16*)d_in[7]; p.w2 = (const u16*)d_in[8]; p.b2 = (const u16*)d_in[9];
  p.out = (u16*)d_out; p.ws = (char*)d_ws + WSPOS(ws_size);
  hipLaunchKernelGGL(fused, dim3(NBLK), dim3(NTHR), 0, stream, p);
}
```

```cpp
#include <hip/hip_runtime.h>
#include <hip/hip_bf16.h>

typedef unsigned short u16;
typedef unsigned long long u64;
typedef unsigned v4u __attribute__((ext_vector_type(4)));
typedef unsigned v2u __attribute__((ext_vector_type(2)));
typedef float v4f __attribute__((ext_vector_type(4)));
typedef float v2f __attribute__((ext_vector_type(2)));
typedef short v4s __attribute__((ext_vector_type(4)));
typedef __bf16 bf16x2 __attribute__((ext_vector_type(2)));
typedef __bf16 bf16x8 __attribute__((ext_vector_type(8)));
#define GAS __attribute__((address_space(1)))
#define LAS __attribute__((address_space(3)))

#define DA 20
#define DB 24
#define DC 28
#define QDELAY 16
#define QSTAG 16
#define WOEARLY 12
#define CWO 230
#define HWAIT 40
#define MSTART 12
#define DELTA3 150
#define DELTAW 245
#define CODEB 15872
constexpr int JM = 4, JS = 6;

constexpr int D = 576, SK = 65536, DH = 128, WIN = 4096, FF = 4608, QW = 4096;
constexpr int NWAVE = 15, NTHR = NWAVE * 64, NBLK = 256;
constexpr unsigned EP_OFF = 0, QP_OFF = 4096, OP_OFF = QP_OFF + 16 * 4 * 256 * 4, ST_OFF = OP_OFF + 16 * 16 * 256 * 4, HP_OFF = ST_OFF + 256 * 128,
                   MP_OFF = HP_OFF + 16 * 576 * 4, HR_OFF = MP_OFF + 144 * 576 * 4, H2_OFF = HR_OFF + 576 * 4, H3_OFF = H2_OFF + 16 * 128,
                   WS_END = H3_OFF + 144 * 128;
constexpr unsigned SENT = 0xFFFFFFFFu;
constexpr unsigned SPIN_CAP = 1u << 16, LSPIN_CAP = 1u << 22;
constexpr int AUX_SC1 = 16;
constexpr unsigned F_L = 0, F_W = 8, F_Z = 12, F_P = 16, F_KD = 20, F_PUB = 24, F_Y = 28, F_T = 36, F_Q = 40, F_GO = 48  , F_P1 = 56, MX_OFF = 64, TG_OFF = 96,
                   Q_OFF = 128, P_OFF = Q_OFF + 512, WG_OFF = P_OFF + 1024, O_OFF = WG_OFF + 256, S_OFF = O_OFF + 512, X_OFF = S_OFF + 512,
                   Z_OFF = X_OFF + 2304, M_END = Z_OFF + 2304;
constexpr unsigned W_OFF = 8192, K_OFF = W_OFF + 24576, V_OFF = K_OFF + 65536, LDS_BYTES = V_OFF + 65536;
constexpr unsigned W1_OFF = W_OFF, W2_OFF = W_OFF + 36864;
static_assert(M_END <= W_OFF && LDS_BYTES <= 160 * 1024, "LDS");

struct Params { const u16 *x, *k, *v, *wq, *wo, *w1, *b1, *w2, *b2; u16* out; char* ws; };

__device__ __forceinline__ float asf(unsigned u) { return __builtin_bit_cast(float, u); }
__device__ __forceinline__ unsigned asu(float f) { return __builtin_bit_cast(unsigned, f); }
__device__ __forceinline__ float lo16(unsigned u) { return asf(u << 16); }
__device__ __forceinline__ float hi16(unsigned u) { return asf(u & 0xffff0000u); }
__device__ __forceinline__ unsigned pk(float lo, float hi) { bf16x2 r; r.x = (__bf16)lo; r.y = (__bf16)hi; return __builtin_bit_cast(unsigned, r); }
__device__ __forceinline__ float rb(float f) { return lo16(pk(f, f)); }
__device__ __forceinline__ v4f mfma(v4u a, v4u b, v4f c) {
  return __builtin_amdgcn_mfma_f32_16x16x32_bf16(__builtin_bit_cast(bf16x8, a), __builtin_bit_cast(bf16x8, b), c, 0, 0, 0);
}
template <int CTRL> __device__ __forceinline__ float dpp(float x) { return asf(__builtin_amdgcn_mov_dpp(asu(x), CTRL, 0xf, 0xf, true)); }
constexpr int XOR1 = 0xB1, XOR2 = 0x4E, XOR3 = 0x1B, XOR7 = 0x141, XOR8 = 0x128, NEWB = 0x150;
__device__ __forceinline__ float t32(float a, float b) { auto s = __builtin_amdgcn_permlane32_swap(asu(a), asu(b), false, false); return asf(s[0]) + asf(s[1]); }
__device__ __forceinline__ float t16(float a, float b) { auto s = __builtin_amdgcn_permlane16_swap(asu(a), asu(b), false, false); return asf(s[0]) + asf(s[1]); }
__device__ __forceinline__ float m32(float a, float b) { auto s = __builtin_amdgcn_permlane32_swap(asu(a), asu(b), false, false); return fmaxf(asf(s[0]), asf(s[1])); }
__device__ __forceinline__ float m16(float a, float b) { auto s = __builtin_amdgcn_permlane16_swap(asu(a), asu(b), false, false); return fmaxf(asf(s[0]), asf(s[1])); }
__device__ __forceinline__ float row_sum(float x) { x += dpp<XOR1>(x); x += dpp<XOR2>(x); x += dpp<XOR7>(x); x += dpp<XOR8>(x); return x; }
__device__ __forceinline__ float row_max(float x) { x = fmaxf(x, dpp<XOR1>(x)); x = fmaxf(x, dpp<XOR2>(x)); x = fmaxf(x, dpp<XOR7>(x)); x = fmaxf(x, dpp<XOR8>(x)); return x; }
__device__ __forceinline__ float wave_sum(float x) { x = row_sum(x); x = t16(x, x); return t32(x, x); }
__device__ __forceinline__ float wave_max(float x) { x = row_max(x); x = m16(x, x); return m32(x, x); }
__device__ __forceinline__ unsigned rfl(unsigned x) { return __builtin_amdgcn_readfirstlane(x); }
__device__ __forceinline__ unsigned now32() { return (unsigned)__builtin_amdgcn_s_memrealtime(); }
#define CBAR() asm volatile("" ::: "memory")
#define SB() __builtin_amdgcn_sched_barrier(0)
#define PIN(x) asm volatile("" : "+v"(x))
#define VMW(n) asm volatile("s_waitcnt vmcnt(%0)" :: "i"(n) : "memory")
#define F4(v) __builtin_bit_cast(v4f, v)
#define U4(v) __builtin_bit_cast(v4u, v)
__device__ __forceinline__ void bar() { asm volatile("s_waitcnt lgkmcnt(0)" ::: "memory"); __builtin_amdgcn_s_barrier(); CBAR(); }
__device__ __forceinline__ unsigned oob(bool keep, unsigned off) { PIN(off); return keep ? off : WS_END; }

template <typename T> __device__ __forceinline__ T lds_ld(unsigned off) { return *(const LAS T*)(size_t)off; }
template <typename T> __device__ __forceinline__ void lds_st(unsigned off, T v) { *(LAS T*)(size_t)off = v; }
template <typename T> __device__ __forceinline__ T lds_vld(unsigned off) { return *(const volatile LAS T*)(size_t)off; }
__device__ __forceinline__ void lds_vst(unsigned off, unsigned v) { CBAR(); *(volatile LAS unsigned*)(size_t)off = v; }
#define lds_inc(off) asm volatile("ds_add_u32 %0, %1 offset:%2" :: "v"(0u), "v"(1u), "i"(off) : "memory")
__device__ __forceinline__ v2u tr4(unsigned a) { return __builtin_bit_cast(v2u, __builtin_amdgcn_ds_read_tr16_b64_v4i16((LAS v4s*)(size_t)a)); }
__device__ __forceinline__ v4u tr8(unsigned a0, unsigned a1) { const v2u x = tr4(a0), y = tr4(a1); return v4u{x.x, x.y, y.x, y.y}; }
template <int SLEEP> __device__ __forceinline__ void wait_cnt(unsigned off, unsigned n) {
#pragma nounroll
  for (unsigned s = 0; s < LSPIN_CAP; ++s) { if (rfl(lds_vld<unsigned>(off)) >= n) break; if (SLEEP) __builtin_amdgcn_s_sleep(SLEEP); }
  CBAR();
}

#define LOOP_END(n) "s_sub_u32 " n ", " n ", 1\n\ts_cmp_lg_u32 " n ", 0\n\ts_cbranch_scc1 1b"
#define PIECE(v, b, g) "global_load_lds_dwordx4 " v ", " b " nt\n\ts_add_u32 m0, m0, 0x400\n\tv_add_u32 " v ", " g ", " v "\n\t"
template <int N, int DEP> __device__ __forceinline__ void dma1(unsigned& voff, unsigned lds, const void* base, unsigned gstep) {
  static_assert(N % 2 == 0, "");
  unsigned n = N / 2;
  asm volatile("s_mov_b32 m0, %2\n\t1:\n\t.rept 2\n\ts_waitcnt vmcnt(%5)\n\t" PIECE("%0", "%3", "%4") ".endr\n\t" LOOP_END("%1")
               : "+v"(voff), "+s"(n) : "s"(lds), "s"(base), "s"(gstep), "i"(DEP - 1) : "memory", "scc");
}
template <int N, int NT> __device__ __forceinline__ void burst(unsigned voff, unsigned lds, const void* base, unsigned gstep) {
  asm volatile("s_mov_b32 m0, %1\n\ts_nop 0\n\t.rept %4\n\t.if %5\n\tglobal_load_lds_dwordx4 %0, %2 nt\n\t.else\n\tglobal_load_lds_dwordx4 %0, %2\n\t.endif\n\t"
               "s_add_u32 m0, m0, 0x400\n\tv_add_u32 %0, %3, %0\n\t.endr" : "+v"(voff) : "s"(lds), "s"(base), "s"(gstep), "i"(N), "i"(NT) : "memory", "scc");
}
template <int NG, int DEP> __device__ __forceinline__ void dma4(unsigned (&v)[4], unsigned lds, const void* base) {
  unsigned n = NG;
  if (NG > 0)
    asm volatile("s_mov_b32 m0, %5\n\t1:\n\t"
                 "s_waitcnt vmcnt(%7)\n\tglobal_load_lds_dwordx4 %0, %6 nt\n\tv_add_u32 %0, 0x1000, %0\n\t"
                 "s_waitcnt vmcnt(%7)\n\tglobal_load_lds_dwordx4 %1, %6 offset:1024 nt\n\tv_add_u32 %1, 0x1000, %1\n\t"
                 "s_waitcnt vmcnt(%7)\n\tglobal_load_lds_dwordx4 %2, %6 offset:2048 nt\n\tv_add_u32 %2, 0x1000, %2\n\t"
                 "s_waitcnt vmcnt(%7)\n\tglobal_load_lds_dwordx4 %3, %6 offset:3072 nt\n\tv_add_u32 %3, 0x1000, %3\n\t"
                 "s_add_u32 m0, m0, 0x1000\n\t" LOOP_END("%4")
                 : "+v"(v[0]), "+v"(v[1]), "+v"(v[2]), "+v"(v[3]), "+s"(n) : "s"(lds), "s"(base), "i"(DEP - 1) : "memory", "scc");
}
template <int NG, int DEP> __device__ __forceinline__ void dma3(unsigned (&v)[3], unsigned lds, const void* base, unsigned gstep) {
  unsigned n = NG;
  asm volatile("s_mov_b32 m0, %4\n\t1:\n\ts_waitcnt vmcnt(%7)\n\t" PIECE("%0", "%5", "%6") "s_waitcnt vmcnt(%7)\n\t" PIECE("%1", "%5", "%6")
               "s_waitcnt vmcnt(%7)\n\t" PIECE("%2", "%5", "%6") LOOP_END("%3")
               : "+v"(v[0]), "+v"(v[1]), "+v"(v[2]), "+s"(n) : "s"(lds), "s"(base), "s"(gstep), "i"(DEP - 1) : "memory", "scc");
}
template <int NG, int A> __device__ __forceinline__ void ramp3(unsigned (&v)[3], unsigned lds, const void* base, unsigned gstep) {
  asm volatile("s_mov_b32 m0, %3\n\t.set rk_, %6\n\t.rept %7\n\ts_waitcnt vmcnt(rk_)\n\t" PIECE("%0", "%4", "%5") PIECE("%1", "%4", "%5") PIECE("%2", "%4", "%5")
               ".set rk_, rk_ - 1\n\t.endr" : "+v"(v[0]), "+v"(v[1]), "+v"(v[2]) : "s"(lds), "s"(base), "s"(gstep), "i"(A), "i"(NG) : "memory", "scc");
}
template <int WAIT> __device__ __forceinline__ void touch(unsigned voff, const void* base) {
  asm volatile("s_mov_b32 m0, %2\n\ts_waitcnt vmcnt(%3)\n\tglobal_load_lds_dword %0, %1" :: "v"(voff), "s"(base), "s"(W_OFF - 256u), "i"(WAIT) : "memory");
}
static_assert(M_END <= W_OFF - 256, "LDS");

#define LDW(off, soff) __builtin_amdgcn_raw_buffer_load_b128(ws, off, soff, AUX_SC1)
#define STW(v, off) __builtin_amdgcn_raw_buffer_store_b128(v, ws, off, 0, AUX_SC1)
__device__ __forceinline__ v4u tag4(v4f v, unsigned T0, unsigned T1) {
  unsigned m = 0xfffff000u; asm("" : "+v"(m));
  return v4u{(asu(v[0]) & m) | T0, asu(v[1]), (asu(v[2]) & m) | T1, asu(v[3])};
}
__device__ __forceinline__ v4f untag4(v4u v, unsigned T0, unsigned T1, unsigned& bad) {
  const unsigned a = v.x ^ T0, c = v.z ^ T1; bad |= a | c;
  return v4f{asf(a), asf(v.y), asf(c), asf(v.w)};
}
#define ALL_OK(bad) (!__any(((bad) & 0xfffu) != 0u))
#define GET_TAGS() const v2u tg_ = lds_ld<v2u>(TG_OFF); const unsigned T0 = rfl(tg_.x), T1 = rfl(tg_.y)
template <int STEP, bool WGT> __device__ __forceinline__ v4f settle(v4u (&t)[4], v4f wg, unsigned off, __amdgpu_buffer_rsrc_t ws, unsigned T0, unsigned T1) {
  v4f a;
#pragma nounroll
  for (unsigned spin = 1;; ++spin) {
    unsigned bad = 0;
    const v4f a0 = untag4(t[0], T0, T1, bad), a1 = untag4(t[1], T0, T1, bad), a2 = untag4(t[2], T0, T1, bad), a3 = untag4(t[3], T0, T1, bad);
    a = WGT ? (a0 * wg[0] + a1 * wg[1]) + (a2 * wg[2] + a3 * wg[3]) : (a0 + a1) + (a2 + a3);
    PIN(a);
    if (__builtin_expect(ALL_OK(spin == SPIN_CAP ? 0u : bad), 1)) break;
    CBAR();
#pragma unroll
    for (int i = 0; i < 4; ++i) t[i] = LDW(off, i * STEP);
  }
  return a;
}
template <int N, int SL> __device__ __forceinline__ unsigned latest(unsigned (&hq)[N], __amdgpu_buffer_rsrc_t ws, unsigned T0) {
  const unsigned tref = now32();
  int late = -100000;
#pragma nounroll
  for (unsigned spin = 0; spin < SPIN_CAP; ++spin) {
    v2u t[N]; bool more = false;
    CBAR();
#pragma unroll
    for (int i = 0; i < N; ++i) t[i] = __builtin_amdgcn_raw_buffer_load_b64(ws, hq[i], 0, AUX_SC1);
#pragma unroll
    for (int i = 0; i < N; ++i) { const bool ok = hq[i] != WS_END && t[i].x == T0; late = ok ? max(late, (int)(t[i].y - tref)) : late; hq[i] = ok ? WS_END : hq[i]; more |= hq[i] != WS_END; }
    if (!__any(more)) break;
    __builtin_amdgcn_s_sleep(SL);
  }
  return tref + min((int)asf(rfl(asu(wave_max((float)late)))), (int)(now32() - tref) + 400);
}


__global__ void __launch_bounds__(NTHR, 4) fused(Params p) {
  __shared__ __attribute__((aligned(16))) char lds_raw[LDS_BYTES];
  u64 pc0; asm volatile("s_getpc_b64 %0" : "=s"(pc0));
  const int b = blockIdx.x, tid = threadIdx.x, wave = rfl(tid >> 6), lane = tid & 63;
  const int g = (b & 7) * 2 + ((b >> 3) & 1), j = b >> 4;
  const bool is_wo = j < 6, is_mlp = j >= 6 && j < 15;
  const int u = g * 9 + (j - 6);
  const int red = ((j + 1) & 15) * 16 + g;
  const __amdgpu_buffer_rsrc_t ws = __builtin_amdgcn_make_buffer_rsrc(p.ws, 0, WS_END, 0x00020000);
  const int r = lane >> 4, c = lane & 15;
  const int q4 = c >> 2, p4 = c & 3;
  const int qt = j >> 2, qc = j & 3;
  const v4f zero = {0.f, 0.f, 0.f, 0.f};
  asm volatile("" :: "v"((LAS char*)lds_raw));
  if (__builtin_expect(wave == 0, 1)) {
    asm volatile("s_mov_b32 m0, %2\n\ts_nop 0\n\tglobal_load_lds_dwordx4 %0, %3\n\tglobal_load_lds_dwordx4 %1, %3 offset:1024"
                 :: "v"(lane * 16), "v"(min(lane * 16u, 112u)), "s"(W_OFF + 18 * 1024), "s"(p.x) : "memory");
    burst<18, 1>((144 * qt + (lane >> 3)) * (QW * 2) + (256 * g + 64 * qc) * 2 + (lane & 7) * 16, W_OFF, p.wq, 8u * QW * 2);
    const size_t hb = ((size_t)g * SK + (SK - WIN) + 256 * j) * DH;
    const u16 *kb = p.k + hb, *vb = p.v + hb;
    unsigned vk[4], vv[4];
#pragma unroll
    for (int n = 0; n < 4; ++n) { vk[n] = r * 256 + ((c ^ (4 * n + r)) << 4); vv[n] = r * 256 + ((c ^ ((r << 2) | n)) << 4); }
    static_assert(DA % 4 == 0 && DA >= 20 && DB % 4 == 0 && DB >= DA && DC % 4 == 0 && DC >= DB && DC >= 24 && DC <= 32, "stage positions");
    bar();
    dma4<1, DA>(vk, K_OFF, kb);
    VMW(22); lds_vst(F_L, 1u);
    dma4<DA / 4 - 1, DA>(vk, K_OFF + 4096, kb);
    VMW(DA); lds_vst(F_L, 2u);
    dma4<(32 + DB - DA) / 4, DB>(vk, K_OFF + DA * 1024, kb);
    VMW(DB); lds_vst(F_L, 3u);
    dma4<(32 - DB) / 4, DB>(vk, K_OFF + (32 + DB) * 1024, kb);
    dma4<DC / 4, DC>(vv, V_OFF, vb);
    VMW(DC); lds_vst(F_L, 4u);
    dma4<(64 - DC) / 4, DC>(vv, V_OFF + DC * 1024, vb);
    VMW(32); lds_vst(F_L, 6u);
    if (!is_wo) {
      const unsigned xh = b & 7, hh = (b >> 3) & 1, id = is_mlp ? hh * 9 + j - 6 : 18 * JM + hh, st = is_mlp ? 18 : 2;
      const unsigned j1 = min(lane / 9, 6) * (FF * 2) + xh * 1152 + (lane % 9) * 128;
      static_assert(18 * JM + 2 * JS >= 83, "jobs");
#define JOB(k, WAIT) touch<WAIT>(min(j1 + (id + (k) * st) * 7u * (FF * 2), (D - 1u) * (FF * 2) + xh * 1152u), p.w1);
      JOB(0, 20)
      VMW(17); lds_vst(F_L, 7u);
      JOB(1, 13)
      VMW(10); lds_vst(F_L, 8u);
      JOB(2, 6)
      VMW(3); lds_vst(F_L, 9u);
      if (is_mlp) {
        unsigned v2 = 32 * u * (D * 2) + lane * 16;
#pragma unroll
        for (int k = 3; k < JM; ++k) JOB(k, 2)
        wait_cnt<0>(F_KD, 4);
        VMW(2); dma1<8, 63>(v2, W2_OFF, p.w2, 1024u);
        VMW(9); dma1<8, 63>(v2, W2_OFF + 8192, p.w2, 1024u);
        VMW(16); dma1<20, DC>(v2, W2_OFF + 16384, p.w2, 1024u);
        lds_vst(F_T, now32()); lds_vst(F_W, 2u);
        VMW(MSTART); lds_vst(F_P1, 1u);
        VMW(0); lds_vst(F_W, 3u);
      } else {
#pragma unroll
        for (int k = 3; k < JS; ++k) JOB(k, 3)
        VMW(0);
      }
#undef JOB
    } else {
      unsigned vo[3];
#pragma unroll
      for (int i = 0; i < 3; ++i) { const int pi = i * 64 + lane; vo[i] = (256 * g + pi / 12) * (D * 2) + 192 * j + (pi % 12) * 16; }
      wait_cnt<0>(F_PUB, 4);
      constexpr int K7 = (DC - 16) / 4, K8 = (DC - 8) / 4, K9 = DC / 4;
      ramp3<K7 - 1, DC - 4>(vo, W_OFF, p.wo, 16u * D * 2);
      VMW(16 + 3 * (K7 - 1)); lds_vst(F_L, 7u);
      ramp3<K8 - K7, DC - 3 - K7>(vo, W_OFF + (K7 - 1) * 3072, p.wo, 16u * D * 2);
      VMW(8 + 3 * (K8 - 1)); lds_vst(F_L, 8u);
      ramp3<K9 - K8, DC - 3 - K8>(vo, W_OFF + (K8 - 1) * 3072, p.wo, 16u * D * 2);
      VMW(3 * (K9 - 1)); lds_vst(F_L, 9u);
      lds_vst(F_T, now32() + CWO); lds_vst(F_W, 1u);
      wait_cnt<0>(F_KD, 4);
      dma3<17 - K9, 3 * K9>(vo, W_OFF + (K9 - 1) * 3072, p.wo, 16u * D * 2);
      VMW(WOEARLY); lds_vst(F_W, 2u);
      VMW(0); lds_vst(F_W, 3u);
    }
  } else {
    if (wave == 1) lds_st<unsigned>((lane & 31) * 4, (lane & 16) ? SENT : 0u);
    if (j == 15 && wave >= 8 && wave <= 12) {
      v4u t;
      asm volatile("global_load_dwordx4 %0, %1, %2\n\tglobal_load_dwordx4 %0, %1, %2 offset:1024\n\tglobal_load_dwordx4 %0, %1, %2 offset:2048\n\t"
                   "global_load_dwordx4 %0, %1, %2 offset:3072\n\ts_barrier\n\ts_waitcnt vmcnt(0)"
                   : "=&v"(t) : "v"(min((wave - 8) * 4096u, CODEB - 4096u) + lane * 16), "s"(pc0) : "memory");
    } else {
      bar();
    }
  }
  if (wave == 1) {
    wait_cnt<1>(F_L, 1);
    const v4u xa = lds_ld<v4u>(W_OFF + 18 * 1024 + lane * 16);
    float xv[9];
#pragma unroll
    for (int k = 0; k < 4; ++k) { xv[2 * k] = lo16(xa[k]); xv[2 * k + 1] = hi16(xa[k]); }
    xv[8] = lo16(lds_ld<u16>(W_OFF + 19 * 1024 + lane * 2));
    lds_st<v4f>(X_OFF + lane * 32, v4f{xv[0], xv[1], xv[2], xv[3]}); lds_st<v4f>(X_OFF + lane * 32 + 16, v4f{xv[4], xv[5], xv[6], xv[7]});
    lds_st<float>(X_OFF + 2048 + lane * 4, xv[8]);
    {
      const float c0 = asf(rfl(asu(xv[0])));
      float s = 0.f, q = 0.f;
#pragma unroll
      for (int i = 0; i < 9; ++i) { const float d = xv[i] - c0; s += d; q = fmaf(d, d, q); }
      s = wave_sum(s); q = wave_sum(q);
      const float md = s * (1.f / D), var = fmaxf(q * (1.f / D) - md * md, 0.f);
      const float mb = rb(c0 + md), rden = 1.f / rb(__builtin_sqrtf(rb(rb(var) + 1e-5f)));
#pragma unroll
      for (int i = 0; i < 9; ++i) xv[i] = rb(xv[i] - mb) * rden;
      lds_st<v4u>(Z_OFF + lane * 16, v4u{pk(xv[0], xv[1]), pk(xv[2], xv[3]), pk(xv[4], xv[5]), pk(xv[6], xv[7])});
      lds_st<u16>(Z_OFF + 1024 + lane * 2, (u16)pk(xv[8], xv[8]));
    }
    lds_vst(F_Y, 1u);
    wait_cnt<1>(F_PUB, 4);
    GET_TAGS();
    const v4u tq = {T0 & 63u, T0 >> 6, T1 & 63u, T1 >> 6};
    unsigned qa = QP_OFF + g * 4096 + lane * 16;
    PIN(qa);
    v4u ga[4], gb[4];
#define QLD(G_) { CBAR(); _Pragma("unroll") for (int t = 0; t < 4; ++t) G_[t] = LDW(qa, t * 1024); SB(); }
#define QCK(G_) { unsigned bad = 0; _Pragma("unroll") for (int t = 0; t < 4; ++t) { G_[t] ^= tq; bad |= G_[t].x | G_[t].y | G_[t].z | G_[t].w; } \
    const bool ok = !__any((bad & 63u) != 0u) || spin + 1 == SPIN_CAP; \
    const v4f qs = (F4(G_[0]) + F4(G_[1])) + (F4(G_[2]) + F4(G_[3])); \
    lds_st<v2u>(Q_OFF + lane * 8, v2u{pk(qs[0], qs[1]), pk(qs[2], qs[3])}); lds_vst(F_Q, ok ? 1u : 0u); SB(); \
    if (ok) break; }
    __builtin_amdgcn_s_sleep(QDELAY);
    QLD(ga)
    __builtin_amdgcn_s_sleep(QSTAG);
#pragma nounroll
    for (unsigned spin = 0;; ++spin) { QLD(gb) QCK(ga) QLD(ga) QCK(gb) }
#undef QLD
#undef QCK
  } else if (wave >= 4 && wave <= 7) {
    const int w = wave - 4;
    if (w == 0) {
      const unsigned ep = *(const __attribute__((address_space(4))) unsigned*)(p.ws + EP_OFF);
      lds_st<v2u>(TG_OFF, v2u{(ep + 1u) & 0xfffu, ((ep >> 16) + 1u) & 0xfffu});
    }
    unsigned ka[4], va[2][2];
#pragma unroll
    for (int s = 0; s < 4; ++s) { ka[s] = K_OFF + (16 * w + c) * 256 + (((4 * s + r) ^ c) << 4); PIN(ka[s]); }
#pragma unroll
    for (int tt = 0; tt < 2; ++tt)
#pragma unroll
      for (int t = 0; t < 2; ++t) { va[tt][t] = V_OFF + 256 * (8 * r + 4 * t + q4) + 16 * ((2 * (2 * w + tt) + (p4 >> 1)) ^ ((q4 << 2) | ((2 * r + t) & 3))) + 8 * (p4 & 1); PIN(va[tt][t]); }
    unsigned opo = oob(c < 4, OP_OFF + (g * 16 + j) * 1024 + ((c & 1) * 128 + 32 * w + 8 * (c & 2) + 4 * r) * 4);
    unsigned wa = W_OFF + (8 * r + q4) * 128 + p4 * 8 + w * 32, wb = W_OFF + (128 + 4 * r + q4) * 128 + p4 * 8 + w * 32;
    unsigned qo = oob(c == 0, QP_OFF + ((g * 4 + qt) * 256 + 64 * qc + 16 * w + 4 * r) * 4);
    PIN(opo); PIN(wa); PIN(wb); PIN(qo);
#pragma nounroll
    for (unsigned s = 0; s < LSPIN_CAP; ++s) { if (rfl(lds_vld<unsigned>(TG_OFF)) != SENT) break; __builtin_amdgcn_s_sleep(1); }
    CBAR();
    GET_TAGS();
    {
      wait_cnt<1>(F_Y, 1);
      v4u yf[4], wf[4];
#pragma unroll
      for (int s = 0; s < 4; ++s) yf[s] = lds_ld<v4u>(Z_OFF + 288 * qt + 16 * r + 64 * s);
      const v2u yl = lds_ld<v2u>(Z_OFF + 288 * qt + 256 + 8 * r);
      wait_cnt<0>(F_L, 2);
#pragma unroll
      for (int s = 0; s < 4; ++s) wf[s] = tr8(wa + s * 4096, wa + s * 4096 + 512);
      const v2u wl = tr4(wb);
      SB();
      const v4f qa = mfma(wf[3], yf[3], mfma(wf[2], yf[2], mfma(wf[1], yf[1], mfma(wf[0], yf[0], zero)))) +
                     __builtin_amdgcn_mfma_f32_16x16x16bf16_1k(__builtin_bit_cast(v4s, wl), __builtin_bit_cast(v4s, yl), zero, 0, 0, 0);
      const v4u tq = {T0 & 63u, T0 >> 6, T1 & 63u, T1 >> 6};
      STW(((U4(qa) + 32u) & ~63u) | tq, qo);
      if (lane == 0) lds_inc(F_PUB);
    }
    v4u kf[2][4], qf[4]; v4f dt[4];
    wait_cnt<2>(F_L, 3);
#pragma unroll
    for (int t = 0; t < 2; ++t)
#pragma unroll
      for (int s = 0; s < 4; ++s) kf[t][s] = lds_ld<v4u>(ka[s] + t * 16384);
    SB();
    wait_cnt<1>(F_Q, 1);
#pragma unroll
    for (int s = 0; s < 4; ++s) qf[s] = lds_ld<v4u>(Q_OFF + (c & 1) * 256 + r * 16 + s * 64);
    SB();
#pragma unroll
    for (int t = 0; t < 2; ++t) dt[t] = mfma(qf[3], kf[t][3], mfma(qf[2], kf[t][2], mfma(qf[1], kf[t][1], mfma(qf[0], kf[t][0], zero))));
    SB();
    wait_cnt<0>(F_L, 4);
#pragma unroll
    for (int t = 0; t < 2; ++t)
#pragma unroll
      for (int s = 0; s < 4; ++s) kf[t][s] = lds_ld<v4u>(ka[s] + (2 + t) * 16384);
    SB();
    if (lane == 0) lds_inc(F_KD);
#pragma unroll
    for (int t = 0; t < 2; ++t) dt[2 + t] = mfma(qf[3], kf[t][3], mfma(qf[2], kf[t][2], mfma(qf[1], kf[t][1], mfma(qf[0], kf[t][0], zero))));
    const bool r1 = r & 1, r2 = r & 2;
    float s0 = r2 ? (r1 ? dt[3][0] : dt[2][0]) : (r1 ? dt[1][0] : dt[0][0]);
    float s1 = r2 ? (r1 ? dt[3][1] : dt[2][1]) : (r1 ? dt[1][1] : dt[0][1]);
    s0 = rb(rb(s0) * 0.08838834764831845f); s1 = rb(rb(s1) * 0.08838834764831845f);
    {
      float m = m32(s0, s1);
      m = row_max(m16(m, m));
      lds_st<unsigned>(MX_OFF + (lane >> 5) * 16 + w * 4, min(asu(m), SENT - 1u));
    }
    v4u ea, eb;
#pragma nounroll
    for (unsigned s = 0; s < LSPIN_CAP; ++s) {
      ea = lds_vld<v4u>(MX_OFF); eb = lds_vld<v4u>(MX_OFF + 16);
      if (rfl(max(max(max(ea.x, ea.y), max(ea.z, ea.w)), max(max(eb.x, eb.y), max(eb.z, eb.w)))) != SENT) break;
    }
    const float M0 = fmaxf(fmaxf(asf(ea.x), asf(ea.y)), fmaxf(asf(ea.z), asf(ea.w))), M1 = fmaxf(fmaxf(asf(eb.x), asf(eb.y)), fmaxf(asf(eb.z), asf(eb.w)));
    const unsigned pp = pk(__builtin_amdgcn_exp2f((s0 - M0) * 1.4426950408889634f), __builtin_amdgcn_exp2f((s1 - M1) * 1.4426950408889634f));
    lds_st<u16>(P_OFF + (64 * r + 16 * w + c) * 2, (u16)pp); lds_st<u16>(P_OFF + 512 + (64 * r + 16 * w + c) * 2, (u16)(pp >> 16));
    if (lane == 0) lds_inc(F_P);
    wait_cnt<0>(F_P, 4);
    v4u pf[8];
#pragma unroll
    for (int s = 0; s < 8; ++s) pf[s] = lds_ld<v4u>(P_OFF + (c & 1) * 512 + r * 16 + s * 64);
    if (w == 0) {
      const v4u ones = {0x3f803f80u, 0x3f803f80u, 0x3f803f80u, 0x3f803f80u};
      v4f ls = zero;
#pragma unroll
      for (int s = 0; s < 8; ++s) ls = mfma(pf[s], ones, ls);
      STW(tag4(v4f{M0, ls[0], M1, ls[1]}, T0, T1), oob(lane < 8, ST_OFF + (g * 16 + j) * 128 + lane * 16));
    }
    v4f oa = zero, ob = zero;
#define PV(s0_, n_) { v4u fa[n_], fb[n_]; \
      _Pragma("unroll") for (int s = 0; s < n_; ++s) { fa[s] = tr8(va[0][0] + (s0_ + s) * 8192, va[0][1] + (s0_ + s) * 8192); fb[s] = tr8(va[1][0] + (s0_ + s) * 8192, va[1][1] + (s0_ + s) * 8192); } \
      SB(); _Pragma("unroll") for (int s = 0; s < n_; ++s) { oa = mfma(fa[s], pf[s0_ + s], oa); ob = mfma(fb[s], pf[s0_ + s], ob); } SB(); }
    wait_cnt<0>(F_L, 6); PV(0, 4)
    wait_cnt<0>(F_L, 7); PV(4, 2)
    wait_cnt<0>(F_L, 8); PV(6, 1)
#undef PV
    {
      v4u fa, fb;
#pragma nounroll
      for (unsigned sp = 0; sp < LSPIN_CAP; ++sp) {
        const unsigned f = lds_vld<unsigned>(F_L);
        CBAR();
        fa = tr8(va[0][0] + 7 * 8192, va[0][1] + 7 * 8192); fb = tr8(va[1][0] + 7 * 8192, va[1][1] + 7 * 8192);
        SB();
        if (rfl(f) >= 9u) break;
      }
      oa = mfma(fa, pf[7], oa); ob = mfma(fb, pf[7], ob);
    }
    STW(tag4((c & 2) ? ob : oa, T0, T1), opo);
  }

  if (is_wo) {
    if (wave >= 4 && wave <= 7) {
      const int w = wave - 4;
      unsigned oo = OP_OFF + (g * 16 + 4 * r) * 1024 + (64 * w + 4 * c) * 4, ol = O_OFF + (64 * w + 4 * c + r) * 2;
      PIN(oo); PIN(ol);
      GET_TAGS();
      wait_cnt<1>(F_W, 2);
      v4u t[4];
#pragma unroll
      for (int i = 0; i < 4; ++i) t[i] = LDW(oo, i * 1024);
      SB();
      wait_cnt<0>(F_GO, 1);
      v4f wg = lds_ld<v4f>(WG_OFF + (w >> 1) * 64 + r * 16);
      PIN(wg);
      SB();
      const v4f a = settle<1024, true>(t, wg, oo, ws, T0, T1);
      const float o = t16(t32(a[0], a[2]), t32(a[1], a[3]));
      lds_st<u16>(ol, (u16)pk(o, o));
      bar();
    } else if (wave >= 8 && wave <= 13) {
      const int t = wave - 8;
      unsigned ho = oob(c == 0, HP_OFF + (g * 576 + j * 96 + 16 * t + 4 * r) * 4), oa = O_OFF + r * 16, wa = W_OFF + (8 * r + q4) * 192 + t * 32 + p4 * 8;
      PIN(ho); PIN(oa); PIN(wa);
      wait_cnt<8>(F_W, 1);
      wait_cnt<2>(F_W, 2);
      GET_TAGS();
      v4u wf[8], of[8];
      constexpr int S1 = 8 - (WOEARLY + 5) / 6;
#pragma unroll
      for (int s = 0; s < S1; ++s) wf[s] = tr8(wa + s * 6144, wa + s * 6144 + 4 * 192);
      wait_cnt<0>(F_W, 3);
#pragma unroll
      for (int s = S1; s < 8; ++s) wf[s] = tr8(wa + s * 6144, wa + s * 6144 + 4 * 192);
#pragma unroll
      for (int s = 0; s < 8; ++s) PIN(wf[s]);
      bar();
#pragma unroll
      for (int s = 0; s < 8; ++s) of[s] = lds_ld<v4u>(oa + s * 64);
      SB();
      v4f acc = zero;
#pragma unroll
      for (int s = 0; s < 8; ++s) acc = mfma(wf[s], of[s], acc);
      STW(tag4(acc, T0, T1), ho);
    } else {
      if (wave == 1) {
        wait_cnt<2>(F_W, 1);
        GET_TAGS();
        STW((v4u{T0, lds_vld<unsigned>(F_T), T1, 0u}), oob(lane < (j == 5 ? 3 : 1), H2_OFF + g * 128 + (j + lane) * 16));
        v4f st;
#pragma nounroll
        for (unsigned spin = 0; spin < SPIN_CAP; ++spin) {
          unsigned bad = 0;
          CBAR();
          st = untag4(LDW(ST_OFF + (g * 16 + c) * 128 + r * 16, 0), T0, T1, bad);
          if (ALL_OK(bad)) break;
          __builtin_amdgcn_s_sleep(4);
        }
        const float mp = (r & 1) ? st[2] : st[0], lp = (r & 1) ? st[3] : st[1];
        float wgt = __builtin_amdgcn_exp2f((mp - row_max(mp)) * 1.4426950408889634f);
        wgt *= __builtin_amdgcn_rcpf(row_sum(wgt * lp));
        lds_st<float>(WG_OFF + lane * 4, wgt);
        lds_vst(F_GO, 1u);
      }
      bar();
    }
  } else if (is_mlp) {
    if (wave >= 8 && wave <= 11) {
      unsigned za = Z_OFF + (c >> 3) * 128 + (8 * r + (c & 7)) * 4, w2a = W2_OFF + (8 * r + q4) * 1152 + 9 * (wave - 8) * 32 + p4 * 8;
      unsigned mo = oob(p4 < 2 || c == 2, MP_OFF + (9 * (wave - 8) + 4 * p4 + q4) * 9216 + u * 64 + r * 16);
      float b1v = lo16(((const GAS u16*)p.b1)[32 * u + 8 * r + (c & 7)]);
      v4u g0 = {0u, 0u, 0u, 0u}, g1 = g0, g2 = g0, g3 = g0, w2f[9];
      PIN(za); PIN(w2a); PIN(mo); PIN(b1v); PIN(g0); PIN(g1); PIN(g2); PIN(g3);
      wait_cnt<8>(F_W, 2);
      GET_TAGS();
      bool have = false;
#pragma nounroll
      for (unsigned sp = 0; sp < LSPIN_CAP; ++sp) {
        const v2u f = lds_vld<v2u>(F_W);
        have = rfl(f.x) >= 3u;
        if (have || rfl(f.y) >= 72u) break;
        __builtin_amdgcn_s_sleep(1);
      }
      CBAR();
      if (have) {
#pragma unroll
        for (int t = 0; t < 9; ++t) w2f[t] = tr8(w2a + t * 32, w2a + t * 32 + 4 * 1152);
      }
      wait_cnt<2>(F_Z, 1);
      wait_cnt<0>(F_Z, 72);
      const v4f sa = lds_ld<v4f>(S_OFF), sb = lds_ld<v4f>(S_OFF + 16), sc = lds_ld<v4f>(S_OFF + 32), sd = lds_ld<v4f>(S_OFF + 48);
      const v2f se = lds_ld<v2f>(S_OFF + 64);
      float zz[9];
#pragma unroll
      for (int i = 0; i < 9; ++i) zz[i] = lds_ld<float>(za + i * 256);
      SB();
      const v4f s4 = (sa + sb) + (sc + sd);
      const float mean = ((s4[1] + s4[3]) + se[1]) * (1.f / D), var = fmaxf(((s4[0] + s4[2]) + se[0]) * (1.f / D) - mean * mean, 0.f);
      const float rr = __builtin_amdgcn_rsqf(fmaf(var, 1.f + 1e-5f, 1e-10f));
      const v2f zp = (v2f{zz[0], zz[1]} + v2f{zz[2], zz[3]}) + (v2f{zz[4], zz[5]} + v2f{zz[6], zz[7]});
      const float t = (zp[0] + zz[8]) + zp[1];
      const float zb = rb(rb((t - mean * dpp<XOR8>(t)) * rr) + b1v);
      const float e = __builtin_amdgcn_exp2f(zb * fmaf(zb * zb, 2.302208198144325f * 0.044715f, 2.302208198144325f));
      const float gl = zb - zb * __builtin_amdgcn_rcpf(e + 1.f);
      const unsigned gp = pk(gl, dpp<XOR1>(gl));
      if (!have) {
        wait_cnt<0>(F_W, 3);
#pragma unroll
        for (int t2 = 0; t2 < 9; ++t2) w2f[t2] = tr8(w2a + t2 * 32, w2a + t2 * 32 + 4 * 1152);
      }
#define GB(G_, bk) G_[0] = __builtin_amdgcn_update_dpp(G_[0], gp, NEWB + 0, 0xf, 1 << (bk), false); G_[1] = __builtin_amdgcn_update_dpp(G_[1], gp, NEWB + 2, 0xf, 1 << (bk), false); \
                   G_[2] = __builtin_amdgcn_update_dpp(G_[2], gp, NEWB + 4, 0xf, 1 << (bk), false); G_[3] = __builtin_amdgcn_update_dpp(G_[3], gp, NEWB + 6, 0xf, 1 << (bk), false);
      GB(g0, 0) GB(g1, 1) GB(g2, 2) GB(g3, 3)
#undef GB
      const v4f o2 = mfma(w2f[8], g0, zero);
      const v4f o0 = mfma(w2f[3], g3, mfma(w2f[2], g2, mfma(w2f[1], g1, mfma(w2f[0], g0, zero))));
      const v4f o1 = mfma(w2f[7], g3, mfma(w2f[6], g2, mfma(w2f[5], g1, mfma(w2f[4], g0, zero))));
      v4f o;
#pragma unroll
      for (int k = 0; k < 4; ++k) { const float a0 = o0[k], a1 = o1[k], a2 = o2[k]; const float lo_ = (c & 2) ? a2 : a0; o[k] = (c & 1) ? a1 : lo_; }
      STW(tag4(o, T0, T1), mo);
    } else if (wave == 1) {
      unsigned hq[2] = {oob((lane & 7) < 6, H2_OFF + lane * 16), oob((lane & 7) < 6, H2_OFF + 1024 + lane * 16)};
      unsigned h3o = oob(lane < 8, H3_OFF + u * 128 + lane * 16);
      PIN(h3o);
      wait_cnt<8>(F_L, 9);
      GET_TAGS();
      __builtin_amdgcn_s_sleep(HWAIT);
      const unsigned due = latest<2, 10>(hq, ws, T0);
      lds_vst(F_GO + 4, due); lds_vst(F_GO, 1u);
      const unsigned ta = now32(), go = (int)(due - ta) <= 0 ? ta + 10 : due;
      wait_cnt<1>(F_W, 2);
      const unsigned own = rfl(lds_vld<unsigned>(F_T)) + DELTAW, all = go + DELTA3;
      STW((v4u{T0, (int)(own - all) > 0 ? own : all, T1, 0u}), h3o);
    } else if (wave != 0) {
      const int ch = wave >= 12 ? (wave == 14 ? 8 : wave - 12) : wave;
      unsigned ho = HP_OFF + (r * 576 + 64 * ch + 4 * c) * 4, zw = Z_OFF + ch * 256 + q4 * 128 + r * 16, sw = S_OFF + r * 128 + ch * 8 + q4 * 4;
      unsigned v1 = (64 * ch + (lane >> 2)) * (FF * 2) + 64 * u + (lane & 3) * 16, w1a = W1_OFF + (64 * ch + 4 * q4 + r) * 64 + p4 * 8;
      v4u f0 = {0x3f803f80u, 0x3f803f80u, 0x3f803f80u, 0x3f803f80u}, f1 = f0, hv[4], w1f[2][2];
      PIN(ho); PIN(zw); PIN(sw); PIN(v1); PIN(w1a); PIN(f0); PIN(f1);
      wait_cnt<8>(F_L, 9);
      wait_cnt<2>(F_KD, 4);
      GET_TAGS();
      float xv = lds_ld<float>(X_OFF + (64 * ch + 4 * c + r) * 4);
      PIN(xv);
      auto take_w1 = [&]() {
        burst<4, 0>(v1, W1_OFF + ch * 4096, p.w1, 16u * FF * 2);
        VMW(0);
#pragma unroll
        for (int s = 0; s < 2; ++s)
#pragma unroll
          for (int t = 0; t < 2; ++t) w1f[s][t] = tr8(w1a + s * 2048 + t * 32, w1a + s * 2048 + t * 32 + 16 * 64);
        PIN(w1f[0][0]); PIN(w1f[0][1]); PIN(w1f[1][0]); PIN(w1f[1][1]);
      };
      bool early = true;
#pragma nounroll
      for (unsigned sp = 0; sp < LSPIN_CAP; ++sp) {
        const v4u f = lds_vld<v4u>(F_GO);
        early = rfl(f.z) != 0u;
        if (early || (rfl(f.x) != 0u && (int)(now32() - rfl(f.y)) >= -20)) break;
        __builtin_amdgcn_s_sleep(1);
      }
      CBAR();
      if (early) take_w1();
      wait_cnt<2>(F_GO, 1);
      {
        const unsigned t0 = now32(), dl = min(max((int)(rfl(lds_vld<unsigned>(F_GO + 4)) - t0), 0), 400);
        while (now32() - t0 + 12 < dl) __builtin_amdgcn_s_sleep(2);
        while (now32() - t0 < dl) {}
      }
      CBAR();
#pragma unroll
      for (int i = 0; i < 4; ++i) hv[i] = LDW(ho, i * 4 * 2304);
      SB();
      if (!early) { wait_cnt<0>(F_P1, 1); take_w1(); }
      const v4f a = settle<4 * 2304, false>(hv, zero, ho, ws, T0, T1);
      const float hs = xv + rb(t16(t32(a[0], a[2]), t32(a[1], a[3])));
      const unsigned pr = pk(hs, dpp<XOR1>(hs));
#define HB(F_, k, n) F_[k] = __builtin_amdgcn_update_dpp(F_[k], pr, NEWB + (n), 0xf, 0xd, false);
      HB(f0, 0, 0) HB(f0, 1, 2) HB(f0, 2, 4) HB(f0, 3, 6) HB(f1, 0, 8) HB(f1, 1, 10) HB(f1, 2, 12) HB(f1, 3, 14)
#undef HB
      const v4f z0 = mfma(w1f[1][0], f1, mfma(w1f[0][0], f0, zero)), z1 = mfma(w1f[1][1], f1, mfma(w1f[0][1], f0, zero)), sq = mfma(f1, f1, mfma(f0, f0, zero));
      if ((c & 11) == 0) { lds_st<v4f>(zw, z0); lds_st<v4f>(zw + 64, z1); lds_st<float>(sw, sq[0]); lds_inc(F_Z); }
      if (u == 0) __builtin_amdgcn_raw_buffer_store_b32((pr << 16) | T0, ws, HR_OFF + (64 * ch + 4 * c + r) * 4, 0, AUX_SC1);
    }
  }
  if (red < 36 && wave == 1) {
    const int col = 16 * red + 4 * (lane & 3) + r;
    float b2v = lo16(((const GAS u16*)p.b2)[col]);
    unsigned mo = MP_OFF + red * 9216 + lane * 16, hro = HR_OFF + col * 4, oo = col * 2, hq[3], hu;
    PIN(b2v); PIN(mo); PIN(hro); PIN(oo);
#pragma unroll
    for (int i = 0; i < 3; ++i) hq[i] = oob(i * 64 + lane < 144, H3_OFF + (i * 64 + lane) * 128);
    wait_cnt<8>(F_L, 9);
    GET_TAGS();
    if (j == 15) __builtin_amdgcn_s_sleep(64);
    const unsigned go = latest<3, 6>(hq, ws, T0);
    while ((int)(now32() - go) < 0) {}
    v4f a;
#pragma nounroll
    for (unsigned spin = 0; spin < SPIN_CAP; ++spin) {
      CBAR();
      hu = __builtin_amdgcn_raw_buffer_load_b32(ws, hro, 0, AUX_SC1);
      SB();
      v4u mv[9];
#pragma unroll
      for (int i = 0; i < 9; ++i) { mv[i] = LDW(mo + (i & 3) * 1024, (i & ~3) * 1024); SB(); }
      hu ^= T0;
      unsigned bad = hu;
      a = untag4(mv[0], T0, T1, bad);
#pragma unroll
      for (int i = 1; i < 9; ++i) { a += untag4(mv[i], T0, T1, bad); PIN(a); SB(); }
      if (ALL_OK(bad)) break;
    }
    float t = t16(t32(a[0], a[2]), t32(a[1], a[3]));
    t += dpp<XOR8>(t); t += dpp<XOR7>(dpp<XOR3>(t));
    const float res = asf(hu) + rb(rb(t) + b2v);
    *(GAS u16*)((GAS char*)p.out + oo) = (u16)pk(res, res);
    if (red == 0 && lane == 0) __builtin_amdgcn_raw_buffer_store_b32(T0 | (T1 << 16), ws, EP_OFF, 0, AUX_SC1);
  }
}

extern "C" void kernel_launch(void* const* d_in, const int* in_sizes, int n_in, void* d_out, int out_size, void* d_ws, size_t ws_size, hipStream_t stream) {
  Params p;
  p.x = (const u16*)d_in[0]; p.k = (const u16*)d_in[1]; p.v = (const u16*)d_in[2]; p.wq = (const u16*)d_in[3]; p.wo = (const u16*)d_in[4];
  p.w1 = (const u16*)d_in[6]; p.b1 = (const u16*)d_in[7]; p.w2 = (const u16*)d_in[8]; p.b2 = (const u16*)d_in[9];
  p.out = (u16*)d_out; p.ws = (char*)d_ws;
  hipLaunchKernelGGL(fused, dim3(NBLK), dim3(NTHR), 0, stream, p);
}
```

```cpp
#include <hip/hip_runtime.h>
#include <hip/hip_bf16.h>

typedef unsigned short u16;
typedef unsigned long long u64;
typedef unsigned v4u __attribute__((ext_vector_type(4)));
typedef unsigned v2u __attribute__((ext_vector_type(2)));
typedef float v4f __attribute__((ext_vector_type(4)));
typedef float v2f __attribute__((ext_vector_type(2)));
typedef short v4s __attribute__((ext_vector_type(4)));
typedef __bf16 bf16x2 __attribute__((ext_vector_type(2)));
typedef __bf16 bf16x8 __attribute__((ext_vector_type(8)));
#define GAS __attribute__((address_space(1)))
#define LAS __attribute__((address_space(3)))

#define DA 20
#define DB 24
#define DC 28
#define QDELAY 16
#define QSTAG 16
#define WOEARLY 12
#define CWO 272
#define HSTART 6
#define SLH 10
#define RWAIT 20
#define TV 0
#define CKL 285
#define PHASE ((b*2654435761u)>>28)
#define ALPHA 0.3f
#define MSTART 24
#define DELTA3 156
#define DELTAW 225
#define CODEB 17408
constexpr int JM = 4, JS = 6;

constexpr int D = 576, SK = 65536, DH = 128, WIN = 4096, FF = 4608, QW = 4096;
constexpr int NWAVE = 15, NTHR = NWAVE * 64, NBLK = 256;
constexpr unsigned EP_OFF = 0, QP_OFF = 4096, OP_OFF = QP_OFF + 16 * 4 * 256 * 4, ST_OFF = OP_OFF + 16 * 16 * 256 * 4, HP_OFF = ST_OFF + 256 * 128,
                   MP_OFF = HP_OFF + 16 * 576 * 4, HR_OFF = MP_OFF + 144 * 576 * 4, H2_OFF = HR_OFF + 576 * 4, H1_OFF = H2_OFF + 16 * 128, H3_OFF = H1_OFF + 16 * 128,
                   WS_END = H3_OFF + 144 * 128;
constexpr unsigned SENT = 0xFFFFFFFFu;
constexpr unsigned SPIN_CAP = 1u << 16, LSPIN_CAP = 1u << 22;
constexpr int AUX_SC1 = 16;
constexpr unsigned F_L = 0, F_W = 8, F_Z = 12, F_P = 16, F_KD = 20, F_PUB = 24, F_Y = 28, F_T = 36, F_Q = 40, F_GO = 48  , F_P1 = 56, MX_OFF = 64, TG_OFF = 96,
                   Q_OFF = 128, P_OFF = Q_OFF + 512, WG_OFF = P_OFF + 1024, O_OFF = WG_OFF + 256, S_OFF = O_OFF + 512, X_OFF = S_OFF + 512,
                   Z_OFF = X_OFF + 2304, M_END = Z_OFF + 2304;
constexpr unsigned W_OFF = 8192, K_OFF = W_OFF + 24576, V_OFF = K_OFF + 65536, LDS_BYTES = V_OFF + 65536;
constexpr unsigned W1_OFF = W_OFF, W2_OFF = W_OFF + 36864;
static_assert(M_END <= W_OFF && LDS_BYTES <= 160 * 1024, "LDS");

struct Params { const u16 *x, *k, *v, *wq, *wo, *w1, *b1, *w2, *b2; u16* out; char* ws; };

__device__ __forceinline__ float asf(unsigned u) { return __builtin_bit_cast(float, u); }
__device__ __forceinline__ unsigned asu(float f) { return __builtin_bit_cast(unsigned, f); }
__device__ __forceinline__ float lo16(unsigned u) { return asf(u << 16); }
__device__ __forceinline__ float hi16(unsigned u) { return asf(u & 0xffff0000u); }
__device__ __forceinline__ unsigned pk(float lo, float hi) { bf16x2 r; r.x = (__bf16)lo; r.y = (__bf16)hi; return __builtin_bit_cast(unsigned, r); }
__device__ __forceinline__ float rb(float f) { return lo16(pk(f, f)); }
__device__ __forceinline__ v4f mfma(v4u a, v4u b, v4f c) {
  return __builtin_amdgcn_mfma_f32_16x16x32_bf16(__builtin_bit_cast(bf16x8, a), __builtin_bit_cast(bf16x8, b), c, 0, 0, 0);
}
template <int CTRL> __device__ __forceinline__ float dpp(float x) { return asf(__builtin_amdgcn_mov_dpp(asu(x), CTRL, 0xf, 0xf, true)); }
constexpr int XOR1 = 0xB1, XOR2 = 0x4E, XOR3 = 0x1B, XOR7 = 0x141, XOR8 = 0x128, NEWB = 0x150;
__device__ __forceinline__ float t32(float a, float b) { auto s = __builtin_amdgcn_permlane32_swap(asu(a), asu(b), false, false); return asf(s[0]) + asf(s[1]); }
__device__ __forceinline__ float t16(float a, float b) { auto s = __builtin_amdgcn_permlane16_swap(asu(a), asu(b), false, false); return asf(s[0]) + asf(s[1]); }
__device__ __forceinline__ float m32(float a, float b) { auto s = __builtin_amdgcn_permlane32_swap(asu(a), asu(b), false, false); return fmaxf(asf(s[0]), asf(s[1])); }
__device__ __forceinline__ float m16(float a, float b) { auto s = __builtin_amdgcn_permlane16_swap(asu(a), asu(b), false, false); return fmaxf(asf(s[0]), asf(s[1])); }
__device__ __forceinline__ float row_sum(float x) { x += dpp<XOR1>(x); x += dpp<XOR2>(x); x += dpp<XOR7>(x); x += dpp<XOR8>(x); return x; }
__device__ __forceinline__ float row_max(float x) { x = fmaxf(x, dpp<XOR1>(x)); x = fmaxf(x, dpp<XOR2>(x)); x = fmaxf(x, dpp<XOR7>(x)); x = fmaxf(x, dpp<XOR8>(x)); return x; }
__device__ __forceinline__ float wave_sum(float x) { x = row_sum(x); x = t16(x, x); return t32(x, x); }
__device__ __forceinline__ float wave_max(float x) { x = row_max(x); x = m16(x, x); return m32(x, x); }
__device__ __forceinline__ unsigned rfl(unsigned x) { return __builtin_amdgcn_readfirstlane(x); }
__device__ __forceinline__ unsigned now32() { return (unsigned)__builtin_amdgcn_s_memrealtime(); }
#define CBAR() asm volatile("" ::: "memory")
#define SB() __builtin_amdgcn_sched_barrier(0)
#define PIN(x) asm volatile("" : "+v"(x))
#define VMW(n) asm volatile("s_waitcnt vmcnt(%0)" :: "i"(n) : "memory")
#define F4(v) __builtin_bit_cast(v4f, v)
#define U4(v) __builtin_bit_cast(v4u, v)
__device__ __forceinline__ void bar() { asm volatile("s_waitcnt lgkmcnt(0)" ::: "memory"); __builtin_amdgcn_s_barrier(); CBAR(); }
__device__ __forceinline__ unsigned oob(bool keep, unsigned off) { PIN(off); return keep ? off : WS_END; }

template <typename T> __device__ __forceinline__ T lds_ld(unsigned off) { return *(const LAS T*)(size_t)off; }
template <typename T> __device__ __forceinline__ void lds_st(unsigned off, T v) { *(LAS T*)(size_t)off = v; }
template <typename T> __device__ __forceinline__ T lds_vld(unsigned off) { return *(const volatile LAS T*)(size_t)off; }
__device__ __forceinline__ void lds_vst(unsigned off, unsigned v) { CBAR(); *(volatile LAS unsigned*)(size_t)off = v; }
#define lds_inc(off) asm volatile("ds_add_u32 %0, %1 offset:%2" :: "v"(0u), "v"(1u), "i"(off) : "memory")
__device__ __forceinline__ v2u tr4(unsigned a) { return __builtin_bit_cast(v2u, __builtin_amdgcn_ds_read_tr16_b64_v4i16((LAS v4s*)(size_t)a)); }
__device__ __forceinline__ v4u tr8(unsigned a0, unsigned a1) { const v2u x = tr4(a0), y = tr4(a1); return v4u{x.x, x.y, y.x, y.y}; }
template <int SLEEP> __device__ __forceinline__ void wait_cnt(unsigned off, unsigned n) {
#pragma nounroll
  for (unsigned s = 0; s < LSPIN_CAP; ++s) { if (rfl(lds_vld<unsigned>(off)) >= n) break; if (SLEEP) __builtin_amdgcn_s_sleep(SLEEP); }
  CBAR();
}

#define LOOP_END(n) "s_sub_u32 " n ", " n ", 1\n\ts_cmp_lg_u32 " n ", 0\n\ts_cbranch_scc1 1b"
#define PIECE(v, b, g) "global_load_lds_dwordx4 " v ", " b " nt\n\ts_add_u32 m0, m0, 0x400\n\tv_add_u32 " v ", " g ", " v "\n\t"
template <int N, int DEP> __device__ __forceinline__ void dma1(unsigned& voff, unsigned lds, const void* base, unsigned gstep) {
  static_assert(N % 2 == 0, "");
  unsigned n = N / 2;
  asm volatile("s_mov_b32 m0, %2\n\t1:\n\t.rept 2\n\ts_waitcnt vmcnt(%5)\n\t" PIECE("%0", "%3", "%4") ".endr\n\t" LOOP_END("%1")
               : "+v"(voff), "+s"(n) : "s"(lds), "s"(base), "s"(gstep), "i"(DEP - 1) : "memory", "scc");
}
template <int N, int NT> __device__ __forceinline__ void burst(unsigned voff, unsigned lds, const void* base, unsigned gstep) {
  asm volatile("s_mov_b32 m0, %1\n\ts_nop 0\n\t.rept %4\n\t.if %5\n\tglobal_load_lds_dwordx4 %0, %2 nt\n\t.else\n\tglobal_load_lds_dwordx4 %0, %2\n\t.endif\n\t"
               "s_add_u32 m0, m0, 0x400\n\tv_add_u32 %0, %3, %0\n\t.endr" : "+v"(voff) : "s"(lds), "s"(base), "s"(gstep), "i"(N), "i"(NT) : "memory", "scc");
}
template <int NG, int DEP> __device__ __forceinline__ void dma4(unsigned (&v)[4], unsigned lds, const void* base) {
  unsigned n = NG;
  if (NG > 0)
    asm volatile("s_mov_b32 m0, %5\n\t1:\n\t"
                 "s_waitcnt vmcnt(%7)\n\tglobal_load_lds_dwordx4 %0, %6 nt\n\tv_add_u16 %0, 0x1000, %0\n\t"
                 "s_waitcnt vmcnt(%7)\n\tglobal_load_lds_dwordx4 %1, %6 offset:1024 nt\n\tv_add_u16 %1, 0x1000, %1\n\t"
                 "s_waitcnt vmcnt(%7)\n\tglobal_load_lds_dwordx4 %2, %6 offset:2048 nt\n\tv_add_u16 %2, 0x1000, %2\n\t"
                 "s_waitcnt vmcnt(%7)\n\tglobal_load_lds_dwordx4 %3, %6 offset:3072 nt\n\tv_add_u16 %3, 0x1000, %3\n\t"
                 "s_add_u32 m0, m0, 0x1000\n\t" LOOP_END("%4")
                 : "+v"(v[0]), "+v"(v[1]), "+v"(v[2]), "+v"(v[3]), "+s"(n) : "s"(lds), "s"(base), "i"(DEP - 1) : "memory", "scc");
}
template <int NG, int DEP> __device__ __forceinline__ void dma3(unsigned (&v)[3], unsigned lds, const void* base, unsigned gstep) {
  unsigned n = NG;
  asm volatile("s_mov_b32 m0, %4\n\t1:\n\ts_waitcnt vmcnt(%7)\n\t" PIECE("%0", "%5", "%6") "s_waitcnt vmcnt(%7)\n\t" PIECE("%1", "%5", "%6")
               "s_waitcnt vmcnt(%7)\n\t" PIECE("%2", "%5", "%6") LOOP_END("%3")
               : "+v"(v[0]), "+v"(v[1]), "+v"(v[2]), "+s"(n) : "s"(lds), "s"(base), "s"(gstep), "i"(DEP - 1) : "memory", "scc");
}
template <int NG, int A> __device__ __forceinline__ void ramp3(unsigned (&v)[3], unsigned lds, const void* base, unsigned gstep) {
  asm volatile("s_mov_b32 m0, %3\n\t.set rk_, %6\n\t.rept %7\n\ts_waitcnt vmcnt(rk_)\n\t" PIECE("%0", "%4", "%5") PIECE("%1", "%4", "%5") PIECE("%2", "%4", "%5")
               ".set rk_, rk_ - 1\n\t.endr" : "+v"(v[0]), "+v"(v[1]), "+v"(v[2]) : "s"(lds), "s"(base), "s"(gstep), "i"(A), "i"(NG) : "memory", "scc");
}
template <int WAIT> __device__ __forceinline__ void touch(unsigned voff, const void* base) {
  if (TV) asm volatile("s_waitcnt vmcnt(%2)\n\tglobal_load_dword v83, %0, %1" :: "v"(voff), "s"(base), "i"(WAIT) : "memory", "v83");
  else asm volatile("s_mov_b32 m0, %2\n\ts_waitcnt vmcnt(%3)\n\tglobal_load_lds_dword %0, %1" :: "v"(voff), "s"(base), "s"(W_OFF - 256u), "i"(WAIT) : "memory");
}
static_assert(M_END <= W_OFF - 256, "LDS");

#define LDW(off, soff) __builtin_amdgcn_raw_buffer_load_b128(ws, off, soff, AUX_SC1)
#define STW(v, off) __builtin_amdgcn_raw_buffer_store_b128(v, ws, off, 0, AUX_SC1)
__device__ __forceinline__ v4u tag4(v4f v, unsigned T0, unsigned T1) {
  unsigned m = 0xfffff000u; asm("" : "+v"(m));
  return v4u{(asu(v[0]) & m) | T0, asu(v[1]), (asu(v[2]) & m) | T1, asu(v[3])};
}
__device__ __forceinline__ v4f untag4(v4u v, unsigned T0, unsigned T1, unsigned& bad) {
  const unsigned a = v.x ^ T0, c = v.z ^ T1; bad |= a | c;
  return v4f{asf(a), asf(v.y), asf(c), asf(v.w)};
}
#define ALL_OK(bad) (!__any(((bad) & 0xfffu) != 0u))
#define GET_TAGS() const v2u tg_ = lds_ld<v2u>(TG_OFF); const unsigned T0 = rfl(tg_.x), T1 = rfl(tg_.y)
template <int STEP, bool WGT> __device__ __forceinline__ v4f settle(v4u (&t)[4], v4f wg, unsigned off, __amdgpu_buffer_rsrc_t ws, unsigned T0, unsigned T1) {
  v4f a;
#pragma nounroll
  for (unsigned spin = 1;; ++spin) {
    unsigned bad = 0;
    const v4f a0 = untag4(t[0], T0, T1, bad), a1 = untag4(t[1], T0, T1, bad), a2 = untag4(t[2], T0, T1, bad), a3 = untag4(t[3], T0, T1, bad);
    a = WGT ? (a0 * wg[0] + a1 * wg[1]) + (a2 * wg[2] + a3 * wg[3]) : (a0 + a1) + (a2 + a3);
    PIN(a);
    if (__builtin_expect(ALL_OK(spin == SPIN_CAP ? 0u : bad), 1)) break;
    CBAR();
#pragma unroll
    for (int i = 0; i < 4; ++i) t[i] = LDW(off, i * STEP);
  }
  return a;
}
template <int N, int SL> __device__ __forceinline__ unsigned latest(unsigned (&hq)[N], __amdgpu_buffer_rsrc_t ws, unsigned T0, float alpha) {
  const unsigned tref = now32();
  int late = -100000, sum = 0;
#pragma nounroll
  for (unsigned spin = 0; spin < SPIN_CAP; ++spin) {
    v2u t[N]; bool more = false;
    CBAR();
#pragma unroll
    for (int i = 0; i < N; ++i) t[i] = __builtin_amdgcn_raw_buffer_load_b64(ws, hq[i], 0, AUX_SC1);
#pragma unroll
    for (int i = 0; i < N; ++i) { const bool ok = hq[i] != WS_END && t[i].x == T0; late = ok ? max(late, (int)(t[i].y - tref)) : late; sum += ok ? (int)(t[i].y - tref) : 0; hq[i] = ok ? WS_END : hq[i]; more |= hq[i] != WS_END; }
    if (!__any(more)) break;
    __builtin_amdgcn_s_sleep(SL);
  }
  const float mx = wave_max((float)late);
  return tref + min((int)asf(rfl(asu(mx - alpha * (mx - wave_sum((float)sum) * (1.f / 96))))), (int)(now32() - tref) + 400);
}

__device__ __forceinline__ unsigned hp_due(__amdgpu_buffer_rsrc_t ws, unsigned T0, int lane) {
  unsigned due = 0;
#pragma nounroll
  for (int k = 0; k < 2; ++k) {
    const unsigned o = (k ? H2_OFF : H1_OFF) + lane * 16;
    unsigned hq[2] = {oob((lane & 7) < 6, o), oob((lane & 7) < 6, o + 1024)};
    if (k) while ((int)(now32() - due) < 0) __builtin_amdgcn_s_sleep(2);
    due = latest<2, SLH>(hq, ws, T0, k ? ALPHA : 0.f);
  }
  return due;
}


__global__ void __launch_bounds__(NTHR, 4) fused(Params p) {
  __shared__ __attribute__((aligned(16))) char lds_raw[LDS_BYTES];
  u64 pc0; asm volatile("s_getpc_b64 %0" : "=s"(pc0));
  const int b = blockIdx.x, tid = threadIdx.x, wave = rfl(tid >> 6), lane = tid & 63;
  const int g = (b & 7) * 2 + ((b >> 3) & 1), j = b >> 4;
  const bool is_wo = j < 6, is_mlp = j >= 6 && j < 15;
  const int u = g * 9 + (j - 6);
  const int red = ((j + 1) & 15) * 16 + g;
  const __amdgpu_buffer_rsrc_t ws = __builtin_amdgcn_make_buffer_rsrc(p.ws, 0, WS_END, 0x00020000);
  const int r = lane >> 4, c = lane & 15;
  const int q4 = c >> 2, p4 = c & 3;
  const int qt = j >> 2, qc = j & 3;
  const v4f zero = {0.f, 0.f, 0.f, 0.f};
  asm volatile("" :: "v"((LAS char*)lds_raw));
  if (__builtin_expect(wave == 0, 1)) {
    asm volatile("s_mov_b32 m0, %2\n\ts_nop 0\n\tglobal_load_lds_dwordx4 %0, %3\n\tglobal_load_lds_dwordx4 %1, %3 offset:1024"
                 :: "v"(lane * 16), "v"(min(lane * 16u, 112u)), "s"(W_OFF + 18 * 1024), "s"(p.x) : "memory");
    burst<18, 1>((144 * qt + (lane >> 3)) * (QW * 2) + (256 * g + 64 * qc) * 2 + (lane & 7) * 16, W_OFF, p.wq, 8u * QW * 2);
    const size_t hb = ((size_t)g * SK + (SK - WIN) + 256 * j) * DH;
    const u16 *kb = p.k + hb, *vb = p.v + hb;
    const unsigned ph = (PHASE & 15) << 12;
    unsigned vk[4], vv[4];
#pragma unroll
    for (int n = 0; n < 4; ++n) { vk[n] = ph + r * 256 + ((c ^ (4 * n + r)) << 4); vv[n] = ph + r * 256 + ((c ^ ((r << 2) | n)) << 4); }
    static_assert(DA % 4 == 0 && DA >= 20 && DB % 4 == 0 && DB >= DA && DC % 4 == 0 && DC >= DB && DC >= 24 && DC <= 32, "stage positions");
    bar();
    dma4<1, DA>(vk, K_OFF, kb);
    VMW(22); lds_vst(F_L, 1u);
    dma4<DA / 4 - 1, DA>(vk, K_OFF + 4096, kb);
    VMW(DA); lds_vst(F_L, 2u);
    dma4<(32 + DB - DA) / 4, DB>(vk, K_OFF + DA * 1024, kb);
    VMW(DB); lds_vst(F_L, 3u);
    dma4<(32 - DB) / 4, DB>(vk, K_OFF + (32 + DB) * 1024, kb);
    dma4<DC / 4, DC>(vv, V_OFF, vb);
    VMW(DC); lds_vst(F_L, 4u);
    dma4<(64 - DC) / 4, DC>(vv, V_OFF + DC * 1024, vb);
    VMW(32); lds_vst(F_L, 6u);
    if (!is_wo) {
      const unsigned xh = b & 7, hh = (b >> 3) & 1, id = is_mlp ? hh * 9 + j - 6 : 18 * JM + hh, st = is_mlp ? 18 : 2;
      const unsigned j1 = min(lane / 9, 6) * (FF * 2) + xh * 1152 + (lane % 9) * 128;
      static_assert(18 * JM + 2 * JS >= 83, "jobs");
#define JOB(k, WAIT) touch<WAIT>(min(j1 + (id + (k) * st) * 7u * (FF * 2), (D - 1u) * (FF * 2) + xh * 1152u), p.w1);
      JOB(0, 20)
      VMW(17); lds_vst(F_L, 7u);
      JOB(1, 13)
      VMW(10); lds_vst(F_L, 8u);
      JOB(2, 6)
      VMW(3); lds_vst(F_L, 9u);
      if (is_mlp) {
        unsigned v2 = 32 * u * (D * 2) + lane * 16;
#pragma unroll
        for (int k = 3; k < JM; ++k) JOB(k, 2)
        wait_cnt<0>(F_KD, 4);
        VMW(2); dma1<8, 63>(v2, W2_OFF, p.w2, 1024u);
        VMW(9); dma1<8, 63>(v2, W2_OFF + 8192, p.w2, 1024u);
        VMW(16); dma1<20, DC>(v2, W2_OFF + 16384, p.w2, 1024u);
        lds_vst(F_T, now32()); lds_vst(F_W, 2u);
        VMW(MSTART); lds_vst(F_P1, 1u);
        VMW(0); lds_vst(F_W, 3u);
      } else {
#pragma unroll
        for (int k = 3; k < JS; ++k) JOB(k, 3)
        VMW(0);
      }
#undef JOB
    } else {
      unsigned vo[3];
#pragma unroll
      for (int i = 0; i < 3; ++i) { const int pi = i * 64 + lane; vo[i] = (256 * g + pi / 12) * (D * 2) + 192 * j + (pi % 12) * 16; }
      wait_cnt<0>(F_PUB, 4);
      constexpr int K7 = (DC - 16) / 4, K8 = (DC - 8) / 4, K9 = DC / 4;
      ramp3<K7 - 1, DC - 4>(vo, W_OFF, p.wo, 16u * D * 2);
      VMW(16 + 3 * (K7 - 1)); lds_vst(F_L, 7u);
      ramp3<K8 - K7, DC - 3 - K7>(vo, W_OFF + (K7 - 1) * 3072, p.wo, 16u * D * 2);
      VMW(8 + 3 * (K8 - 1)); lds_vst(F_L, 8u);
      ramp3<K9 - K8, DC - 3 - K8>(vo, W_OFF + (K8 - 1) * 3072, p.wo, 16u * D * 2);
      VMW(3 * (K9 - 1)); lds_vst(F_L, 9u);
      lds_vst(F_T, now32() + CWO); lds_vst(F_W, 1u);
      wait_cnt<0>(F_KD, 4);
      dma3<17 - K9, 3 * K9>(vo, W_OFF + (K9 - 1) * 3072, p.wo, 16u * D * 2);
      VMW(WOEARLY); lds_vst(F_W, 2u);
      VMW(0); lds_vst(F_W, 3u);
    }
  } else {
    if (wave == 1) lds_st<unsigned>((lane & 31) * 4, (lane & 16) ? SENT : 0u);
    if (wave == 8 && (b >> 3) * 1024 < CODEB) {
      v4u t;
      asm volatile("global_load_dwordx4 %0, %1, %2\n\ts_barrier\n\ts_waitcnt vmcnt(0)" : "=&v"(t) : "v"(min((b >> 3) * 1024u, CODEB - 1024u) + lane * 16), "s"(pc0) : "memory");
    } else {
      bar();
    }
  }
  if (wave == 1) {
    wait_cnt<1>(F_L, 1);
    const v4u xa = lds_ld<v4u>(W_OFF + 18 * 1024 + lane * 16);
    float xv[9];
#pragma unroll
    for (int k = 0; k < 4; ++k) { xv[2 * k] = lo16(xa[k]); xv[2 * k + 1] = hi16(xa[k]); }
    xv[8] = lo16(lds_ld<u16>(W_OFF + 19 * 1024 + lane * 2));
    lds_st<v4f>(X_OFF + lane * 32, v4f{xv[0], xv[1], xv[2], xv[3]}); lds_st<v4f>(X_OFF + lane * 32 + 16, v4f{xv[4], xv[5], xv[6], xv[7]});
    lds_st<float>(X_OFF + 2048 + lane * 4, xv[8]);
    {
      const float c0 = asf(rfl(asu(xv[0])));
      float s = 0.f, q = 0.f;
#pragma unroll
      for (int i = 0; i < 9; ++i) { const float d = xv[i] - c0; s += d; q = fmaf(d, d, q); }
      s = wave_sum(s); q = wave_sum(q);
      const float md = s * (1.f / D), var = fmaxf(q * (1.f / D) - md * md, 0.f);
      const float mb = rb(c0 + md), rden = 1.f / rb(__builtin_sqrtf(rb(rb(var) + 1e-5f)));
#pragma unroll
      for (int i = 0; i < 9; ++i) xv[i] = rb(xv[i] - mb) * rden;
      lds_st<v4u>(Z_OFF + lane * 16, v4u{pk(xv[0], xv[1]), pk(xv[2], xv[3]), pk(xv[4], xv[5]), pk(xv[6], xv[7])});
      lds_st<u16>(Z_OFF + 1024 + lane * 2, (u16)pk(xv[8], xv[8]));
    }
    lds_vst(F_Y, 1u);
    wait_cnt<1>(F_PUB, 4);
    GET_TAGS();
    const v4u tq = {T0 & 63u, T0 >> 6, T1 & 63u, T1 >> 6};
    unsigned qa = QP_OFF + g * 4096 + lane * 16;
    PIN(qa);
    v4u ga[4], gb[4];
#define QLD(G_) { CBAR(); _Pragma("unroll") for (int t = 0; t < 4; ++t) G_[t] = LDW(qa, t * 1024); SB(); }
#define QCK(G_) { unsigned bad = 0; _Pragma("unroll") for (int t = 0; t < 4; ++t) { G_[t] ^= tq; bad |= G_[t].x | G_[t].y | G_[t].z | G_[t].w; } \
    const bool ok = !__any((bad & 63u) != 0u) || spin + 1 == SPIN_CAP; \
    const v4f qs = (F4(G_[0]) + F4(G_[1])) + (F4(G_[2]) + F4(G_[3])); \
    lds_st<v2u>(Q_OFF + lane * 8, v2u{pk(qs[0], qs[1]), pk(qs[2], qs[3])}); lds_vst(F_Q, ok ? 1u : 0u); SB(); \
    if (ok) break; }
    __builtin_amdgcn_s_sleep(QDELAY);
    QLD(ga)
    __builtin_amdgcn_s_sleep(QSTAG);
#pragma nounroll
    for (unsigned spin = 0;; ++spin) { QLD(gb) QCK(ga) QLD(ga) QCK(gb) }
#undef QLD
#undef QCK
  } else if (wave >= 4 && wave <= 7) {
    const int w = wave - 4;
    if (w == 0) {
      const unsigned ep = *(const __attribute__((address_space(4))) unsigned*)(p.ws + EP_OFF);
      lds_st<v2u>(TG_OFF, v2u{(ep + 1u) & 0xfffu, ((ep >> 16) + 1u) & 0xfffu});
    }
    unsigned ka[4], va[2][2];
#pragma unroll
    for (int s = 0; s < 4; ++s) { ka[s] = K_OFF + (16 * w + c) * 256 + (((4 * s + r) ^ c) << 4); PIN(ka[s]); }
#pragma unroll
    for (int tt = 0; tt < 2; ++tt)
#pragma unroll
      for (int t = 0; t < 2; ++t) { va[tt][t] = V_OFF + 256 * (8 * r + 4 * t + q4) + 16 * ((2 * (2 * w + tt) + (p4 >> 1)) ^ ((q4 << 2) | ((2 * r + t) & 3))) + 8 * (p4 & 1); PIN(va[tt][t]); }
    unsigned opo = oob(c < 4, OP_OFF + (g * 16 + j) * 1024 + ((c & 1) * 128 + 32 * w + 8 * (c & 2) + 4 * r) * 4);
    unsigned wa = W_OFF + (8 * r + q4) * 128 + p4 * 8 + w * 32, wb = W_OFF + (128 + 4 * r + q4) * 128 + p4 * 8 + w * 32;
    unsigned qo = oob(c == 0, QP_OFF + ((g * 4 + qt) * 256 + 64 * qc + 16 * w + 4 * r) * 4);
    PIN(opo); PIN(wa); PIN(wb); PIN(qo);
#pragma nounroll
    for (unsigned s = 0; s < LSPIN_CAP; ++s) { if (rfl(lds_vld<unsigned>(TG_OFF)) != SENT) break; __builtin_amdgcn_s_sleep(1); }
    CBAR();
    GET_TAGS();
    {
      wait_cnt<1>(F_Y, 1);
      v4u yf[4], wf[4];
#pragma unroll
      for (int s = 0; s < 4; ++s) yf[s] = lds_ld<v4u>(Z_OFF + 288 * qt + 16 * r + 64 * s);
      const v2u yl = lds_ld<v2u>(Z_OFF + 288 * qt + 256 + 8 * r);
      wait_cnt<0>(F_L, 2);
#pragma unroll
      for (int s = 0; s < 4; ++s) wf[s] = tr8(wa + s * 4096, wa + s * 4096 + 512);
      const v2u wl = tr4(wb);
      SB();
      const v4f qa = mfma(wf[3], yf[3], mfma(wf[2], yf[2], mfma(wf[1], yf[1], mfma(wf[0], yf[0], zero)))) +
                     __builtin_amdgcn_mfma_f32_16x16x16bf16_1k(__builtin_bit_cast(v4s, wl), __builtin_bit_cast(v4s, yl), zero, 0, 0, 0);
      const v4u tq = {T0 & 63u, T0 >> 6, T1 & 63u, T1 >> 6};
      STW(((U4(qa) + 32u) & ~63u) | tq, qo);
      if (lane == 0) lds_inc(F_PUB);
    }
    v4u kf[2][4], qf[4]; v4f dt[4];
    wait_cnt<2>(F_L, 3);
#pragma unroll
    for (int t = 0; t < 2; ++t)
#pragma unroll
      for (int s = 0; s < 4; ++s) kf[t][s] = lds_ld<v4u>(ka[s] + t * 16384);
    SB();
    wait_cnt<1>(F_Q, 1);
#pragma unroll
    for (int s = 0; s < 4; ++s) qf[s] = lds_ld<v4u>(Q_OFF + (c & 1) * 256 + r * 16 + s * 64);
    SB();
#pragma unroll
    for (int t = 0; t < 2; ++t) dt[t] = mfma(qf[3], kf[t][3], mfma(qf[2], kf[t][2], mfma(qf[1], kf[t][1], mfma(qf[0], kf[t][0], zero))));
    SB();
    wait_cnt<0>(F_L, 4);
#pragma unroll
    for (int t = 0; t < 2; ++t)
#pragma unroll
      for (int s = 0; s < 4; ++s) kf[t][s] = lds_ld<v4u>(ka[s] + (2 + t) * 16384);
    SB();
    if (lane == 0) lds_inc(F_KD);
#pragma unroll
    for (int t = 0; t < 2; ++t) dt[2 + t] = mfma(qf[3], kf[t][3], mfma(qf[2], kf[t][2], mfma(qf[1], kf[t][1], mfma(qf[0], kf[t][0], zero))));
    const bool r1 = r & 1, r2 = r & 2;
    float s0 = r2 ? (r1 ? dt[3][0] : dt[2][0]) : (r1 ? dt[1][0] : dt[0][0]);
    float s1 = r2 ? (r1 ? dt[3][1] : dt[2][1]) : (r1 ? dt[1][1] : dt[0][1]);
    s0 = rb(rb(s0) * 0.08838834764831845f); s1 = rb(rb(s1) * 0.08838834764831845f);
    {
      float m = m32(s0, s1);
      m = row_max(m16(m, m));
      lds_st<unsigned>(MX_OFF + (lane >> 5) * 16 + w * 4, min(asu(m), SENT - 1u));
    }
    v4u ea, eb;
#pragma nounroll
    for (unsigned s = 0; s < LSPIN_CAP; ++s) {
      ea = lds_vld<v4u>(MX_OFF); eb = lds_vld<v4u>(MX_OFF + 16);
      if (rfl(max(max(max(ea.x, ea.y), max(ea.z, ea.w)), max(max(eb.x, eb.y), max(eb.z, eb.w)))) != SENT) break;
    }
    const float M0 = fmaxf(fmaxf(asf(ea.x), asf(ea.y)), fmaxf(asf(ea.z), asf(ea.w))), M1 = fmaxf(fmaxf(asf(eb.x), asf(eb.y)), fmaxf(asf(eb.z), asf(eb.w)));
    const unsigned pp = pk(__builtin_amdgcn_exp2f((s0 - M0) * 1.4426950408889634f), __builtin_amdgcn_exp2f((s1 - M1) * 1.4426950408889634f));
    lds_st<u16>(P_OFF + (64 * r + 16 * w + c) * 2, (u16)pp); lds_st<u16>(P_OFF + 512 + (64 * r + 16 * w + c) * 2, (u16)(pp >> 16));
    if (lane == 0) lds_inc(F_P);
    wait_cnt<0>(F_P, 4);
    v4u pf[8];
#pragma unroll
    for (int s = 0; s < 8; ++s) pf[s] = lds_ld<v4u>(P_OFF + (c & 1) * 512 + r * 16 + s * 64);
    if (w == 0) {
      const v4u ones = {0x3f803f80u, 0x3f803f80u, 0x3f803f80u, 0x3f803f80u};
      v4f ls = zero;
#pragma unroll
      for (int s = 0; s < 8; ++s) ls = mfma(pf[s], ones, ls);
      STW(tag4(v4f{M0, ls[0], M1, ls[1]}, T0, T1), oob(lane < 8, ST_OFF + (g * 16 + j) * 128 + lane * 16));
    }
    v4f oa = zero, ob = zero;
#define PV(s0_, n_) { v4u fa[n_], fb[n_]; \
      _Pragma("unroll") for (int s = 0; s < n_; ++s) { fa[s] = tr8(va[0][0] + (s0_ + s) * 8192, va[0][1] + (s0_ + s) * 8192); fb[s] = tr8(va[1][0] + (s0_ + s) * 8192, va[1][1] + (s0_ + s) * 8192); } \
      SB(); _Pragma("unroll") for (int s = 0; s < n_; ++s) { oa = mfma(fa[s], pf[s0_ + s], oa); ob = mfma(fb[s], pf[s0_ + s], ob); } SB(); }
    wait_cnt<0>(F_L, 6); PV(0, 4)
    wait_cnt<0>(F_L, 7); PV(4, 2)
    wait_cnt<0>(F_L, 8); PV(6, 1)
#undef PV
    {
      v4u fa, fb;
#pragma nounroll
      for (unsigned sp = 0; sp < LSPIN_CAP; ++sp) {
        const unsigned f = lds_vld<unsigned>(F_L);
        CBAR();
        fa = tr8(va[0][0] + 7 * 8192, va[0][1] + 7 * 8192); fb = tr8(va[1][0] + 7 * 8192, va[1][1] + 7 * 8192);
        SB();
        if (rfl(f) >= 9u) break;
      }
      oa = mfma(fa, pf[7], oa); ob = mfma(fb, pf[7], ob);
    }
    STW(tag4((c & 2) ? ob : oa, T0, T1), opo);
  }

  if (is_wo) {
    if (wave >= 4 && wave <= 7) {
      const int w = wave - 4;
      unsigned oo = OP_OFF + (g * 16 + 4 * r) * 1024 + (64 * w + 4 * c) * 4, ol = O_OFF + (64 * w + 4 * c + r) * 2;
      PIN(oo); PIN(ol);
      GET_TAGS();
      wait_cnt<1>(F_W, 2);
      v4u t[4];
#pragma unroll
      for (int i = 0; i < 4; ++i) t[i] = LDW(oo, i * 1024);
      SB();
      wait_cnt<0>(F_GO, 1);
      v4f wg = lds_ld<v4f>(WG_OFF + (w >> 1) * 64 + r * 16);
      PIN(wg);
      SB();
      const v4f a = settle<1024, true>(t, wg, oo, ws, T0, T1);
      const float o = t16(t32(a[0], a[2]), t32(a[1], a[3]));
      lds_st<u16>(ol, (u16)pk(o, o));
      bar();
    } else if (wave >= 8 && wave <= 13) {
      const int t = wave - 8;
      unsigned ho = oob(c == 0, HP_OFF + (g * 576 + j * 96 + 16 * t + 4 * r) * 4), oa = O_OFF + r * 16, wa = W_OFF + (8 * r + q4) * 192 + t * 32 + p4 * 8;
      PIN(ho); PIN(oa); PIN(wa);
      wait_cnt<8>(F_W, 1);
      wait_cnt<2>(F_W, 2);
      GET_TAGS();
      v4u wf[8], of[8];
      constexpr int S1 = 8 - (WOEARLY + 5) / 6;
#pragma unroll
      for (int s = 0; s < S1; ++s) wf[s] = tr8(wa + s * 6144, wa + s * 6144 + 4 * 192);
      wait_cnt<0>(F_W, 3);
#pragma unroll
      for (int s = S1; s < 8; ++s) wf[s] = tr8(wa + s * 6144, wa + s * 6144 + 4 * 192);
#pragma unroll
      for (int s = 0; s < 8; ++s) PIN(wf[s]);
      bar();
#pragma unroll
      for (int s = 0; s < 8; ++s) of[s] = lds_ld<v4u>(oa + s * 64);
      SB();
      v4f acc = zero;
#pragma unroll
      for (int s = 0; s < 8; ++s) acc = mfma(wf[s], of[s], acc);
      STW(tag4(acc, T0, T1), ho);
    } else {
      if (wave == 1) {
        wait_cnt<2>(F_W, 1);
        GET_TAGS();
        v4f st;
#pragma nounroll
        for (unsigned spin = 0; spin < SPIN_CAP; ++spin) {
          unsigned bad = 0;
          CBAR();
          st = untag4(LDW(ST_OFF + (g * 16 + c) * 128 + r * 16, 0), T0, T1, bad);
          if (ALL_OK(bad)) break;
          __builtin_amdgcn_s_sleep(4);
        }
        const float mp = (r & 1) ? st[2] : st[0], lp = (r & 1) ? st[3] : st[1];
        float wgt = __builtin_amdgcn_exp2f((mp - row_max(mp)) * 1.4426950408889634f);
        wgt *= __builtin_amdgcn_rcpf(row_sum(wgt * lp));
        lds_st<float>(WG_OFF + lane * 4, wgt);
        lds_vst(F_GO, 1u);
      } else if (wave == 2) {
        unsigned ho = oob(lane < (j == 5 ? 3 : 1), H2_OFF + g * 128 + (j + lane) * 16);
        PIN(ho);
        wait_cnt<8>(F_PUB, 4);
        GET_TAGS();
        wait_cnt<4>(F_L, 4);
        __builtin_amdgcn_raw_buffer_store_b128(v4u{T0, now32() + CKL, T1, 0u}, ws, ho, H1_OFF - H2_OFF, AUX_SC1);
        wait_cnt<2>(F_W, 1);
        STW((v4u{T0, lds_vld<unsigned>(F_T), T1, 0u}), ho);
      }
      bar();
    }
  } else if (is_mlp) {
    if (wave >= 8 && wave <= 11) {
      unsigned za = Z_OFF + (c >> 3) * 128 + (8 * r + (c & 7)) * 4, w2a = W2_OFF + (8 * r + q4) * 1152 + 9 * (wave - 8) * 32 + p4 * 8;
      unsigned mo = oob(p4 < 2 || c == 2, MP_OFF + (9 * (wave - 8) + 4 * p4 + q4) * 9216 + u * 64 + r * 16);
      float b1v = lo16(((const GAS u16*)p.b1)[32 * u + 8 * r + (c & 7)]);
      v4u g0 = {0u, 0u, 0u, 0u}, g1 = g0, g2 = g0, g3 = g0, w2f[9];
      PIN(za); PIN(w2a); PIN(mo); PIN(b1v); PIN(g0); PIN(g1); PIN(g2); PIN(g3);
      wait_cnt<8>(F_W, 2);
      GET_TAGS();
      bool have = false;
#pragma nounroll
      for (unsigned sp = 0; sp < LSPIN_CAP; ++sp) {
        const v2u f = lds_vld<v2u>(F_W);
        have = rfl(f.x) >= 3u;
        if (have || rfl(f.y) >= 72u) break;
        __builtin_amdgcn_s_sleep(1);
      }
      CBAR();
      if (have) {
#pragma unroll
        for (int t = 0; t < 9; ++t) w2f[t] = tr8(w2a + t * 32, w2a + t * 32 + 4 * 1152);
      }
      wait_cnt<2>(F_Z, 1);
      wait_cnt<0>(F_Z, 72);
      const v4f sa = lds_ld<v4f>(S_OFF), sb = lds_ld<v4f>(S_OFF + 16), sc = lds_ld<v4f>(S_OFF + 32), sd = lds_ld<v4f>(S_OFF + 48);
      const v2f se = lds_ld<v2f>(S_OFF + 64);
      float zz[9];
#pragma unroll
      for (int i = 0; i < 9; ++i) zz[i] = lds_ld<float>(za + i * 256);
      SB();
      const v4f s4 = (sa + sb) + (sc + sd);
      const float mean = ((s4[1] + s4[3]) + se[1]) * (1.f / D), var = fmaxf(((s4[0] + s4[2]) + se[0]) * (1.f / D) - mean * mean, 0.f);
      const float rr = __builtin_amdgcn_rsqf(fmaf(var, 1.f + 1e-5f, 1e-10f));
      const v2f zp = (v2f{zz[0], zz[1]} + v2f{zz[2], zz[3]}) + (v2f{zz[4], zz[5]} + v2f{zz[6], zz[7]});
      const float t = (zp[0] + zz[8]) + zp[1];
      const float zb = rb(rb((t - mean * dpp<XOR8>(t)) * rr) + b1v);
      const float e = __builtin_amdgcn_exp2f(zb * fmaf(zb * zb, 2.302208198144325f * 0.044715f, 2.302208198144325f));
      const float gl = zb - zb * __builtin_amdgcn_rcpf(e + 1.f);
      const unsigned gp = pk(gl, dpp<XOR1>(gl));
      if (!have) {
        wait_cnt<0>(F_W, 3);
#pragma unroll
        for (int t2 = 0; t2 < 9; ++t2) w2f[t2] = tr8(w2a + t2 * 32, w2a + t2 * 32 + 4 * 1152);
      }
#define GB(G_, bk) G_[0] = __builtin_amdgcn_update_dpp(G_[0], gp, NEWB + 0, 0xf, 1 << (bk), false); G_[1] = __builtin_amdgcn_update_dpp(G_[1], gp, NEWB + 2, 0xf, 1 << (bk), false); \
                   G_[2] = __builtin_amdgcn_update_dpp(G_[2], gp, NEWB + 4, 0xf, 1 << (bk), false); G_[3] = __builtin_amdgcn_update_dpp(G_[3], gp, NEWB + 6, 0xf, 1 << (bk), false);
      GB(g0, 0) GB(g1, 1) GB(g2, 2) GB(g3, 3)
#undef GB
      const v4f o2 = mfma(w2f[8], g0, zero);
      const v4f o0 = mfma(w2f[3], g3, mfma(w2f[2], g2, mfma(w2f[1], g1, mfma(w2f[0], g0, zero))));
      const v4f o1 = mfma(w2f[7], g3, mfma(w2f[6], g2, mfma(w2f[5], g1, mfma(w2f[4], g0, zero))));
      v4f o;
#pragma unroll
      for (int k = 0; k < 4; ++k) { const float a0 = o0[k], a1 = o1[k], a2 = o2[k]; const float lo_ = (c & 2) ? a2 : a0; o[k] = (c & 1) ? a1 : lo_; }
      STW(tag4(o, T0, T1), mo);
    } else if (wave == 1) {
      unsigned h3o = oob(lane < 8, H3_OFF + u * 128 + lane * 16);
      PIN(h3o);
      wait_cnt<4>(F_L, HSTART);
      GET_TAGS();
      const unsigned due = hp_due(ws, T0, lane);
      lds_vst(F_GO + 4, due); lds_vst(F_GO, 1u);
      const unsigned ta = now32(), go = (int)(due - ta) <= 0 ? ta + 10 : due;
      wait_cnt<1>(F_W, 2);
      const unsigned own = rfl(lds_vld<unsigned>(F_T)) + DELTAW, all = go + DELTA3;
      STW((v4u{T0, (int)(own - all) > 0 ? own : all, T1, 0u}), h3o);
    } else if (wave != 0) {
      const int ch = wave >= 12 ? (wave == 14 ? 8 : wave - 12) : wave;
      unsigned ho = HP_OFF + (r * 576 + 64 * ch + 4 * c) * 4, zw = Z_OFF + ch * 256 + q4 * 128 + r * 16, sw = S_OFF + r * 128 + ch * 8 + q4 * 4;
      unsigned v1 = (64 * ch + (lane >> 2)) * (FF * 2) + 64 * u + (lane & 3) * 16, w1a = W1_OFF + (64 * ch + 4 * q4 + r) * 64 + p4 * 8;
      v4u f0 = {0x3f803f80u, 0x3f803f80u, 0x3f803f80u, 0x3f803f80u}, f1 = f0, hv[4], w1f[2][2];
      PIN(ho); PIN(zw); PIN(sw); PIN(v1); PIN(w1a); PIN(f0); PIN(f1);
      wait_cnt<8>(F_L, 9);
      wait_cnt<2>(F_KD, 4);
      GET_TAGS();
      float xv = lds_ld<float>(X_OFF + (64 * ch + 4 * c + r) * 4);
      PIN(xv);
      auto take_w1 = [&]() {
        burst<4, 0>(v1, W1_OFF + ch * 4096, p.w1, 16u * FF * 2);
        VMW(0);
#pragma unroll
        for (int s = 0; s < 2; ++s)
#pragma unroll
          for (int t = 0; t < 2; ++t) w1f[s][t] = tr8(w1a + s * 2048 + t * 32, w1a + s * 2048 + t * 32 + 16 * 64);
        PIN(w1f[0][0]); PIN(w1f[0][1]); PIN(w1f[1][0]); PIN(w1f[1][1]);
      };
      bool early = true;
#pragma nounroll
      for (unsigned sp = 0; sp < LSPIN_CAP; ++sp) {
        const v4u f = lds_vld<v4u>(F_GO);
        early = rfl(f.z) != 0u;
        if (early || (rfl(f.x) != 0u && (int)(now32() - rfl(f.y)) >= -20)) break;
        __builtin_amdgcn_s_sleep(1);
      }
      CBAR();
      if (early) take_w1();
      wait_cnt<2>(F_GO, 1);
      {
        const unsigned t0 = now32(), dl = min(max((int)(rfl(lds_vld<unsigned>(F_GO + 4)) - t0), 0), 400);
        while (now32() - t0 + 12 < dl) __builtin_amdgcn_s_sleep(2);
        while (now32() - t0 < dl) {}
      }
      CBAR();
#pragma unroll
      for (int i = 0; i < 4; ++i) hv[i] = LDW(ho, i * 4 * 2304);
      SB();
      if (!early) { wait_cnt<0>(F_P1, 1); take_w1(); }
      const v4f a = settle<4 * 2304, false>(hv, zero, ho, ws, T0, T1);
      const float hs = xv + rb(t16(t32(a[0], a[2]), t32(a[1], a[3])));
      const unsigned pr = pk(hs, dpp<XOR1>(hs));
#define HB(F_, k, n) F_[k] = __builtin_amdgcn_update_dpp(F_[k], pr, NEWB + (n), 0xf, 0xd, false);
      HB(f0, 0, 0) HB(f0, 1, 2) HB(f0, 2, 4) HB(f0, 3, 6) HB(f1, 0, 8) HB(f1, 1, 10) HB(f1, 2, 12) HB(f1, 3, 14)
#undef HB
      const v4f z0 = mfma(w1f[1][0], f1, mfma(w1f[0][0], f0, zero)), z1 = mfma(w1f[1][1], f1, mfma(w1f[0][1], f0, zero)), sq = mfma(f1, f1, mfma(f0, f0, zero));
      if ((c & 11) == 0) { lds_st<v4f>(zw, z0); lds_st<v4f>(zw + 64, z1); lds_st<float>(sw, sq[0]); lds_inc(F_Z); }
      if (u == 0) __builtin_amdgcn_raw_buffer_store_b32((pr << 16) | T0, ws, HR_OFF + (64 * ch + 4 * c + r) * 4, 0, AUX_SC1);
    }
  }
  if (red < 36 && wave == 1) {
    const int col = 16 * red + 4 * (lane & 3) + r;
    float b2v = lo16(((const GAS u16*)p.b2)[col]);
    unsigned mo = MP_OFF + red * 9216 + lane * 16, hro = HR_OFF + col * 4, oo = col * 2, hq[3], hu;
    PIN(b2v); PIN(mo); PIN(hro); PIN(oo);
#pragma unroll
    for (int i = 0; i < 3; ++i) hq[i] = oob(i * 64 + lane < 144, H3_OFF + (i * 64 + lane) * 128);
    wait_cnt<8>(F_L, 9);
    GET_TAGS();
    if (j == 15) __builtin_amdgcn_s_sleep(16);
    if (RWAIT >= 0) {
      const unsigned due = hp_due(ws, T0, lane) + RWAIT;
      while ((int)(now32() - due) < 0) __builtin_amdgcn_s_sleep(2);
    }
    const unsigned go = latest<3, 2>(hq, ws, T0, 0.f);
    while ((int)(now32() - go) < 0) {}
    v4f a;
#pragma nounroll
    for (unsigned spin = 0; spin < SPIN_CAP; ++spin) {
      CBAR();
      hu = __builtin_amdgcn_raw_buffer_load_b32(ws, hro, 0, AUX_SC1);
      SB();
      v4u mv[9];
#pragma unroll
      for (int i = 0; i < 9; ++i) { mv[i] = LDW(mo + (i & 3) * 1024, (i & ~3) * 1024); SB(); }
      hu ^= T0;
      unsigned bad = hu;
      a = untag4(mv[0], T0, T1, bad);
#pragma unroll
      for (int i = 1; i < 9; ++i) { a += untag4(mv[i], T0, T1, bad); PIN(a); SB(); }
      if (ALL_OK(bad)) break;
    }
    float t = t16(t32(a[0], a[2]), t32(a[1], a[3]));
    t += dpp<XOR8>(t); t += dpp<XOR7>(dpp<XOR3>(t));
    const float res = asf(hu) + rb(rb(t) + b2v);
    *(GAS u16*)((GAS char*)p.out + oo) = (u16)pk(res, res);
    if (red == 0 && lane == 0) __builtin_amdgcn_raw_buffer_store_b32(T0 | (T1 << 16), ws, EP_OFF, 0, AUX_SC1);
  }
}

extern "C" void kernel_launch(void* const* d_in, const int* in_sizes, int n_in, void* d_out, int out_size, void* d_ws, size_t ws_size, hipStream_t stream) {
  Params p;
  p.x = (const u16*)d_in[0]; p.k = (const u16*)d_in[1]; p.v = (const u16*)d_in[2]; p.wq = (const u16*)d_in[3]; p.wo = (const u16*)d_in[4];
  p.w1 = (const u16*)d_in[6]; p.b1 = (const u16*)d_in[7]; p.w2 = (const u16*)d_in[8]; p.b2 = (const u16*)d_in[9];
  p.out = (u16*)d_out; p.ws = (char*)d_ws;
  hipLaunchKernelGGL(fused, dim3(NBLK), dim3(NTHR), 0, stream, p);
}
```

```cpp
#include <hip/hip_runtime.h>
#include <hip/hip_bf16.h>

typedef unsigned short u16;
typedef unsigned long long u64;
typedef unsigned v4u __attribute__((ext_vector_type(4)));
typedef unsigned v2u __attribute__((ext_vector_type(2)));
typedef float v4f __attribute__((ext_vector_type(4)));
typedef float v2f __attribute__((ext_vector_type(2)));
typedef short v4s __attribute__((ext_vector_type(4)));
typedef __bf16 bf16x2 __attribute__((ext_vector_type(2)));
typedef __bf16 bf16x8 __attribute__((ext_vector_type(8)));
#define GAS __attribute__((address_space(1)))
#define LAS __attribute__((address_space(3)))

#define DA 20
#define DB 24
#define DC 28
#define QDELAY 36
#define WOEARLY 12
#define CWO 270
#define HSTART 6
#define SLH 10
#define RWAIT 20
#define CKL 335
#define PHASE (j+g)
#define ALPHA 0.3f
#define ALPHA1 0.4f
#define LATEADD 25
#define KPF 1
#define EPE 0
#define OAUX 0
#define MSTART 24
#define DELTA3 156
#define DELTAW 225
#define CODEB 17152
constexpr int JM = 4, JS = 6;

constexpr int D = 576, SK = 65536, DH = 128, WIN = 4096, FF = 4608, QW = 4096;
constexpr int NWAVE = 15, NTHR = NWAVE * 64, NBLK = 256;
constexpr unsigned EP_OFF = 0, QP_OFF = 4096, OP_OFF = QP_OFF + 16 * 4 * 256 * 4, ST_OFF = OP_OFF + 16 * 16 * 256 * 4, HP_OFF = ST_OFF + 256 * 128,
                   MP_OFF = HP_OFF + 16 * 576 * 4, HR_OFF = MP_OFF + 144 * 576 * 4, H2_OFF = HR_OFF + 576 * 4, H1_OFF = H2_OFF + 16 * 128, H3_OFF = H1_OFF + 16 * 128,
                   WS_END = H3_OFF + 144 * 128;
constexpr unsigned SENT = 0xFFFFFFFFu;
constexpr unsigned SPIN_CAP = 1u << 16, LSPIN_CAP = 1u << 22;
constexpr int AUX_SC1 = 16;
constexpr unsigned F_L = 0, F_W = 8, F_Z = 12, F_P = 16, F_KD = 20, F_PUB = 24, F_Y = 28, F_T = 36, F_Q = 40, F_GO = 48  , F_P1 = 56, MX_OFF = 64, TG_OFF = 96,
                   Q_OFF = 128, P_OFF = Q_OFF + 512, WG_OFF = P_OFF + 1024, O_OFF = WG_OFF + 256, S_OFF = O_OFF + 512, X_OFF = S_OFF + 512,
                   Z_OFF = X_OFF + 2304, M_END = Z_OFF + 2304;
constexpr unsigned W_OFF = 8192, K_OFF = W_OFF + 24576, V_OFF = K_OFF + 65536, LDS_BYTES = V_OFF + 65536;
constexpr unsigned W1_OFF = W_OFF, W2_OFF = W_OFF + 36864;
static_assert(M_END <= W_OFF && LDS_BYTES <= 160 * 1024, "LDS");

struct Params { const u16 *x, *k, *v, *wq, *wo, *w1, *b1, *w2, *b2; u16* out; char* ws; };

__device__ __forceinline__ float asf(unsigned u) { return __builtin_bit_cast(float, u); }
__device__ __forceinline__ unsigned asu(float f) { return __builtin_bit_cast(unsigned, f); }
__device__ __forceinline__ float lo16(unsigned u) { return asf(u << 16); }
__device__ __forceinline__ float hi16(unsigned u) { return asf(u & 0xffff0000u); }
__device__ __forceinline__ unsigned pk(float lo, float hi) { bf16x2 r; r.x = (__bf16)lo; r.y = (__bf16)hi; return __builtin_bit_cast(unsigned, r); }
__device__ __forceinline__ float rb(float f) { return lo16(pk(f, f)); }
__device__ __forceinline__ v4f mfma(v4u a, v4u b, v4f c) {
  return __builtin_amdgcn_mfma_f32_16x16x32_bf16(__builtin_bit_cast(bf16x8, a), __builtin_bit_cast(bf16x8, b), c, 0, 0, 0);
}
template <int CTRL> __device__ __forceinline__ float dpp(float x) { return asf(__builtin_amdgcn_mov_dpp(asu(x), CTRL, 0xf, 0xf, true)); }
constexpr int XOR1 = 0xB1, XOR2 = 0x4E, XOR3 = 0x1B, XOR7 = 0x141, XOR8 = 0x128, NEWB = 0x150;
__device__ __forceinline__ float t32(float a, float b) { auto s = __builtin_amdgcn_permlane32_swap(asu(a), asu(b), false, false); return asf(s[0]) + asf(s[1]); }
__device__ __forceinline__ float t16(float a, float b) { auto s = __builtin_amdgcn_permlane16_swap(asu(a), asu(b), false, false); return asf(s[0]) + asf(s[1]); }
__device__ __forceinline__ float m32(float a, float b) { auto s = __builtin_amdgcn_permlane32_swap(asu(a), asu(b), false, false); return fmaxf(asf(s[0]), asf(s[1])); }
__device__ __forceinline__ float m16(float a, float b) { auto s = __builtin_amdgcn_permlane16_swap(asu(a), asu(b), false, false); return fmaxf(asf(s[0]), asf(s[1])); }
__device__ __forceinline__ float row_sum(float x) { x += dpp<XOR1>(x); x += dpp<XOR2>(x); x += dpp<XOR7>(x); x += dpp<XOR8>(x); return x; }
__device__ __forceinline__ float row_max(float x) { x = fmaxf(x, dpp<XOR1>(x)); x = fmaxf(x, dpp<XOR2>(x)); x = fmaxf(x, dpp<XOR7>(x)); x = fmaxf(x, dpp<XOR8>(x)); return x; }
__device__ __forceinline__ float wave_sum(float x) { x = row_sum(x); x = t16(x, x); return t32(x, x); }
__device__ __forceinline__ float wave_max(float x) { x = row_max(x); x = m16(x, x); return m32(x, x); }
__device__ __forceinline__ unsigned rfl(unsigned x) { return __builtin_amdgcn_readfirstlane(x); }
__device__ __forceinline__ unsigned now32() { return (unsigned)__builtin_amdgcn_s_memrealtime(); }
#define CBAR() asm volatile("" ::: "memory")
#define SB() __builtin_amdgcn_sched_barrier(0)
#define PIN(x) asm volatile("" : "+v"(x))
#define VMW(n) asm volatile("s_waitcnt vmcnt(%0)" :: "i"(n) : "memory")
#define F4(v) __builtin_bit_cast(v4f, v)
#define U4(v) __builtin_bit_cast(v4u, v)
__device__ __forceinline__ void bar() { asm volatile("s_waitcnt lgkmcnt(0)" ::: "memory"); __builtin_amdgcn_s_barrier(); CBAR(); }
__device__ __forceinline__ unsigned oob(bool keep, unsigned off) { PIN(off); return keep ? off : WS_END; }

template <typename T> __device__ __forceinline__ T lds_ld(unsigned off) { return *(const LAS T*)(size_t)off; }
template <typename T> __device__ __forceinline__ void lds_st(unsigned off, T v) { *(LAS T*)(size_t)off = v; }
template <typename T> __device__ __forceinline__ T lds_vld(unsigned off) { return *(const volatile LAS T*)(size_t)off; }
__device__ __forceinline__ void lds_vst(unsigned off, unsigned v) { CBAR(); *(volatile LAS unsigned*)(size_t)off = v; }
#define lds_inc(off) asm volatile("ds_add_u32 %0, %1 offset:%2" :: "v"(0u), "v"(1u), "i"(off) : "memory")
__device__ __forceinline__ v2u tr4(unsigned a) { return __builtin_bit_cast(v2u, __builtin_amdgcn_ds_read_tr16_b64_v4i16((LAS v4s*)(size_t)a)); }
__device__ __forceinline__ v4u tr8(unsigned a0, unsigned a1) { const v2u x = tr4(a0), y = tr4(a1); return v4u{x.x, x.y, y.x, y.y}; }
template <int SLEEP> __device__ __forceinline__ void wait_cnt(unsigned off, unsigned n) {
#pragma nounroll
  for (unsigned s = 0; s < LSPIN_CAP; ++s) { if (rfl(lds_vld<unsigned>(off)) >= n) break; if (SLEEP) __builtin_amdgcn_s_sleep(SLEEP); }
  CBAR();
}

#define LOOP_END(n) "s_sub_u32 " n ", " n ", 1\n\ts_cmp_lg_u32 " n ", 0\n\ts_cbranch_scc1 1b"
#define PIECE(v, b, g) "global_load_lds_dwordx4 " v ", " b " nt\n\ts_add_u32 m0, m0, 0x400\n\tv_add_u32 " v ", " g ", " v "\n\t"
template <int N, int DEP> __device__ __forceinline__ void dma1(unsigned& voff, unsigned lds, const void* base, unsigned gstep) {
  static_assert(N % 2 == 0, "");
  unsigned n = N / 2;
  asm volatile("s_mov_b32 m0, %2\n\t1:\n\t.rept 2\n\ts_waitcnt vmcnt(%5)\n\t" PIECE("%0", "%3", "%4") ".endr\n\t" LOOP_END("%1")
               : "+v"(voff), "+s"(n) : "s"(lds), "s"(base), "s"(gstep), "i"(DEP - 1) : "memory", "scc");
}
template <int N, int NT> __device__ __forceinline__ void burst(unsigned voff, unsigned lds, const void* base, unsigned gstep) {
  asm volatile("s_mov_b32 m0, %1\n\ts_nop 0\n\t.rept %4\n\t.if %5\n\tglobal_load_lds_dwordx4 %0, %2 nt\n\t.else\n\tglobal_load_lds_dwordx4 %0, %2\n\t.endif\n\t"
               "s_add_u32 m0, m0, 0x400\n\tv_add_u32 %0, %3, %0\n\t.endr" : "+v"(voff) : "s"(lds), "s"(base), "s"(gstep), "i"(N), "i"(NT) : "memory", "scc");
}
template <int NG, int DEP> __device__ __forceinline__ void dma4(unsigned (&v)[4], unsigned lds, const void* base) {
  unsigned n = NG;
  if (NG > 0)
    asm volatile("s_mov_b32 m0, %5\n\t1:\n\t"
                 "s_waitcnt vmcnt(%7)\n\tglobal_load_lds_dwordx4 %0, %6 nt\n\tv_add_u16 %0, 0x1000, %0\n\t"
                 "s_waitcnt vmcnt(%7)\n\tglobal_load_lds_dwordx4 %1, %6 offset:1024 nt\n\tv_add_u16 %1, 0x1000, %1\n\t"
                 "s_waitcnt vmcnt(%7)\n\tglobal_load_lds_dwordx4 %2, %6 offset:2048 nt\n\tv_add_u16 %2, 0x1000, %2\n\t"
                 "s_waitcnt vmcnt(%7)\n\tglobal_load_lds_dwordx4 %3, %6 offset:3072 nt\n\tv_add_u16 %3, 0x1000, %3\n\t"
                 "s_add_u32 m0, m0, 0x1000\n\t" LOOP_END("%4")
                 : "+v"(v[0]), "+v"(v[1]), "+v"(v[2]), "+v"(v[3]), "+s"(n) : "s"(lds), "s"(base), "i"(DEP - 1) : "memory", "scc");
}
template <int NG, int DEP> __device__ __forceinline__ void dma3(unsigned (&v)[3], unsigned lds, const void* base, unsigned gstep) {
  unsigned n = NG;
  asm volatile("s_mov_b32 m0, %4\n\t1:\n\ts_waitcnt vmcnt(%7)\n\t" PIECE("%0", "%5", "%6") "s_waitcnt vmcnt(%7)\n\t" PIECE("%1", "%5", "%6")
               "s_waitcnt vmcnt(%7)\n\t" PIECE("%2", "%5", "%6") LOOP_END("%3")
               : "+v"(v[0]), "+v"(v[1]), "+v"(v[2]), "+s"(n) : "s"(lds), "s"(base), "s"(gstep), "i"(DEP - 1) : "memory", "scc");
}
template <int NG, int A> __device__ __forceinline__ void ramp3(unsigned (&v)[3], unsigned lds, const void* base, unsigned gstep) {
  asm volatile("s_mov_b32 m0, %3\n\t.set rk_, %6\n\t.rept %7\n\ts_waitcnt vmcnt(rk_)\n\t" PIECE("%0", "%4", "%5") PIECE("%1", "%4", "%5") PIECE("%2", "%4", "%5")
               ".set rk_, rk_ - 1\n\t.endr" : "+v"(v[0]), "+v"(v[1]), "+v"(v[2]) : "s"(lds), "s"(base), "s"(gstep), "i"(A), "i"(NG) : "memory", "scc");
}
template <int WAIT> __device__ __forceinline__ void touch(unsigned voff, const void* base) {
  asm volatile("s_mov_b32 m0, %2\n\ts_waitcnt vmcnt(%3)\n\tglobal_load_lds_dword %0, %1" :: "v"(voff), "s"(base), "s"(W_OFF - 256u), "i"(WAIT) : "memory");
}
static_assert(M_END <= W_OFF - 256, "LDS");

#define LDW(off, soff) __builtin_amdgcn_raw_buffer_load_b128(ws, off, soff, AUX_SC1)
#define STW(v, off) __builtin_amdgcn_raw_buffer_store_b128(v, ws, off, 0, AUX_SC1)
__device__ __forceinline__ v4u tag4(v4f v, unsigned T0, unsigned T1) {
  unsigned m = 0xfffff000u; asm("" : "+v"(m));
  return v4u{(asu(v[0]) & m) | T0, asu(v[1]), (asu(v[2]) & m) | T1, asu(v[3])};
}
__device__ __forceinline__ v4f untag4(v4u v, unsigned T0, unsigned T1, unsigned& bad) {
  const unsigned a = v.x ^ T0, c = v.z ^ T1; bad |= a | c;
  return v4f{asf(a), asf(v.y), asf(c), asf(v.w)};
}
#define ALL_OK(bad) (!__any(((bad) & 0xfffu) != 0u))
#define GET_TAGS() const v2u tg_ = lds_ld<v2u>(TG_OFF); const unsigned T0 = rfl(tg_.x), T1 = rfl(tg_.y)
template <int STEP, bool WGT> __device__ __forceinline__ v4f settle(v4u (&t)[4], v4f wg, unsigned off, __amdgpu_buffer_rsrc_t ws, unsigned T0, unsigned T1) {
  v4f a;
#pragma nounroll
  for (unsigned spin = 1;; ++spin) {
    unsigned bad = 0;
    const v4f a0 = untag4(t[0], T0, T1, bad), a1 = untag4(t[1], T0, T1, bad), a2 = untag4(t[2], T0, T1, bad), a3 = untag4(t[3], T0, T1, bad);
    a = WGT ? (a0 * wg[0] + a1 * wg[1]) + (a2 * wg[2] + a3 * wg[3]) : (a0 + a1) + (a2 + a3);
    PIN(a);
    if (__builtin_expect(ALL_OK(spin == SPIN_CAP ? 0u : bad), 1)) break;
    CBAR();
#pragma unroll
    for (int i = 0; i < 4; ++i) t[i] = LDW(off, i * STEP);
  }
  return a;
}
template <int N, int SL> __device__ __forceinline__ unsigned latest(unsigned (&hq)[N], __amdgpu_buffer_rsrc_t ws, unsigned T0, float alpha) {
  const unsigned tref = now32();
  int late = -100000, sum = 0;
#pragma nounroll
  for (unsigned spin = 0; spin < SPIN_CAP; ++spin) {
    v2u t[N]; bool more = false;
    CBAR();
#pragma unroll
    for (int i = 0; i < N; ++i) t[i] = __builtin_amdgcn_raw_buffer_load_b64(ws, hq[i], 0, AUX_SC1);
#pragma unroll
    for (int i = 0; i < N; ++i) { const bool ok = hq[i] != WS_END && t[i].x == T0; late = ok ? max(late, (int)(t[i].y - tref)) : late; sum += ok ? (int)(t[i].y - tref) : 0; hq[i] = ok ? WS_END : hq[i]; more |= hq[i] != WS_END; }
    if (!__any(more)) break;
    __builtin_amdgcn_s_sleep(SL);
  }
  const float mx = wave_max((float)late);
  return tref + min((int)asf(rfl(asu(mx - alpha * (mx - wave_sum((float)sum) * (1.f / 96))))), (int)(now32() - tref) + 400);
}

__device__ __forceinline__ unsigned hp_due(__amdgpu_buffer_rsrc_t ws, unsigned T0, int lane) {
  unsigned due = 0;
#pragma nounroll
  for (int k = 0; k < 2; ++k) {
    const unsigned o = (k ? H2_OFF : H1_OFF) + lane * 16;
    unsigned hq[2] = {oob((lane & 7) < 6, o), oob((lane & 7) < 6, o + 1024)};
    if (k) while ((int)(now32() - due) < 0) __builtin_amdgcn_s_sleep(2);
    due = latest<2, SLH>(hq, ws, T0, k ? ALPHA : ALPHA1);
  }
  return due;
}


__global__ void __launch_bounds__(NTHR, 4) fused(Params p) {
  __shared__ __attribute__((aligned(16))) char lds_raw[LDS_BYTES];
  if (KPF) asm volatile("s_load_dword s72, %0, 0x0" :: "s"(__builtin_amdgcn_kernarg_segment_ptr()) : "s72");
  u64 pc0; asm volatile("s_getpc_b64 %0" : "=s"(pc0));
  const int b = blockIdx.x, tid = threadIdx.x, wave = rfl(tid >> 6), lane = tid & 63;
  const int g = (b & 7) * 2 + ((b >> 3) & 1), j = b >> 4;
  const bool is_wo = j < 6, is_mlp = j >= 6 && j < 15;
  const int u = g * 9 + (j - 6);
  const int red = ((j + 1) & 15) * 16 + g;
  const __amdgpu_buffer_rsrc_t ws = __builtin_amdgcn_make_buffer_rsrc(p.ws, 0, WS_END, 0x00020000);
  const int r = lane >> 4, c = lane & 15;
  const int q4 = c >> 2, p4 = c & 3;
  const int qt = j >> 2, qc = j & 3;
  const v4f zero = {0.f, 0.f, 0.f, 0.f};
  asm volatile("" :: "v"((LAS char*)lds_raw));
  if (__builtin_expect(wave == 0, 1)) {
    asm volatile("s_mov_b32 m0, %2\n\ts_nop 0\n\tglobal_load_lds_dwordx4 %0, %3\n\tglobal_load_lds_dwordx4 %1, %3 offset:1024"
                 :: "v"(lane * 16), "v"(min(lane * 16u, 112u)), "s"(W_OFF + 18 * 1024), "s"(p.x) : "memory");
    burst<18, 1>((144 * qt + (lane >> 3)) * (QW * 2) + (256 * g + 64 * qc) * 2 + (lane & 7) * 16, W_OFF, p.wq, 8u * QW * 2);
    const size_t hb = ((size_t)g * SK + (SK - WIN) + 256 * j) * DH;
    const u16 *kb = p.k + hb, *vb = p.v + hb;
    const unsigned ph = (PHASE & 15) << 12;
    unsigned vk[4], vv[4];
#pragma unroll
    for (int n = 0; n < 4; ++n) { vk[n] = ph + r * 256 + ((c ^ (4 * n + r)) << 4); vv[n] = ph + r * 256 + ((c ^ ((r << 2) | n)) << 4); }
    static_assert(DA % 4 == 0 && DA >= 20 && DB % 4 == 0 && DB >= DA && DC % 4 == 0 && DC >= DB && DC >= 24 && DC <= 32, "stage positions");
    bar();
    dma4<1, DA>(vk, K_OFF, kb);
    VMW(22); lds_vst(F_L, 1u);
    dma4<DA / 4 - 1, DA>(vk, K_OFF + 4096, kb);
    VMW(DA); lds_vst(F_L, 2u);
    dma4<(32 + DB - DA) / 4, DB>(vk, K_OFF + DA * 1024, kb);
    VMW(DB); lds_vst(F_L, 3u);
    dma4<(32 - DB) / 4, DB>(vk, K_OFF + (32 + DB) * 1024, kb);
    dma4<DC / 4, DC>(vv, V_OFF, vb);
    VMW(DC); lds_vst(F_L, 4u);
    dma4<(64 - DC) / 4, DC>(vv, V_OFF + DC * 1024, vb);
    VMW(32); lds_vst(F_L, 6u);
    if (!is_wo) {
      const unsigned xh = b & 7, hh = (b >> 3) & 1, id = is_mlp ? hh * 9 + j - 6 : 18 * JM + hh, st = is_mlp ? 18 : 2;
      const unsigned j1 = min(lane / 9, 6) * (FF * 2) + xh * 1152 + (lane % 9) * 128;
      static_assert(18 * JM + 2 * JS >= 83, "jobs");
#define JOB(k, WAIT) touch<WAIT>(min(j1 + (id + (k) * st) * 7u * (FF * 2), (D - 1u) * (FF * 2) + xh * 1152u), p.w1);
      JOB(0, 20)
      VMW(17); lds_vst(F_L, 7u);
      JOB(1, 13)
      VMW(10); lds_vst(F_L, 8u);
      JOB(2, 6)
      VMW(3); lds_vst(F_L, 9u);
      if (is_mlp) {
        unsigned v2 = 32 * u * (D * 2) + lane * 16;
#pragma unroll
        for (int k = 3; k < JM; ++k) JOB(k, 2)
        wait_cnt<0>(F_KD, 4);
        VMW(2); dma1<8, 63>(v2, W2_OFF, p.w2, 1024u);
        VMW(9); dma1<8, 63>(v2, W2_OFF + 8192, p.w2, 1024u);
        VMW(16); dma1<20, DC>(v2, W2_OFF + 16384, p.w2, 1024u);
        lds_vst(F_T, now32()); lds_vst(F_W, 2u);
        VMW(MSTART); lds_vst(F_P1, 1u);
        VMW(0); lds_vst(F_W, 3u);
      } else {
#pragma unroll
        for (int k = 3; k < JS; ++k) JOB(k, 3)
        VMW(0);
      }
#undef JOB
    } else {
      unsigned vo[3];
#pragma unroll
      for (int i = 0; i < 3; ++i) { const int pi = i * 64 + lane; vo[i] = (256 * g + pi / 12) * (D * 2) + 192 * j + (pi % 12) * 16; }
      wait_cnt<0>(F_PUB, 4);
      constexpr int K7 = (DC - 16) / 4, K8 = (DC - 8) / 4, K9 = DC / 4;
      ramp3<K7 - 1, DC - 4>(vo, W_OFF, p.wo, 16u * D * 2);
      VMW(16 + 3 * (K7 - 1)); lds_vst(F_L, 7u);
      ramp3<K8 - K7, DC - 3 - K7>(vo, W_OFF + (K7 - 1) * 3072, p.wo, 16u * D * 2);
      VMW(8 + 3 * (K8 - 1)); lds_vst(F_L, 8u);
      ramp3<K9 - K8, DC - 3 - K8>(vo, W_OFF + (K8 - 1) * 3072, p.wo, 16u * D * 2);
      VMW(3 * (K9 - 1)); lds_vst(F_L, 9u);
      lds_vst(F_T, now32() + CWO); lds_vst(F_W, 1u);
      wait_cnt<0>(F_KD, 4);
      dma3<17 - K9, 3 * K9>(vo, W_OFF + (K9 - 1) * 3072, p.wo, 16u * D * 2);
      VMW(WOEARLY); lds_vst(F_W, 2u);
      VMW(0); lds_vst(F_W, 3u);
    }
  } else {
    if (wave == 1) lds_st<unsigned>((lane & 31) * 4, (lane & 16) ? SENT : 0u);
    if (wave == 8 && (b >> 3) * 1024 < CODEB) {
      v4u t;
      asm volatile("global_load_dwordx4 %0, %1, %2\n\ts_barrier\n\ts_waitcnt vmcnt(0)" : "=&v"(t) : "v"(min((b >> 3) * 1024u, CODEB - 1024u) + lane * 16), "s"(pc0) : "memory");
    } else {
      bar();
    }
  }
  if (wave == 1) {
    wait_cnt<1>(F_L, 1);
    const v4u xa = lds_ld<v4u>(W_OFF + 18 * 1024 + lane * 16);
    float xv[9];
#pragma unroll
    for (int k = 0; k < 4; ++k) { xv[2 * k] = lo16(xa[k]); xv[2 * k + 1] = hi16(xa[k]); }
    xv[8] = lo16(lds_ld<u16>(W_OFF + 19 * 1024 + lane * 2));
    lds_st<v4f>(X_OFF + lane * 32, v4f{xv[0], xv[1], xv[2], xv[3]}); lds_st<v4f>(X_OFF + lane * 32 + 16, v4f{xv[4], xv[5], xv[6], xv[7]});
    lds_st<float>(X_OFF + 2048 + lane * 4, xv[8]);
    {
      const float c0 = asf(rfl(asu(xv[0])));
      float s = 0.f, q = 0.f;
#pragma unroll
      for (int i = 0; i < 9; ++i) { const float d = xv[i] - c0; s += d; q = fmaf(d, d, q); }
      s = wave_sum(s); q = wave_sum(q);
      const float md = s * (1.f / D), var = fmaxf(q * (1.f / D) - md * md, 0.f);
      const float mb = rb(c0 + md), rden = 1.f / rb(__builtin_sqrtf(rb(rb(var) + 1e-5f)));
#pragma unroll
      for (int i = 0; i < 9; ++i) xv[i] = rb(xv[i] - mb) * rden;
      lds_st<v4u>(Z_OFF + lane * 16, v4u{pk(xv[0], xv[1]), pk(xv[2], xv[3]), pk(xv[4], xv[5]), pk(xv[6], xv[7])});
      lds_st<u16>(Z_OFF + 1024 + lane * 2, (u16)pk(xv[8], xv[8]));
    }
    lds_vst(F_Y, 1u);
    wait_cnt<1>(F_PUB, 4);
    GET_TAGS();
    const v4u tq = {T0 & 63u, T0 >> 6, T1 & 63u, T1 >> 6};
    unsigned qa = QP_OFF + g * 4096 + lane * 16;
    PIN(qa);
    __builtin_amdgcn_s_sleep(QDELAY);
    v4f qs;
#pragma nounroll
    for (unsigned spin = 0; spin < SPIN_CAP; ++spin) {
      v4u gq[4]; unsigned bad = 0;
      CBAR();
#pragma unroll
      for (int t = 0; t < 4; ++t) gq[t] = LDW(qa, t * 1024);
#pragma unroll
      for (int t = 0; t < 4; ++t) { gq[t] ^= tq; bad |= gq[t].x | gq[t].y | gq[t].z | gq[t].w; }
      qs = (F4(gq[0]) + F4(gq[1])) + (F4(gq[2]) + F4(gq[3]));
      if (!__any((bad & 63u) != 0u)) break;
    }
    lds_st<v2u>(Q_OFF + lane * 8, v2u{pk(qs[0], qs[1]), pk(qs[2], qs[3])});
    lds_vst(F_Q, 1u);
  } else if (wave >= 4 && wave <= 7) {
    const int w = wave - 4;
    if (w == 0) {
      const unsigned ep = *(const __attribute__((address_space(4))) unsigned*)(p.ws + EP_OFF);
      lds_st<v2u>(TG_OFF, v2u{(ep + 1u) & 0xfffu, ((ep >> 16) + 1u) & 0xfffu});
    }
    unsigned ka[4], va[2][2];
#pragma unroll
    for (int s = 0; s < 4; ++s) { ka[s] = K_OFF + (16 * w + c) * 256 + (((4 * s + r) ^ c) << 4); PIN(ka[s]); }
#pragma unroll
    for (int tt = 0; tt < 2; ++tt)
#pragma unroll
      for (int t = 0; t < 2; ++t) { va[tt][t] = V_OFF + 256 * (8 * r + 4 * t + q4) + 16 * ((2 * (2 * w + tt) + (p4 >> 1)) ^ ((q4 << 2) | ((2 * r + t) & 3))) + 8 * (p4 & 1); PIN(va[tt][t]); }
    unsigned opo = oob(c < 4, OP_OFF + (g * 16 + j) * 1024 + ((c & 1) * 128 + 32 * w + 8 * (c & 2) + 4 * r) * 4);
    unsigned wa = W_OFF + (8 * r + q4) * 128 + p4 * 8 + w * 32, wb = W_OFF + (128 + 4 * r + q4) * 128 + p4 * 8 + w * 32;
    unsigned qo = oob(c == 0, QP_OFF + ((g * 4 + qt) * 256 + 64 * qc + 16 * w + 4 * r) * 4);
    PIN(opo); PIN(wa); PIN(wb); PIN(qo);
#pragma nounroll
    for (unsigned s = 0; s < LSPIN_CAP; ++s) { if (rfl(lds_vld<unsigned>(TG_OFF)) != SENT) break; __builtin_amdgcn_s_sleep(1); }
    CBAR();
    GET_TAGS();
    {
      wait_cnt<1>(F_Y, 1);
      v4u yf[4], wf[4];
#pragma unroll
      for (int s = 0; s < 4; ++s) yf[s] = lds_ld<v4u>(Z_OFF + 288 * qt + 16 * r + 64 * s);
      const v2u yl = lds_ld<v2u>(Z_OFF + 288 * qt + 256 + 8 * r);
      wait_cnt<0>(F_L, 2);
#pragma unroll
      for (int s = 0; s < 4; ++s) wf[s] = tr8(wa + s * 4096, wa + s * 4096 + 512);
      const v2u wl = tr4(wb);
      SB();
      const v4f qa = mfma(wf[3], yf[3], mfma(wf[2], yf[2], mfma(wf[1], yf[1], mfma(wf[0], yf[0], zero)))) +
                     __builtin_amdgcn_mfma_f32_16x16x16bf16_1k(__builtin_bit_cast(v4s, wl), __builtin_bit_cast(v4s, yl), zero, 0, 0, 0);
      const v4u tq = {T0 & 63u, T0 >> 6, T1 & 63u, T1 >> 6};
      STW(((U4(qa) + 32u) & ~63u) | tq, qo);
      if (lane == 0) lds_inc(F_PUB);
    }
    v4u kf[2][4], qf[4]; v4f dt[4];
    wait_cnt<2>(F_L, 3);
#pragma unroll
    for (int t = 0; t < 2; ++t)
#pragma unroll
      for (int s = 0; s < 4; ++s) kf[t][s] = lds_ld<v4u>(ka[s] + t * 16384);
    SB();
    wait_cnt<1>(F_Q, 1);
#pragma unroll
    for (int s = 0; s < 4; ++s) qf[s] = lds_ld<v4u>(Q_OFF + (c & 1) * 256 + r * 16 + s * 64);
    SB();
#pragma unroll
    for (int t = 0; t < 2; ++t) dt[t] = mfma(qf[3], kf[t][3], mfma(qf[2], kf[t][2], mfma(qf[1], kf[t][1], mfma(qf[0], kf[t][0], zero))));
    SB();
    wait_cnt<0>(F_L, 4);
#pragma unroll
    for (int t = 0; t < 2; ++t)
#pragma unroll
      for (int s = 0; s < 4; ++s) kf[t][s] = lds_ld<v4u>(ka[s] + (2 + t) * 16384);
    SB();
    if (lane == 0) lds_inc(F_KD);
#pragma unroll
    for (int t = 0; t < 2; ++t) dt[2 + t] = mfma(qf[3], kf[t][3], mfma(qf[2], kf[t][2], mfma(qf[1], kf[t][1], mfma(qf[0], kf[t][0], zero))));
    const bool r1 = r & 1, r2 = r & 2;
    float s0 = r2 ? (r1 ? dt[3][0] : dt[2][0]) : (r1 ? dt[1][0] : dt[0][0]);
    float s1 = r2 ? (r1 ? dt[3][1] : dt[2][1]) : (r1 ? dt[1][1] : dt[0][1]);
    s0 = rb(rb(s0) * 0.08838834764831845f); s1 = rb(rb(s1) * 0.08838834764831845f);
    {
      float m = m32(s0, s1);
      m = row_max(m16(m, m));
      lds_st<unsigned>(MX_OFF + (lane >> 5) * 16 + w * 4, min(asu(m), SENT - 1u));
    }
    v4u ea, eb;
#pragma nounroll
    for (unsigned s = 0; s < LSPIN_CAP; ++s) {
      ea = lds_vld<v4u>(MX_OFF); eb = lds_vld<v4u>(MX_OFF + 16);
      if (rfl(max(max(max(ea.x, ea.y), max(ea.z, ea.w)), max(max(eb.x, eb.y), max(eb.z, eb.w)))) != SENT) break;
    }
    const float M0 = fmaxf(fmaxf(asf(ea.x), asf(ea.y)), fmaxf(asf(ea.z), asf(ea.w))), M1 = fmaxf(fmaxf(asf(eb.x), asf(eb.y)), fmaxf(asf(eb.z), asf(eb.w)));
    const unsigned pp = pk(__builtin_amdgcn_exp2f((s0 - M0) * 1.4426950408889634f), __builtin_amdgcn_exp2f((s1 - M1) * 1.4426950408889634f));
    lds_st<u16>(P_OFF + (64 * r + 16 * w + c) * 2, (u16)pp); lds_st<u16>(P_OFF + 512 + (64 * r + 16 * w + c) * 2, (u16)(pp >> 16));
    if (lane == 0) lds_inc(F_P);
    wait_cnt<0>(F_P, 4);
    v4u pf[8];
#pragma unroll
    for (int s = 0; s < 8; ++s) pf[s] = lds_ld<v4u>(P_OFF + (c & 1) * 512 + r * 16 + s * 64);
    if (w == 0) {
      const v4u ones = {0x3f803f80u, 0x3f803f80u, 0x3f803f80u, 0x3f803f80u};
      v4f ls = zero;
#pragma unroll
      for (int s = 0; s < 8; ++s) ls = mfma(pf[s], ones, ls);
      STW(tag4(v4f{M0, ls[0], M1, ls[1]}, T0, T1), oob(lane < 8, ST_OFF + (g * 16 + j) * 128 + lane * 16));
    }
    v4f oa = zero, ob = zero;
#define PV(s0_, n_) { v4u fa[n_], fb[n_]; \
      _Pragma("unroll") for (int s = 0; s < n_; ++s) { fa[s] = tr8(va[0][0] + (s0_ + s) * 8192, va[0][1] + (s0_ + s) * 8192); fb[s] = tr8(va[1][0] + (s0_ + s) * 8192, va[1][1] + (s0_ + s) * 8192); } \
      SB(); _Pragma("unroll") for (int s = 0; s < n_; ++s) { oa = mfma(fa[s], pf[s0_ + s], oa); ob = mfma(fb[s], pf[s0_ + s], ob); } SB(); }
    wait_cnt<0>(F_L, 6); PV(0, 4)
    wait_cnt<0>(F_L, 7); PV(4, 2)
    wait_cnt<0>(F_L, 8); PV(6, 1)
#undef PV
    {
      v4u fa, fb;
#pragma nounroll
      for (unsigned sp = 0; sp < LSPIN_CAP; ++sp) {
        const unsigned f = lds_vld<unsigned>(F_L);
        CBAR();
        fa = tr8(va[0][0] + 7 * 8192, va[0][1] + 7 * 8192); fb = tr8(va[1][0] + 7 * 8192, va[1][1] + 7 * 8192);
        SB();
        if (rfl(f) >= 9u) break;
      }
      oa = mfma(fa, pf[7], oa); ob = mfma(fb, pf[7], ob);
    }
    STW(tag4((c & 2) ? ob : oa, T0, T1), opo);
  }

  if (is_wo) {
    if (wave >= 4 && wave <= 7) {
      const int w = wave - 4;
      unsigned oo = OP_OFF + (g * 16 + 4 * r) * 1024 + (64 * w + 4 * c) * 4, ol = O_OFF + (64 * w + 4 * c + r) * 2;
      PIN(oo); PIN(ol);
      GET_TAGS();
      wait_cnt<1>(F_W, 2);
      v4u t[4];
#pragma unroll
      for (int i = 0; i < 4; ++i) t[i] = LDW(oo, i * 1024);
      SB();
      wait_cnt<0>(F_GO, 1);
      v4f wg = lds_ld<v4f>(WG_OFF + (w >> 1) * 64 + r * 16);
      PIN(wg);
      SB();
      const v4f a = settle<1024, true>(t, wg, oo, ws, T0, T1);
      const float o = t16(t32(a[0], a[2]), t32(a[1], a[3]));
      lds_st<u16>(ol, (u16)pk(o, o));
      bar();
    } else if (wave >= 8 && wave <= 13) {
      const int t = wave - 8;
      unsigned ho = oob(c == 0, HP_OFF + (g * 576 + j * 96 + 16 * t + 4 * r) * 4), oa = O_OFF + r * 16, wa = W_OFF + (8 * r + q4) * 192 + t * 32 + p4 * 8;
      PIN(ho); PIN(oa); PIN(wa);
      wait_cnt<8>(F_W, 1);
      wait_cnt<2>(F_W, 2);
      GET_TAGS();
      v4u wf[8], of[8];
      constexpr int S1 = 8 - (WOEARLY + 5) / 6;
#pragma unroll
      for (int s = 0; s < S1; ++s) wf[s] = tr8(wa + s * 6144, wa + s * 6144 + 4 * 192);
      wait_cnt<0>(F_W, 3);
#pragma unroll
      for (int s = S1; s < 8; ++s) wf[s] = tr8(wa + s * 6144, wa + s * 6144 + 4 * 192);
#pragma unroll
      for (int s = 0; s < 8; ++s) PIN(wf[s]);
      bar();
#pragma unroll
      for (int s = 0; s < 8; ++s) of[s] = lds_ld<v4u>(oa + s * 64);
      SB();
      v4f acc = zero;
#pragma unroll
      for (int s = 0; s < 8; ++s) acc = mfma(wf[s], of[s], acc);
      STW(tag4(acc, T0, T1), ho);
    } else {
      if (wave == 1) {
        wait_cnt<2>(F_W, 1);
        GET_TAGS();
        v4f st;
#pragma nounroll
        for (unsigned spin = 0; spin < SPIN_CAP; ++spin) {
          unsigned bad = 0;
          CBAR();
          st = untag4(LDW(ST_OFF + (g * 16 + c) * 128 + r * 16, 0), T0, T1, bad);
          if (ALL_OK(bad)) break;
          __builtin_amdgcn_s_sleep(4);
        }
        const float mp = (r & 1) ? st[2] : st[0], lp = (r & 1) ? st[3] : st[1];
        float wgt = __builtin_amdgcn_exp2f((mp - row_max(mp)) * 1.4426950408889634f);
        wgt *= __builtin_amdgcn_rcpf(row_sum(wgt * lp));
        lds_st<float>(WG_OFF + lane * 4, wgt);
        lds_vst(F_GO, 1u);
      } else if (wave == 2) {
        unsigned ho = oob(lane < (j == 5 ? 3 : 1), H2_OFF + g * 128 + (j + lane) * 16);
        PIN(ho);
        wait_cnt<8>(F_PUB, 4);
        GET_TAGS();
        wait_cnt<4>(F_L, 4);
        __builtin_amdgcn_raw_buffer_store_b128(v4u{T0, now32() + CKL, T1, 0u}, ws, ho, H1_OFF - H2_OFF, AUX_SC1);
        wait_cnt<2>(F_W, 1);
        STW((v4u{T0, lds_vld<unsigned>(F_T), T1, 0u}), ho);
      }
      bar();
    }
  } else if (is_mlp) {
    if (wave >= 8 && wave <= 11) {
      unsigned za = Z_OFF + (c >> 3) * 128 + (8 * r + (c & 7)) * 4, w2a = W2_OFF + (8 * r + q4) * 1152 + 9 * (wave - 8) * 32 + p4 * 8;
      unsigned mo = oob(p4 < 2 || c == 2, MP_OFF + (9 * (wave - 8) + 4 * p4 + q4) * 9216 + u * 64 + r * 16);
      float b1v = lo16(((const GAS u16*)p.b1)[32 * u + 8 * r + (c & 7)]);
      v4u g0 = {0u, 0u, 0u, 0u}, g1 = g0, g2 = g0, g3 = g0, w2f[9];
      PIN(za); PIN(w2a); PIN(mo); PIN(b1v); PIN(g0); PIN(g1); PIN(g2); PIN(g3);
      wait_cnt<8>(F_W, 2);
      GET_TAGS();
      bool have = false;
#pragma nounroll
      for (unsigned sp = 0; sp < LSPIN_CAP; ++sp) {
        const v2u f = lds_vld<v2u>(F_W);
        have = rfl(f.x) >= 3u;
        if (have || rfl(f.y) >= 72u) break;
        __builtin_amdgcn_s_sleep(1);
      }
      CBAR();
      if (have) {
#pragma unroll
        for (int t = 0; t < 9; ++t) w2f[t] = tr8(w2a + t * 32, w2a + t * 32 + 4 * 1152);
      }
      wait_cnt<2>(F_Z, 1);
      wait_cnt<0>(F_Z, 72);
      const v4f sa = lds_ld<v4f>(S_OFF), sb = lds_ld<v4f>(S_OFF + 16), sc = lds_ld<v4f>(S_OFF + 32), sd = lds_ld<v4f>(S_OFF + 48);
      const v2f se = lds_ld<v2f>(S_OFF + 64);
      float zz[9];
#pragma unroll
      for (int i = 0; i < 9; ++i) zz[i] = lds_ld<float>(za + i * 256);
      SB();
      const v4f s4 = (sa + sb) + (sc + sd);
      const float mean = ((s4[1] + s4[3]) + se[1]) * (1.f / D), var = fmaxf(((s4[0] + s4[2]) + se[0]) * (1.f / D) - mean * mean, 0.f);
      const float rr = __builtin_amdgcn_rsqf(fmaf(var, 1.f + 1e-5f, 1e-10f));
      const v2f zp = (v2f{zz[0], zz[1]} + v2f{zz[2], zz[3]}) + (v2f{zz[4], zz[5]} + v2f{zz[6], zz[7]});
      const float t = (zp[0] + zz[8]) + zp[1];
      const float zb = rb(rb((t - mean * dpp<XOR8>(t)) * rr) + b1v);
      const float e = __builtin_amdgcn_exp2f(zb * fmaf(zb * zb, 2.302208198144325f * 0.044715f, 2.302208198144325f));
      const float gl = zb - zb * __builtin_amdgcn_rcpf(e + 1.f);
      const unsigned gp = pk(gl, dpp<XOR1>(gl));
      if (!have) {
        wait_cnt<0>(F_W, 3);
#pragma unroll
        for (int t2 = 0; t2 < 9; ++t2) w2f[t2] = tr8(w2a + t2 * 32, w2a + t2 * 32 + 4 * 1152);
      }
#define GB(G_, bk) G_[0] = __builtin_amdgcn_update_dpp(G_[0], gp, NEWB + 0, 0xf, 1 << (bk), false); G_[1] = __builtin_amdgcn_update_dpp(G_[1], gp, NEWB + 2, 0xf, 1 << (bk), false); \
                   G_[2] = __builtin_amdgcn_update_dpp(G_[2], gp, NEWB + 4, 0xf, 1 << (bk), false); G_[3] = __builtin_amdgcn_update_dpp(G_[3], gp, NEWB + 6, 0xf, 1 << (bk), false);
      GB(g0, 0) GB(g1, 1) GB(g2, 2) GB(g3, 3)
#undef GB
      const v4f o2 = mfma(w2f[8], g0, zero);
      const v4f o0 = mfma(w2f[3], g3, mfma(w2f[2], g2, mfma(w2f[1], g1, mfma(w2f[0], g0, zero))));
      const v4f o1 = mfma(w2f[7], g3, mfma(w2f[6], g2, mfma(w2f[5], g1, mfma(w2f[4], g0, zero))));
      v4f o;
#pragma unroll
      for (int k = 0; k < 4; ++k) { const float a0 = o0[k], a1 = o1[k], a2 = o2[k]; const float lo_ = (c & 2) ? a2 : a0; o[k] = (c & 1) ? a1 : lo_; }
      STW(tag4(o, T0, T1), mo);
    } else if (wave == 1) {
      unsigned h3o = oob(lane < 8, H3_OFF + u * 128 + lane * 16);
      PIN(h3o);
      wait_cnt<4>(F_L, HSTART);
      GET_TAGS();
      const unsigned due = hp_due(ws, T0, lane);
      lds_vst(F_GO + 4, due); lds_vst(F_GO, 1u);
      const unsigned ta = now32(), go = (int)(due - ta) <= LATEADD ? ta + LATEADD : due;
      wait_cnt<1>(F_W, 2);
      const unsigned own = rfl(lds_vld<unsigned>(F_T)) + DELTAW, all = go + DELTA3;
      STW((v4u{T0, (int)(own - all) > 0 ? own : all, T1, 0u}), h3o);
    } else if (wave != 0) {
      const int ch = wave >= 12 ? (wave == 14 ? 8 : wave - 12) : wave;
      unsigned ho = HP_OFF + (r * 576 + 64 * ch + 4 * c) * 4, zw = Z_OFF + ch * 256 + q4 * 128 + r * 16, sw = S_OFF + r * 128 + ch * 8 + q4 * 4;
      unsigned v1 = (64 * ch + (lane >> 2)) * (FF * 2) + 64 * u + (lane & 3) * 16, w1a = W1_OFF + (64 * ch + 4 * q4 + r) * 64 + p4 * 8;
      v4u f0 = {0x3f803f80u, 0x3f803f80u, 0x3f803f80u, 0x3f803f80u}, f1 = f0, hv[4], w1f[2][2];
      PIN(ho); PIN(zw); PIN(sw); PIN(v1); PIN(w1a); PIN(f0); PIN(f1);
      wait_cnt<8>(F_L, 9);
      wait_cnt<2>(F_KD, 4);
      GET_TAGS();
      float xv = lds_ld<float>(X_OFF + (64 * ch + 4 * c + r) * 4);
      PIN(xv);
      auto take_w1 = [&]() {
        burst<4, 0>(v1, W1_OFF + ch * 4096, p.w1, 16u * FF * 2);
        VMW(0);
#pragma unroll
        for (int s = 0; s < 2; ++s)
#pragma unroll
          for (int t = 0; t < 2; ++t) w1f[s][t] = tr8(w1a + s * 2048 + t * 32, w1a + s * 2048 + t * 32 + 16 * 64);
        PIN(w1f[0][0]); PIN(w1f[0][1]); PIN(w1f[1][0]); PIN(w1f[1][1]);
      };
      bool early = true;
#pragma nounroll
      for (unsigned sp = 0; sp < LSPIN_CAP; ++sp) {
        const v4u f = lds_vld<v4u>(F_GO);
        early = rfl(f.z) != 0u;
        if (early || (rfl(f.x) != 0u && (int)(now32() - rfl(f.y)) >= -20)) break;
        __builtin_amdgcn_s_sleep(1);
      }
      CBAR();
      if (early) take_w1();
      wait_cnt<2>(F_GO, 1);
      {
        const unsigned t0 = now32(), dl = min(max((int)(rfl(lds_vld<unsigned>(F_GO + 4)) - t0), 0), 400);
        while (now32() - t0 + 12 < dl) __builtin_amdgcn_s_sleep(2);
        while (now32() - t0 < dl) {}
      }
      CBAR();
#pragma unroll
      for (int i = 0; i < 4; ++i) hv[i] = LDW(ho, i * 4 * 2304);
      SB();
      if (!early) { wait_cnt<0>(F_P1, 1); take_w1(); }
      const v4f a = settle<4 * 2304, false>(hv, zero, ho, ws, T0, T1);
      const float hs = xv + rb(t16(t32(a[0], a[2]), t32(a[1], a[3])));
      const unsigned pr = pk(hs, dpp<XOR1>(hs));
#define HB(F_, k, n) F_[k] = __builtin_amdgcn_update_dpp(F_[k], pr, NEWB + (n), 0xf, 0xd, false);
      HB(f0, 0, 0) HB(f0, 1, 2) HB(f0, 2, 4) HB(f0, 3, 6) HB(f1, 0, 8) HB(f1, 1, 10) HB(f1, 2, 12) HB(f1, 3, 14)
#undef HB
      const v4f z0 = mfma(w1f[1][0], f1, mfma(w1f[0][0], f0, zero)), z1 = mfma(w1f[1][1], f1, mfma(w1f[0][1], f0, zero)), sq = mfma(f1, f1, mfma(f0, f0, zero));
      if ((c & 11) == 0) { lds_st<v4f>(zw, z0); lds_st<v4f>(zw + 64, z1); lds_st<float>(sw, sq[0]); lds_inc(F_Z); }
      if (u == 0) {
        __builtin_amdgcn_raw_buffer_store_b32((pr << 16) | T0, ws, HR_OFF + (64 * ch + 4 * c + r) * 4, 0, AUX_SC1);
        if (EPE && ch == 0 && lane == 0) __builtin_amdgcn_raw_buffer_store_b32(T0 | (T1 << 16), ws, EP_OFF, 0, AUX_SC1);
      }
    }
  }
  if (red < 36 && wave == 1) {
    const int col = 16 * red + 4 * (lane & 3) + r;
    float b2v = lo16(((const GAS u16*)p.b2)[col]);
    unsigned mo = MP_OFF + red * 9216 + lane * 16, hro = HR_OFF + col * 4, oo = col * 2, hq[3], hu;
    PIN(b2v); PIN(mo); PIN(hro); PIN(oo);
#pragma unroll
    for (int i = 0; i < 3; ++i) hq[i] = oob(i * 64 + lane < 144, H3_OFF + (i * 64 + lane) * 128);
    wait_cnt<8>(F_L, 9);
    GET_TAGS();
    if (j == 15) __builtin_amdgcn_s_sleep(16);
    if (RWAIT >= 0) {
      const unsigned due = hp_due(ws, T0, lane) + RWAIT;
      while ((int)(now32() - due) < 0) __builtin_amdgcn_s_sleep(2);
    }
    const unsigned go = latest<3, 2>(hq, ws, T0, 0.f);
    while ((int)(now32() - go) < 0) {}
    v4f a;
#pragma nounroll
    for (unsigned spin = 0; spin < SPIN_CAP; ++spin) {
      CBAR();
      hu = __builtin_amdgcn_raw_buffer_load_b32(ws, hro, 0, AUX_SC1);
      SB();
      v4u mv[9];
#pragma unroll
      for (int i = 0; i < 9; ++i) { mv[i] = LDW(mo + (i & 3) * 1024, (i & ~3) * 1024); SB(); }
      hu ^= T0;
      unsigned bad = hu;
      a = untag4(mv[0], T0, T1, bad);
#pragma unroll
      for (int i = 1; i < 9; ++i) { a += untag4(mv[i], T0, T1, bad); PIN(a); SB(); }
      if (ALL_OK(bad)) break;
    }
    float t = t16(t32(a[0], a[2]), t32(a[1], a[3]));
    t += dpp<XOR8>(t); t += dpp<XOR7>(dpp<XOR3>(t));
    const float res = asf(hu) + rb(rb(t) + b2v);
    __builtin_amdgcn_raw_buffer_store_b16((u16)pk(res, res), __builtin_amdgcn_make_buffer_rsrc(p.out, 0, D * 2, 0x00020000), oo, 0, OAUX);
    if (!EPE && red == 0 && lane == 0) __builtin_amdgcn_raw_buffer_store_b32(T0 | (T1 << 16), ws, EP_OFF, 0, AUX_SC1);
  }
}

extern "C" void kernel_launch(void* const* d_in, const int* in_sizes, int n_in, void* d_out, int out_size, void* d_ws, size_t ws_size, hipStream_t stream) {
  Params p;
  p.x = (const u16*)d_in[0]; p.k = (const u16*)d_in[1]; p.v = (const u16*)d_in[2]; p.wq = (const u16*)d_in[3]; p.wo = (const u16*)d_in[4];
  p.w1 = (const u16*)d_in[6]; p.b1 = (const u16*)d_in[7]; p.w2 = (const u16*)d_in[8]; p.b2 = (const u16*)d_in[9];
  p.out = (u16*)d_out; p.ws = (char*)d_ws;
  hipLaunchKernelGGL(fused, dim3(NBLK), dim3(NTHR), 0, stream, p);
}
```

```cpp
#include <hip/hip_runtime.h>
#include <hip/hip_bf16.h>

typedef unsigned short u16;
typedef unsigned long long u64;
typedef unsigned v4u __attribute__((ext_vector_type(4)));
typedef unsigned v2u __attribute__((ext_vector_type(2)));
typedef float v4f __attribute__((ext_vector_type(4)));
typedef float v2f __attribute__((ext_vector_type(2)));
typedef short v4s __attribute__((ext_vector_type(4)));
typedef __bf16 bf16x2 __attribute__((ext_vector_type(2)));
typedef __bf16 bf16x8 __attribute__((ext_vector_type(8)));
#define GAS __attribute__((address_space(1)))
#define LAS __attribute__((address_space(3)))

#define DA 20
#define DB 24
#define DC 28
#define QDELAY 36
#define WOEARLY 8
#define HWAIT 10
#define SLH 10
#define RWAIT 50
#define CKL 485
#define ALPHA 0.4f
#define LATEADD 25
#define STWAIT 16
#define MSTART 24
#define DELTA3 154
#define DELTAW 218
#define CODEB 16640
constexpr int JM = 4, JS = 6;

constexpr int D = 576, SK = 65536, DH = 128, WIN = 4096, FF = 4608, QW = 4096;
constexpr int NWAVE = 15, NTHR = NWAVE * 64, NBLK = 256;
constexpr unsigned EP_OFF = 0, QP_OFF = 4096, OP_OFF = QP_OFF + 16 * 4 * 256 * 4, ST_OFF = OP_OFF + 16 * 16 * 256 * 4, HP_OFF = ST_OFF + 256 * 128,
                   MP_OFF = HP_OFF + 16 * 576 * 4, HR_OFF = MP_OFF + 144 * 576 * 4, H1_OFF = HR_OFF + 576 * 4, H3_OFF = H1_OFF + 16 * 128,
                   WS_END = H3_OFF + 144 * 128;
constexpr unsigned SENT = 0xFFFFFFFFu;
constexpr unsigned SPIN_CAP = 1u << 16, LSPIN_CAP = 1u << 22;
constexpr int AUX_SC1 = 16;
constexpr unsigned F_L = 0, F_W = 8, F_Z = 12, F_P = 16, F_KD = 20, F_PUB = 24, F_Y = 28, F_T = 36, F_Q = 40, F_GO = 48  , F_P1 = 56, MX_OFF = 64, TG_OFF = 96,
                   Q_OFF = 128, P_OFF = Q_OFF + 512, WG_OFF = P_OFF + 1024, O_OFF = WG_OFF + 256, S_OFF = O_OFF + 512, X_OFF = S_OFF + 512,
                   Z_OFF = X_OFF + 2304, M_END = Z_OFF + 2304;
constexpr unsigned W_OFF = 8192, K_OFF = W_OFF + 24576, V_OFF = K_OFF + 65536, LDS_BYTES = V_OFF + 65536;
constexpr unsigned W1_OFF = W_OFF, W2_OFF = W_OFF + 36864;
static_assert(M_END <= W_OFF && LDS_BYTES <= 160 * 1024, "LDS");

struct Params { const u16 *x, *k, *v, *wq, *wo, *w1, *b1, *w2, *b2; u16* out; char* ws; };

__device__ __forceinline__ float asf(unsigned u) { return __builtin_bit_cast(float, u); }
__device__ __forceinline__ unsigned asu(float f) { return __builtin_bit_cast(unsigned, f); }
__device__ __forceinline__ float lo16(unsigned u) { return asf(u << 16); }
__device__ __forceinline__ float hi16(unsigned u) { return asf(u & 0xffff0000u); }
__device__ __forceinline__ unsigned pk(float lo, float hi) { bf16x2 r; r.x = (__bf16)lo; r.y = (__bf16)hi; return __builtin_bit_cast(unsigned, r); }
__device__ __forceinline__ float rb(float f) { return lo16(pk(f, f)); }
__device__ __forceinline__ v4f mfma(v4u a, v4u b, v4f c) {
  return __builtin_amdgcn_mfma_f32_16x16x32_bf16(__builtin_bit_cast(bf16x8, a), __builtin_bit_cast(bf16x8, b), c, 0, 0, 0);
}
template <int CTRL> __device__ __forceinline__ float dpp(float x) { return asf(__builtin_amdgcn_mov_dpp(asu(x), CTRL, 0xf, 0xf, true)); }
constexpr int XOR1 = 0xB1, XOR2 = 0x4E, XOR3 = 0x1B, XOR7 = 0x141, XOR8 = 0x128, NEWB = 0x150;
__device__ __forceinline__ float t32(float a, float b) { auto s = __builtin_amdgcn_permlane32_swap(asu(a), asu(b), false, false); return asf(s[0]) + asf(s[1]); }
__device__ __forceinline__ float t16(float a, float b) { auto s = __builtin_amdgcn_permlane16_swap(asu(a), asu(b), false, false); return asf(s[0]) + asf(s[1]); }
__device__ __forceinline__ float m32(float a, float b) { auto s = __builtin_amdgcn_permlane32_swap(asu(a), asu(b), false, false); return fmaxf(asf(s[0]), asf(s[1])); }
__device__ __forceinline__ float m16(float a, float b) { auto s = __builtin_amdgcn_permlane16_swap(asu(a), asu(b), false, false); return fmaxf(asf(s[0]), asf(s[1])); }
__device__ __forceinline__ float row_sum(float x) { x += dpp<XOR1>(x); x += dpp<XOR2>(x); x += dpp<XOR7>(x); x += dpp<XOR8>(x); return x; }
__device__ __forceinline__ float row_max(float x) { x = fmaxf(x, dpp<XOR1>(x)); x = fmaxf(x, dpp<XOR2>(x)); x = fmaxf(x, dpp<XOR7>(x)); x = fmaxf(x, dpp<XOR8>(x)); return x; }
__device__ __forceinline__ float wave_sum(float x) { x = row_sum(x); x = t16(x, x); return t32(x, x); }
__device__ __forceinline__ float wave_max(float x) { x = row_max(x); x = m16(x, x); return m32(x, x); }
__device__ __forceinline__ unsigned rfl(unsigned x) { return __builtin_amdgcn_readfirstlane(x); }
__device__ __forceinline__ unsigned now32() { return (unsigned)__builtin_amdgcn_s_memrealtime(); }
#define CBAR() asm volatile("" ::: "memory")
#define SB() __builtin_amdgcn_sched_barrier(0)
#define PIN(x) asm volatile("" : "+v"(x))
#define VMW(n) asm volatile("s_waitcnt vmcnt(%0)" :: "i"(n) : "memory")
#define F4(v) __builtin_bit_cast(v4f, v)
#define U4(v) __builtin_bit_cast(v4u, v)
__device__ __forceinline__ void bar() { asm volatile("s_waitcnt lgkmcnt(0)" ::: "memory"); __builtin_amdgcn_s_barrier(); CBAR(); }
__device__ __forceinline__ unsigned oob(bool keep, unsigned off) { PIN(off); return keep ? off : WS_END; }

template <typename T> __device__ __forceinline__ T lds_ld(unsigned off) { return *(const LAS T*)(size_t)off; }
template <typename T> __device__ __forceinline__ void lds_st(unsigned off, T v) { *(LAS T*)(size_t)off = v; }
template <typename T> __device__ __forceinline__ T lds_vld(unsigned off) { return *(const volatile LAS T*)(size_t)off; }
__device__ __forceinline__ void lds_vst(unsigned off, unsigned v) { CBAR(); *(volatile LAS unsigned*)(size_t)off = v; }
#define lds_inc(off) asm volatile("ds_add_u32 %0, %1 offset:%2" :: "v"(0u), "v"(1u), "i"(off) : "memory")
__device__ __forceinline__ v2u tr4(unsigned a) { return __builtin_bit_cast(v2u, __builtin_amdgcn_ds_read_tr16_b64_v4i16((LAS v4s*)(size_t)a)); }
__device__ __forceinline__ v4u tr8(unsigned a0, unsigned a1) { const v2u x = tr4(a0), y = tr4(a1); return v4u{x.x, x.y, y.x, y.y}; }
template <int SLEEP> __device__ __forceinline__ void wait_cnt(unsigned off, unsigned n) {
#pragma nounroll
  for (unsigned s = 0; s < LSPIN_CAP; ++s) { if (rfl(lds_vld<unsigned>(off)) >= n) break; if (SLEEP) __builtin_amdgcn_s_sleep(SLEEP); }
  CBAR();
}

#define LOOP_END(n) "s_sub_u32 " n ", " n ", 1\n\ts_cmp_lg_u32 " n ", 0\n\ts_cbranch_scc1 1b"
#define PIECE(v, b, g) "global_load_lds_dwordx4 " v ", " b " nt\n\ts_add_u32 m0, m0, 0x400\n\tv_add_u32 " v ", " g ", " v "\n\t"
template <int N, int DEP> __device__ __forceinline__ void dma1(unsigned& voff, unsigned lds, const void* base, unsigned gstep) {
  static_assert(N % 2 == 0, "");
  unsigned n = N / 2;
  asm volatile("s_mov_b32 m0, %2\n\t1:\n\t.rept 2\n\ts_waitcnt vmcnt(%5)\n\t" PIECE("%0", "%3", "%4") ".endr\n\t" LOOP_END("%1")
               : "+v"(voff), "+s"(n) : "s"(lds), "s"(base), "s"(gstep), "i"(DEP - 1) : "memory", "scc");
}
template <int N, int NT> __device__ __forceinline__ void burst(unsigned voff, unsigned lds, const void* base, unsigned gstep) {
  asm volatile("s_mov_b32 m0, %1\n\ts_nop 0\n\t.rept %4\n\t.if %5\n\tglobal_load_lds_dwordx4 %0, %2 nt\n\t.else\n\tglobal_load_lds_dwordx4 %0, %2\n\t.endif\n\t"
               "s_add_u32 m0, m0, 0x400\n\tv_add_u32 %0, %3, %0\n\t.endr" : "+v"(voff) : "s"(lds), "s"(base), "s"(gstep), "i"(N), "i"(NT) : "memory", "scc");
}
template <int NG, int DEP> __device__ __forceinline__ void dma4(unsigned (&v)[4], unsigned lds, const void* base) {
  unsigned n = NG;
  if (NG > 0)
    asm volatile("s_mov_b32 m0, %5\n\t1:\n\t"
                 "s_waitcnt vmcnt(%7)\n\tglobal_load_lds_dwordx4 %0, %6 nt\n\tv_add_u16 %0, 0x1000, %0\n\t"
                 "s_waitcnt vmcnt(%7)\n\tglobal_load_lds_dwordx4 %1, %6 offset:1024 nt\n\tv_add_u16 %1, 0x1000, %1\n\t"
                 "s_waitcnt vmcnt(%7)\n\tglobal_load_lds_dwordx4 %2, %6 offset:2048 nt\n\tv_add_u16 %2, 0x1000, %2\n\t"
                 "s_waitcnt vmcnt(%7)\n\tglobal_load_lds_dwordx4 %3, %6 offset:3072 nt\n\tv_add_u16 %3, 0x1000, %3\n\t"
                 "s_add_u32 m0, m0, 0x1000\n\t" LOOP_END("%4")
                 : "+v"(v[0]), "+v"(v[1]), "+v"(v[2]), "+v"(v[3]), "+s"(n) : "s"(lds), "s"(base), "i"(DEP - 1) : "memory", "scc");
}
template <int NG, int DEP> __device__ __forceinline__ void dma3(unsigned (&v)[3], unsigned lds, const void* base, unsigned gstep) {
  unsigned n = NG;
  asm volatile("s_mov_b32 m0, %4\n\t1:\n\ts_waitcnt vmcnt(%7)\n\t" PIECE("%0", "%5", "%6") "s_waitcnt vmcnt(%7)\n\t" PIECE("%1", "%5", "%6")
               "s_waitcnt vmcnt(%7)\n\t" PIECE("%2", "%5", "%6") LOOP_END("%3")
               : "+v"(v[0]), "+v"(v[1]), "+v"(v[2]), "+s"(n) : "s"(lds), "s"(base), "s"(gstep), "i"(DEP - 1) : "memory", "scc");
}
template <int NG, int A> __device__ __forceinline__ void ramp3(unsigned (&v)[3], unsigned lds, const void* base, unsigned gstep) {
  asm volatile("s_mov_b32 m0, %3\n\t.set rk_, %6\n\t.rept %7\n\ts_waitcnt vmcnt(rk_)\n\t" PIECE("%0", "%4", "%5") PIECE("%1", "%4", "%5") PIECE("%2", "%4", "%5")
               ".set rk_, rk_ - 1\n\t.endr" : "+v"(v[0]), "+v"(v[1]), "+v"(v[2]) : "s"(lds), "s"(base), "s"(gstep), "i"(A), "i"(NG) : "memory", "scc");
}
template <int WAIT> __device__ __forceinline__ void touch(unsigned voff, const void* base) {
  asm volatile("s_mov_b32 m0, %2\n\ts_waitcnt vmcnt(%3)\n\tglobal_load_lds_dword %0, %1" :: "v"(voff), "s"(base), "s"(W_OFF - 256u), "i"(WAIT) : "memory");
}
static_assert(M_END <= W_OFF - 256, "LDS");

#define LDW(off, soff) __builtin_amdgcn_raw_buffer_load_b128(ws, off, soff, AUX_SC1)
#define STW(v, off) __builtin_amdgcn_raw_buffer_store_b128(v, ws, off, 0, AUX_SC1)
__device__ __forceinline__ v4u tag4(v4f v, unsigned T0, unsigned T1) {
  unsigned m = 0xfffff000u; asm("" : "+v"(m));
  return v4u{(asu(v[0]) & m) | T0, asu(v[1]), (asu(v[2]) & m) | T1, asu(v[3])};
}
__device__ __forceinline__ v4f untag4(v4u v, unsigned T0, unsigned T1, unsigned& bad) {
  const unsigned a = v.x ^ T0, c = v.z ^ T1; bad |= a | c;
  return v4f{asf(a), asf(v.y), asf(c), asf(v.w)};
}
#define ALL_OK(bad) (!__any(((bad) & 0xfffu) != 0u))
#define GET_TAGS() const v2u tg_ = lds_ld<v2u>(TG_OFF); const unsigned T0 = rfl(tg_.x), T1 = rfl(tg_.y)
template <int STEP, bool WGT> __device__ __forceinline__ v4f settle(v4u (&t)[4], v4f wg, unsigned off, __amdgpu_buffer_rsrc_t ws, unsigned T0, unsigned T1) {
  v4f a;
#pragma nounroll
  for (unsigned spin = 1;; ++spin) {
    unsigned bad = 0;
    const v4f a0 = untag4(t[0], T0, T1, bad), a1 = untag4(t[1], T0, T1, bad), a2 = untag4(t[2], T0, T1, bad), a3 = untag4(t[3], T0, T1, bad);
    a = WGT ? (a0 * wg[0] + a1 * wg[1]) + (a2 * wg[2] + a3 * wg[3]) : (a0 + a1) + (a2 + a3);
    PIN(a);
    if (__builtin_expect(ALL_OK(spin == SPIN_CAP ? 0u : bad), 1)) break;
    CBAR();
#pragma unroll
    for (int i = 0; i < 4; ++i) t[i] = LDW(off, i * STEP);
  }
  return a;
}
template <int N, int SL> __device__ __forceinline__ unsigned latest(unsigned (&hq)[N], __amdgpu_buffer_rsrc_t ws, unsigned T0, float alpha) {
  const unsigned tref = now32();
  int late = -100000, sum = 0;
#pragma nounroll
  for (unsigned spin = 0; spin < SPIN_CAP; ++spin) {
    v2u t[N]; bool more = false;
    CBAR();
#pragma unroll
    for (int i = 0; i < N; ++i) t[i] = __builtin_amdgcn_raw_buffer_load_b64(ws, hq[i], 0, AUX_SC1);
#pragma unroll
    for (int i = 0; i < N; ++i) { const bool ok = hq[i] != WS_END && t[i].x == T0; late = ok ? max(late, (int)(t[i].y - tref)) : late; sum += ok ? (int)(t[i].y - tref) : 0; hq[i] = ok ? WS_END : hq[i]; more |= hq[i] != WS_END; }
    if (!__any(more)) break;
    __builtin_amdgcn_s_sleep(SL);
  }
  const float mx = wave_max((float)late);
  return tref + min((int)asf(rfl(asu(mx - alpha * (mx - wave_sum((float)sum) * (1.f / 96))))), (int)(now32() - tref) + 400);
}

__device__ __forceinline__ unsigned hp_due(__amdgpu_buffer_rsrc_t ws, unsigned T0, int lane) {
  unsigned hq[2] = {oob((lane & 7) < 6, H1_OFF + lane * 16), oob((lane & 7) < 6, H1_OFF + 1024 + lane * 16)};
  return latest<2, SLH>(hq, ws, T0, ALPHA);
}


__global__ void __launch_bounds__(NTHR, 4) fused(Params p) {
  __shared__ __attribute__((aligned(16))) char lds_raw[LDS_BYTES];
  u64 pc0; asm volatile("s_getpc_b64 %0" : "=s"(pc0));
  const int b = blockIdx.x, tid = threadIdx.x, wave = rfl(tid >> 6), lane = tid & 63;
  const int g = (b & 7) * 2 + ((b >> 3) & 1), j = b >> 4;
  const bool is_wo = j < 6, is_mlp = j >= 6 && j < 15;
  const int u = g * 9 + (j - 6);
  const int red = ((j + 1) & 15) * 16 + g;
  const __amdgpu_buffer_rsrc_t ws = __builtin_amdgcn_make_buffer_rsrc(p.ws, 0, WS_END, 0x00020000);
  const int r = lane >> 4, c = lane & 15;
  const int q4 = c >> 2, p4 = c & 3;
  const int qt = j >> 2, qc = j & 3;
  const v4f zero = {0.f, 0.f, 0.f, 0.f};
  asm volatile("" :: "v"((LAS char*)lds_raw));
  if (__builtin_expect(wave == 0, 1)) {
    asm volatile("s_mov_b32 m0, %2\n\ts_nop 0\n\tglobal_load_lds_dwordx4 %0, %3\n\tglobal_load_lds_dwordx4 %1, %3 offset:1024"
                 :: "v"(lane * 16), "v"(min(lane * 16u, 112u)), "s"(W_OFF + 18 * 1024), "s"(p.x) : "memory");
    burst<18, 1>((144 * qt + (lane >> 3)) * (QW * 2) + (256 * g + 64 * qc) * 2 + (lane & 7) * 16, W_OFF, p.wq, 8u * QW * 2);
    const size_t hb = ((size_t)g * SK + (SK - WIN) + 256 * j) * DH;
    const u16 *kb = p.k + hb, *vb = p.v + hb;
    const unsigned ph = ((j + g) & 15) << 12;
    unsigned vk[4], vv[4];
#pragma unroll
    for (int n = 0; n < 4; ++n) { vk[n] = ph + r * 256 + ((c ^ (4 * n + r)) << 4); vv[n] = ph + r * 256 + ((c ^ ((r << 2) | n)) << 4); }
    static_assert(DA % 4 == 0 && DA >= 20 && DB % 4 == 0 && DB >= DA && DC % 4 == 0 && DC >= DB && DC >= 24 && DC <= 32, "stage positions");
    bar();
    dma4<1, DA>(vk, K_OFF, kb);
    VMW(22); lds_vst(F_L, 1u);
    dma4<DA / 4 - 1, DA>(vk, K_OFF + 4096, kb);
    VMW(DA); lds_vst(F_L, 2u);
    dma4<(32 + DB - DA) / 4, DB>(vk, K_OFF + DA * 1024, kb);
    VMW(DB); lds_vst(F_L, 3u);
    dma4<(32 - DB) / 4, DB>(vk, K_OFF + (32 + DB) * 1024, kb);
    dma4<DC / 4, DC>(vv, V_OFF, vb);
    VMW(DC); lds_vst(F_L, 4u);
    dma4<(64 - DC) / 4, DC>(vv, V_OFF + DC * 1024, vb);
    VMW(32); lds_vst(F_L, 6u);
    if (!is_wo) {
      const unsigned xh = b & 7, hh = (b >> 3) & 1, id = is_mlp ? hh * 9 + j - 6 : 18 * JM + hh, st = is_mlp ? 18 : 2;
      const unsigned j1 = min(lane / 9, 6) * (FF * 2) + xh * 1152 + (lane % 9) * 128;
      static_assert(18 * JM + 2 * JS >= 83, "jobs");
#define JOB(k, WAIT) touch<WAIT>(min(j1 + (id + (k) * st) * 7u * (FF * 2), (D - 1u) * (FF * 2) + xh * 1152u), p.w1);
      JOB(0, 20)
      VMW(17); lds_vst(F_L, 7u);
      JOB(1, 13)
      VMW(10); lds_vst(F_L, 8u);
      JOB(2, 6)
      VMW(3); lds_vst(F_L, 9u);
      if (is_mlp) {
        unsigned v2 = 32 * u * (D * 2) + lane * 16;
#pragma unroll
        for (int k = 3; k < JM; ++k) JOB(k, 2)
        wait_cnt<0>(F_KD, 4);
        VMW(2); dma1<8, 63>(v2, W2_OFF, p.w2, 1024u);
        VMW(9); dma1<8, 63>(v2, W2_OFF + 8192, p.w2, 1024u);
        VMW(16); dma1<20, DC>(v2, W2_OFF + 16384, p.w2, 1024u);
        lds_vst(F_T, now32()); lds_vst(F_W, 2u);
        VMW(MSTART); lds_vst(F_P1, 1u);
        VMW(0); lds_vst(F_W, 3u);
      } else {
#pragma unroll
        for (int k = 3; k < JS; ++k) JOB(k, 3)
        VMW(0);
      }
#undef JOB
    } else {
      unsigned vo[3];
#pragma unroll
      for (int i = 0; i < 3; ++i) { const int pi = i * 64 + lane; vo[i] = (256 * g + pi / 12) * (D * 2) + 192 * j + (pi % 12) * 16; }
      wait_cnt<0>(F_PUB, 4);
      constexpr int K7 = (DC - 16) / 4, K8 = (DC - 8) / 4, K9 = DC / 4;
      ramp3<K7 - 1, DC - 4>(vo, W_OFF, p.wo, 16u * D * 2);
      VMW(16 + 3 * (K7 - 1)); lds_vst(F_L, 7u);
      ramp3<K8 - K7, DC - 3 - K7>(vo, W_OFF + (K7 - 1) * 3072, p.wo, 16u * D * 2);
      VMW(8 + 3 * (K8 - 1)); lds_vst(F_L, 8u);
      ramp3<K9 - K8, DC - 3 - K8>(vo, W_OFF + (K8 - 1) * 3072, p.wo, 16u * D * 2);
      VMW(3 * (K9 - 1)); lds_vst(F_L, 9u);
      lds_vst(F_W, 1u);
      wait_cnt<0>(F_KD, 4);
      dma3<17 - K9, 3 * K9>(vo, W_OFF + (K9 - 1) * 3072, p.wo, 16u * D * 2);
      VMW(WOEARLY); lds_vst(F_W, 2u);
      VMW(0); lds_vst(F_W, 3u);
    }
  } else {
    if (wave == 1) lds_st<unsigned>((lane & 31) * 4, (lane & 16) ? SENT : 0u);
    if (wave == 8 && (b >> 3) * 1024 < CODEB) {
      v4u t;
      asm volatile("global_load_dwordx4 %0, %1, %2\n\ts_barrier\n\ts_waitcnt vmcnt(0)" : "=&v"(t) : "v"(min((b >> 3) * 1024u, CODEB - 1024u) + lane * 16), "s"(pc0) : "memory");
    } else {
      bar();
    }
  }
  if (wave == 1) {
    wait_cnt<1>(F_L, 1);
    const v4u xa = lds_ld<v4u>(W_OFF + 18 * 1024 + lane * 16);
    float xv[9];
#pragma unroll
    for (int k = 0; k < 4; ++k) { xv[2 * k] = lo16(xa[k]); xv[2 * k + 1] = hi16(xa[k]); }
    xv[8] = lo16(lds_ld<u16>(W_OFF + 19 * 1024 + lane * 2));
    lds_st<v4f>(X_OFF + lane * 32, v4f{xv[0], xv[1], xv[2], xv[3]}); lds_st<v4f>(X_OFF + lane * 32 + 16, v4f{xv[4], xv[5], xv[6], xv[7]});
    lds_st<float>(X_OFF + 2048 + lane * 4, xv[8]);
    {
      const float c0 = asf(rfl(asu(xv[0])));
      float s = 0.f, q = 0.f;
#pragma unroll
      for (int i = 0; i < 9; ++i) { const float d = xv[i] - c0; s += d; q = fmaf(d, d, q); }
      s = wave_sum(s); q = wave_sum(q);
      const float md = s * (1.f / D), var = fmaxf(q * (1.f / D) - md * md, 0.f);
      const float mb = rb(c0 + md), rden = 1.f / rb(__builtin_sqrtf(rb(rb(var) + 1e-5f)));
#pragma unroll
      for (int i = 0; i < 9; ++i) xv[i] = rb(xv[i] - mb) * rden;
      lds_st<v4u>(Z_OFF + lane * 16, v4u{pk(xv[0], xv[1]), pk(xv[2], xv[3]), pk(xv[4], xv[5]), pk(xv[6], xv[7])});
      lds_st<u16>(Z_OFF + 1024 + lane * 2, (u16)pk(xv[8], xv[8]));
    }
    lds_vst(F_Y, 1u);
    wait_cnt<1>(F_PUB, 4);
    GET_TAGS();
    const v4u tq = {T0 & 63u, T0 >> 6, T1 & 63u, T1 >> 6};
    unsigned qa = QP_OFF + g * 4096 + lane * 16;
    PIN(qa);
    __builtin_amdgcn_s_sleep(QDELAY);
    v4f qs;
#pragma nounroll
    for (unsigned spin = 0; spin < SPIN_CAP; ++spin) {
      v4u gq[4]; unsigned bad = 0;
      CBAR();
#pragma unroll
      for (int t = 0; t < 4; ++t) gq[t] = LDW(qa, t * 1024);
#pragma unroll
      for (int t = 0; t < 4; ++t) { gq[t] ^= tq; bad |= gq[t].x | gq[t].y | gq[t].z | gq[t].w; }
      qs = (F4(gq[0]) + F4(gq[1])) + (F4(gq[2]) + F4(gq[3]));
      if (!__any((bad & 63u) != 0u)) break;
    }
    lds_st<v2u>(Q_OFF + lane * 8, v2u{pk(qs[0], qs[1]), pk(qs[2], qs[3])});
    lds_vst(F_Q, 1u);
  } else if (wave >= 4 && wave <= 7) {
    const int w = wave - 4;
    if (w == 0) {
      const unsigned ep = *(const __attribute__((address_space(4))) unsigned*)(p.ws + EP_OFF);
      lds_st<v2u>(TG_OFF, v2u{(ep + 1u) & 0xfffu, ((ep >> 16) + 1u) & 0xfffu});
    }
    unsigned ka[4], va[2][2];
#pragma unroll
    for (int s = 0; s < 4; ++s) { ka[s] = K_OFF + (16 * w + c) * 256 + (((4 * s + r) ^ c) << 4); PIN(ka[s]); }
#pragma unroll
    for (int tt = 0; tt < 2; ++tt)
#pragma unroll
      for (int t = 0; t < 2; ++t) { va[tt][t] = V_OFF + 256 * (8 * r + 4 * t + q4) + 16 * ((2 * (2 * w + tt) + (p4 >> 1)) ^ ((q4 << 2) | ((2 * r + t) & 3))) + 8 * (p4 & 1); PIN(va[tt][t]); }
    unsigned opo = oob(c < 4, OP_OFF + (g * 16 + j) * 1024 + ((c & 1) * 128 + 32 * w + 8 * (c & 2) + 4 * r) * 4);
    unsigned wa = W_OFF + (8 * r + q4) * 128 + p4 * 8 + w * 32, wb = W_OFF + (128 + 4 * r + q4) * 128 + p4 * 8 + w * 32;
    unsigned qo = oob(c == 0, QP_OFF + ((g * 4 + qt) * 256 + 64 * qc + 16 * w + 4 * r) * 4);
    PIN(opo); PIN(wa); PIN(wb); PIN(qo);
#pragma nounroll
    for (unsigned s = 0; s < LSPIN_CAP; ++s) { if (rfl(lds_vld<unsigned>(TG_OFF)) != SENT) break; __builtin_amdgcn_s_sleep(1); }
    CBAR();
    GET_TAGS();
    {
      wait_cnt<1>(F_Y, 1);
      v4u yf[4], wf[4];
#pragma unroll
      for (int s = 0; s < 4; ++s) yf[s] = lds_ld<v4u>(Z_OFF + 288 * qt + 16 * r + 64 * s);
      const v2u yl = lds_ld<v2u>(Z_OFF + 288 * qt + 256 + 8 * r);
      wait_cnt<0>(F_L, 2);
#pragma unroll
      for (int s = 0; s < 4; ++s) wf[s] = tr8(wa + s * 4096, wa + s * 4096 + 512);
      const v2u wl = tr4(wb);
      SB();
      const v4f qa = mfma(wf[3], yf[3], mfma(wf[2], yf[2], mfma(wf[1], yf[1], mfma(wf[0], yf[0], zero)))) +
                     __builtin_amdgcn_mfma_f32_16x16x16bf16_1k(__builtin_bit_cast(v4s, wl), __builtin_bit_cast(v4s, yl), zero, 0, 0, 0);
      const v4u tq = {T0 & 63u, T0 >> 6, T1 & 63u, T1 >> 6};
      STW(((U4(qa) + 32u) & ~63u) | tq, qo);
      if (lane == 0) lds_inc(F_PUB);
    }
    v4u kf[2][4], qf[4]; v4f dt[4];
    wait_cnt<2>(F_L, 3);
#pragma unroll
    for (int t = 0; t < 2; ++t)
#pragma unroll
      for (int s = 0; s < 4; ++s) kf[t][s] = lds_ld<v4u>(ka[s] + t * 16384);
    SB();
    wait_cnt<1>(F_Q, 1);
#pragma unroll
    for (int s = 0; s < 4; ++s) qf[s] = lds_ld<v4u>(Q_OFF + (c & 1) * 256 + r * 16 + s * 64);
    SB();
#pragma unroll
    for (int t = 0; t < 2; ++t) dt[t] = mfma(qf[3], kf[t][3], mfma(qf[2], kf[t][2], mfma(qf[1], kf[t][1], mfma(qf[0], kf[t][0], zero))));
    SB();
    wait_cnt<0>(F_L, 4);
#pragma unroll
    for (int t = 0; t < 2; ++t)
#pragma unroll
      for (int s = 0; s < 4; ++s) kf[t][s] = lds_ld<v4u>(ka[s] + (2 + t) * 16384);
    SB();
    if (lane == 0) lds_inc(F_KD);
#pragma unroll
    for (int t = 0; t < 2; ++t) dt[2 + t] = mfma(qf[3], kf[t][3], mfma(qf[2], kf[t][2], mfma(qf[1], kf[t][1], mfma(qf[0], kf[t][0], zero))));
    const bool r1 = r & 1, r2 = r & 2;
    float s0 = r2 ? (r1 ? dt[3][0] : dt[2][0]) : (r1 ? dt[1][0] : dt[0][0]);
    float s1 = r2 ? (r1 ? dt[3][1] : dt[2][1]) : (r1 ? dt[1][1] : dt[0][1]);
    s0 = rb(rb(s0) * 0.08838834764831845f); s1 = rb(rb(s1) * 0.08838834764831845f);
    {
      float m = m32(s0, s1);
      m = row_max(m16(m, m));
      lds_st<unsigned>(MX_OFF + (lane >> 5) * 16 + w * 4, min(asu(m), SENT - 1u));
    }
    v4u ea, eb;
#pragma nounroll
    for (unsigned s = 0; s < LSPIN_CAP; ++s) {
      ea = lds_vld<v4u>(MX_OFF); eb = lds_vld<v4u>(MX_OFF + 16);
      if (rfl(max(max(max(ea.x, ea.y), max(ea.z, ea.w)), max(max(eb.x, eb.y), max(eb.z, eb.w)))) != SENT) break;
    }
    const float M0 = fmaxf(fmaxf(asf(ea.x), asf(ea.y)), fmaxf(asf(ea.z), asf(ea.w))), M1 = fmaxf(fmaxf(asf(eb.x), asf(eb.y)), fmaxf(asf(eb.z), asf(eb.w)));
    const unsigned pp = pk(__builtin_amdgcn_exp2f((s0 - M0) * 1.4426950408889634f), __builtin_amdgcn_exp2f((s1 - M1) * 1.4426950408889634f));
    lds_st<u16>(P_OFF + (64 * r + 16 * w + c) * 2, (u16)pp); lds_st<u16>(P_OFF + 512 + (64 * r + 16 * w + c) * 2, (u16)(pp >> 16));
    if (lane == 0) lds_inc(F_P);
    wait_cnt<0>(F_P, 4);
    v4u pf[8];
#pragma unroll
    for (int s = 0; s < 8; ++s) pf[s] = lds_ld<v4u>(P_OFF + (c & 1) * 512 + r * 16 + s * 64);
    if (w == 0) {
      const v4u ones = {0x3f803f80u, 0x3f803f80u, 0x3f803f80u, 0x3f803f80u};
      v4f ls = zero;
#pragma unroll
      for (int s = 0; s < 8; ++s) ls = mfma(pf[s], ones, ls);
      STW(tag4(v4f{M0, ls[0], M1, ls[1]}, T0, T1), oob(lane < 8, ST_OFF + (g * 16 + j) * 128 + lane * 16));
    }
    v4f oa = zero, ob = zero;
#define PV(s0_, n_) { v4u fa[n_], fb[n_]; \
      _Pragma("unroll") for (int s = 0; s < n_; ++s) { fa[s] = tr8(va[0][0] + (s0_ + s) * 8192, va[0][1] + (s0_ + s) * 8192); fb[s] = tr8(va[1][0] + (s0_ + s) * 8192, va[1][1] + (s0_ + s) * 8192); } \
      SB(); _Pragma("unroll") for (int s = 0; s < n_; ++s) { oa = mfma(fa[s], pf[s0_ + s], oa); ob = mfma(fb[s], pf[s0_ + s], ob); } SB(); }
    wait_cnt<0>(F_L, 6); PV(0, 4)
    wait_cnt<0>(F_L, 7); PV(4, 2)
    wait_cnt<0>(F_L, 8); PV(6, 1)
#undef PV
    {
      v4u fa, fb;
#pragma nounroll
      for (unsigned sp = 0; sp < LSPIN_CAP; ++sp) {
        const unsigned f = lds_vld<unsigned>(F_L);
        CBAR();
        fa = tr8(va[0][0] + 7 * 8192, va[0][1] + 7 * 8192); fb = tr8(va[1][0] + 7 * 8192, va[1][1] + 7 * 8192);
        SB();
        if (rfl(f) >= 9u) break;
      }
      oa = mfma(fa, pf[7], oa); ob = mfma(fb, pf[7], ob);
    }
    STW(tag4((c & 2) ? ob : oa, T0, T1), opo);
  }

  if (is_wo) {
    if (wave >= 4 && wave <= 7) {
      const int w = wave - 4;
      unsigned oo = OP_OFF + (g * 16 + 4 * r) * 1024 + (64 * w + 4 * c) * 4, ol = O_OFF + (64 * w + 4 * c + r) * 2;
      PIN(oo); PIN(ol);
      GET_TAGS();
      wait_cnt<1>(F_W, 2);
      v4u t[4];
#pragma unroll
      for (int i = 0; i < 4; ++i) t[i] = LDW(oo, i * 1024);
      SB();
      wait_cnt<0>(F_GO, 1);
      v4f wg = lds_ld<v4f>(WG_OFF + (w >> 1) * 64 + r * 16);
      PIN(wg);
      SB();
      const v4f a = settle<1024, true>(t, wg, oo, ws, T0, T1);
      const float o = t16(t32(a[0], a[2]), t32(a[1], a[3]));
      lds_st<u16>(ol, (u16)pk(o, o));
      bar();
    } else if (wave >= 8 && wave <= 13) {
      const int t = wave - 8;
      unsigned ho = oob(c == 0, HP_OFF + (g * 576 + j * 96 + 16 * t + 4 * r) * 4), oa = O_OFF + r * 16, wa = W_OFF + (8 * r + q4) * 192 + t * 32 + p4 * 8;
      PIN(ho); PIN(oa); PIN(wa);
      wait_cnt<8>(F_W, 1);
      wait_cnt<2>(F_W, 2);
      GET_TAGS();
      v4u wf[8], of[8];
      constexpr int S1 = 8 - (WOEARLY + 5) / 6;
#pragma unroll
      for (int s = 0; s < S1; ++s) wf[s] = tr8(wa + s * 6144, wa + s * 6144 + 4 * 192);
      wait_cnt<0>(F_W, 3);
#pragma unroll
      for (int s = S1; s < 8; ++s) wf[s] = tr8(wa + s * 6144, wa + s * 6144 + 4 * 192);
#pragma unroll
      for (int s = 0; s < 8; ++s) PIN(wf[s]);
      bar();
#pragma unroll
      for (int s = 0; s < 8; ++s) of[s] = lds_ld<v4u>(oa + s * 64);
      SB();
      v4f acc = zero;
#pragma unroll
      for (int s = 0; s < 8; ++s) acc = mfma(wf[s], of[s], acc);
      STW(tag4(acc, T0, T1), ho);
    } else {
      if (wave == 1) {
        wait_cnt<2>(F_W, 1);
        GET_TAGS();
        __builtin_amdgcn_s_sleep(STWAIT);
        v4f st;
#pragma nounroll
        for (unsigned spin = 0; spin < SPIN_CAP; ++spin) {
          unsigned bad = 0;
          CBAR();
          st = untag4(LDW(ST_OFF + (g * 16 + c) * 128 + r * 16, 0), T0, T1, bad);
          if (ALL_OK(bad)) break;
          __builtin_amdgcn_s_sleep(4);
        }
        const float mp = (r & 1) ? st[2] : st[0], lp = (r & 1) ? st[3] : st[1];
        float wgt = __builtin_amdgcn_exp2f((mp - row_max(mp)) * 1.4426950408889634f);
        wgt *= __builtin_amdgcn_rcpf(row_sum(wgt * lp));
        lds_st<float>(WG_OFF + lane * 4, wgt);
        lds_vst(F_GO, 1u);
      } else if (wave == 2) {
        unsigned ho = oob(lane < (j == 5 ? 3 : 1), H1_OFF + g * 128 + (j + lane) * 16);
        PIN(ho);
        wait_cnt<8>(F_PUB, 4);
        GET_TAGS();
        wait_cnt<2>(F_L, 4);
        STW((v4u{T0, now32() + CKL, T1, 0u}), ho);
      }
      bar();
    }
  } else if (is_mlp) {
    if (wave >= 8 && wave <= 11) {
      unsigned za = Z_OFF + (c >> 3) * 128 + (8 * r + (c & 7)) * 4, w2a = W2_OFF + (8 * r + q4) * 1152 + 9 * (wave - 8) * 32 + p4 * 8;
      unsigned mo = oob(p4 < 2 || c == 2, MP_OFF + (9 * (wave - 8) + 4 * p4 + q4) * 9216 + u * 64 + r * 16);
      float b1v = lo16(((const GAS u16*)p.b1)[32 * u + 8 * r + (c & 7)]);
      v4u g0 = {0u, 0u, 0u, 0u}, g1 = g0, g2 = g0, g3 = g0, w2f[9];
      PIN(za); PIN(w2a); PIN(mo); PIN(b1v); PIN(g0); PIN(g1); PIN(g2); PIN(g3);
      wait_cnt<8>(F_W, 2);
      GET_TAGS();
      bool have = false;
#pragma nounroll
      for (unsigned sp = 0; sp < LSPIN_CAP; ++sp) {
        const v2u f = lds_vld<v2u>(F_W);
        have = rfl(f.x) >= 3u;
        if (have || rfl(f.y) >= 72u) break;
        __builtin_amdgcn_s_sleep(1);
      }
      CBAR();
      if (have) {
#pragma unroll
        for (int t = 0; t < 9; ++t) w2f[t] = tr8(w2a + t * 32, w2a + t * 32 + 4 * 1152);
      }
      wait_cnt<2>(F_Z, 1);
      wait_cnt<0>(F_Z, 72);
      const v4f sa = lds_ld<v4f>(S_OFF), sb = lds_ld<v4f>(S_OFF + 16), sc = lds_ld<v4f>(S_OFF + 32), sd = lds_ld<v4f>(S_OFF + 48);
      const v2f se = lds_ld<v2f>(S_OFF + 64);
      float zz[9];
#pragma unroll
      for (int i = 0; i < 9; ++i) zz[i] = lds_ld<float>(za + i * 256);
      SB();
      const v4f s4 = (sa + sb) + (sc + sd);
      const float mean = ((s4[1] + s4[3]) + se[1]) * (1.f / D), var = fmaxf(((s4[0] + s4[2]) + se[0]) * (1.f / D) - mean * mean, 0.f);
      const float rr = __builtin_amdgcn_rsqf(fmaf(var, 1.f + 1e-5f, 1e-10f));
      const v2f zp = (v2f{zz[0], zz[1]} + v2f{zz[2], zz[3]}) + (v2f{zz[4], zz[5]} + v2f{zz[6], zz[7]});
      const float t = (zp[0] + zz[8]) + zp[1];
      const float zb = rb(rb((t - mean * dpp<XOR8>(t)) * rr) + b1v);
      const float e = __builtin_amdgcn_exp2f(zb * fmaf(zb * zb, 2.302208198144325f * 0.044715f, 2.302208198144325f));
      const float gl = zb - zb * __builtin_amdgcn_rcpf(e + 1.f);
      const unsigned gp = pk(gl, dpp<XOR1>(gl));
      if (!have) {
        wait_cnt<0>(F_W, 3);
#pragma unroll
        for (int t2 = 0; t2 < 9; ++t2) w2f[t2] = tr8(w2a + t2 * 32, w2a + t2 * 32 + 4 * 1152);
      }
#define GB(G_, bk) G_[0] = __builtin_amdgcn_update_dpp(G_[0], gp, NEWB + 0, 0xf, 1 << (bk), false); G_[1] = __builtin_amdgcn_update_dpp(G_[1], gp, NEWB + 2, 0xf, 1 << (bk), false); \
                   G_[2] = __builtin_amdgcn_update_dpp(G_[2], gp, NEWB + 4, 0xf, 1 << (bk), false); G_[3] = __builtin_amdgcn_update_dpp(G_[3], gp, NEWB + 6, 0xf, 1 << (bk), false);
      GB(g0, 0) GB(g1, 1) GB(g2, 2) GB(g3, 3)
#undef GB
      const v4f o2 = mfma(w2f[8], g0, zero);
      const v4f o0 = mfma(w2f[3], g3, mfma(w2f[2], g2, mfma(w2f[1], g1, mfma(w2f[0], g0, zero))));
      const v4f o1 = mfma(w2f[7], g3, mfma(w2f[6], g2, mfma(w2f[5], g1, mfma(w2f[4], g0, zero))));
      v4f o;
#pragma unroll
      for (int k = 0; k < 4; ++k) { const float a0 = o0[k], a1 = o1[k], a2 = o2[k]; const float lo_ = (c & 2) ? a2 : a0; o[k] = (c & 1) ? a1 : lo_; }
      STW(tag4(o, T0, T1), mo);
    } else if (wave == 1) {
      unsigned h3o = oob(lane < 8, H3_OFF + u * 128 + lane * 16);
      PIN(h3o);
      wait_cnt<8>(F_L, 9);
      GET_TAGS();
      __builtin_amdgcn_s_sleep(HWAIT);
      const unsigned due = hp_due(ws, T0, lane);
      lds_vst(F_GO + 4, due); lds_vst(F_GO, 1u);
      const unsigned ta = now32(), go = (int)(due - ta) <= LATEADD ? ta + LATEADD : due;
      wait_cnt<1>(F_W, 2);
      const unsigned own = rfl(lds_vld<unsigned>(F_T)) + DELTAW, all = go + DELTA3;
      STW((v4u{T0, (int)(own - all) > 0 ? own : all, T1, 0u}), h3o);
    } else if (wave != 0) {
      const int ch = wave >= 12 ? (wave == 14 ? 8 : wave - 12) : wave;
      unsigned ho = HP_OFF + (r * 576 + 64 * ch + 4 * c) * 4, zw = Z_OFF + ch * 256 + q4 * 128 + r * 16, sw = S_OFF + r * 128 + ch * 8 + q4 * 4;
      unsigned v1 = (64 * ch + (lane >> 2)) * (FF * 2) + 64 * u + (lane & 3) * 16, w1a = W1_OFF + (64 * ch + 4 * q4 + r) * 64 + p4 * 8;
      v4u f0 = {0x3f803f80u, 0x3f803f80u, 0x3f803f80u, 0x3f803f80u}, f1 = f0, hv[4], w1f[2][2];
      PIN(ho); PIN(zw); PIN(sw); PIN(v1); PIN(w1a); PIN(f0); PIN(f1);
      wait_cnt<8>(F_L, 9);
      wait_cnt<2>(F_KD, 4);
      GET_TAGS();
      float xv = lds_ld<float>(X_OFF + (64 * ch + 4 * c + r) * 4);
      PIN(xv);
      auto take_w1 = [&]() {
        burst<4, 0>(v1, W1_OFF + ch * 4096, p.w1, 16u * FF * 2);
        VMW(0);
#pragma unroll
        for (int s = 0; s < 2; ++s)
#pragma unroll
          for (int t = 0; t < 2; ++t) w1f[s][t] = tr8(w1a + s * 2048 + t * 32, w1a + s * 2048 + t * 32 + 16 * 64);
        PIN(w1f[0][0]); PIN(w1f[0][1]); PIN(w1f[1][0]); PIN(w1f[1][1]);
      };
      bool early = true;
#pragma nounroll
      for (unsigned sp = 0; sp < LSPIN_CAP; ++sp) {
        const v4u f = lds_vld<v4u>(F_GO);
        early = rfl(f.z) != 0u;
        if (early || (rfl(f.x) != 0u && (int)(now32() - rfl(f.y)) >= -20)) break;
        __builtin_amdgcn_s_sleep(1);
      }
      CBAR();
      if (early) take_w1();
      wait_cnt<2>(F_GO, 1);
      {
        const unsigned t0 = now32(), dl = min(max((int)(rfl(lds_vld<unsigned>(F_GO + 4)) - t0), 0), 400);
        while (now32() - t0 + 12 < dl) __builtin_amdgcn_s_sleep(2);
        while (now32() - t0 < dl) {}
      }
      CBAR();
#pragma unroll
      for (int i = 0; i < 4; ++i) hv[i] = LDW(ho, i * 4 * 2304);
      SB();
      if (!early) { wait_cnt<0>(F_P1, 1); take_w1(); }
      const v4f a = settle<4 * 2304, false>(hv, zero, ho, ws, T0, T1);
      const float hs = xv + rb(t16(t32(a[0], a[2]), t32(a[1], a[3])));
      const unsigned pr = pk(hs, dpp<XOR1>(hs));
#define HB(F_, k, n) F_[k] = __builtin_amdgcn_update_dpp(F_[k], pr, NEWB + (n), 0xf, 0xd, false);
      HB(f0, 0, 0) HB(f0, 1, 2) HB(f0, 2, 4) HB(f0, 3, 6) HB(f1, 0, 8) HB(f1, 1, 10) HB(f1, 2, 12) HB(f1, 3, 14)
#undef HB
      const v4f z0 = mfma(w1f[1][0], f1, mfma(w1f[0][0], f0, zero)), z1 = mfma(w1f[1][1], f1, mfma(w1f[0][1], f0, zero)), sq = mfma(f1, f1, mfma(f0, f0, zero));
      if ((c & 11) == 0) { lds_st<v4f>(zw, z0); lds_st<v4f>(zw + 64, z1); lds_st<float>(sw, sq[0]); lds_inc(F_Z); }
      if (u == 0) {
        __builtin_amdgcn_raw_buffer_store_b32((pr << 16) | T0, ws, HR_OFF + (64 * ch + 4 * c + r) * 4, 0, AUX_SC1);
        if (ch == 0 && lane == 0) __builtin_amdgcn_raw_buffer_store_b32(T0 | (T1 << 16), ws, EP_OFF, 0, AUX_SC1);
      }
    }
  }
  if (red < 36 && wave == 1) {
    const int col = 16 * red + 4 * (lane & 3) + r;
    float b2v = lo16(((const GAS u16*)p.b2)[col]);
    unsigned mo = MP_OFF + red * 9216 + lane * 16, hro = HR_OFF + col * 4, oo = col * 2, hq[3], hu;
    PIN(b2v); PIN(mo); PIN(hro); PIN(oo);
#pragma unroll
    for (int i = 0; i < 3; ++i) hq[i] = oob(i * 64 + lane < 144, H3_OFF + (i * 64 + lane) * 128);
    wait_cnt<8>(F_L, 9);
    GET_TAGS();
    if (j == 15) __builtin_amdgcn_s_sleep(16);
    {
      const unsigned due = hp_due(ws, T0, lane) + RWAIT;
      while ((int)(now32() - due) < 0) __builtin_amdgcn_s_sleep(2);
    }
    const unsigned go = latest<3, 2>(hq, ws, T0, 0.f);
    while ((int)(now32() - go) < 0) {}
    v4f a;
#pragma nounroll
    for (unsigned spin = 0; spin < SPIN_CAP; ++spin) {
      CBAR();
      hu = __builtin_amdgcn_raw_buffer_load_b32(ws, hro, 0, AUX_SC1);
      SB();
      v4u mv[9];
#pragma unroll
      for (int i = 0; i < 9; ++i) { mv[i] = LDW(mo + (i & 3) * 1024, (i & ~3) * 1024); SB(); }
      hu ^= T0;
      unsigned bad = hu;
      a = untag4(mv[0], T0, T1, bad);
#pragma unroll
      for (int i = 1; i < 9; ++i) { a += untag4(mv[i], T0, T1, bad); PIN(a); SB(); }
      if (ALL_OK(bad)) break;
    }
    float t = t16(t32(a[0], a[2]), t32(a[1], a[3]));
    t += dpp<XOR8>(t); t += dpp<XOR7>(dpp<XOR3>(t));
    const float res = asf(hu) + rb(rb(t) + b2v);
    __builtin_amdgcn_raw_buffer_store_b16((u16)pk(res, res), __builtin_amdgcn_make_buffer_rsrc(p.out, 0, D * 2, 0x00020000), oo, 0, 0);
  }
}

extern "C" void kernel_launch(void* const* d_in, const int* in_sizes, int n_in, void* d_out, int out_size, void* d_ws, size_t ws_size, hipStream_t stream) {
  Params p;
  p.x = (const u16*)d_in[0]; p.k = (const u16*)d_in[1]; p.v = (const u16*)d_in[2]; p.wq = (const u16*)d_in[3]; p.wo = (const u16*)d_in[4];
  p.w1 = (const u16*)d_in[6]; p.b1 = (const u16*)d_in[7]; p.w2 = (const u16*)d_in[8]; p.b2 = (const u16*)d_in[9];
  p.out = (u16*)d_out; p.ws = (char*)d_ws;
  hipLaunchKernelGGL(fused, dim3(NBLK), dim3(NTHR), 0, stream, p);
}
```
